```python
import torch

def _softmax(x, axis=-1):
    return torch.softmax(x, dim=axis)
def _gelu(x):
    return torch.nn.functional.gelu(x, approximate="tanh")
def _silu(x):
    return torch.nn.functional.silu(x)
def _sigmoid(x):
    return torch.sigmoid(x)
def _layernorm(x, axis=-1, eps=1e-5):
    m = x.mean(dim=axis, keepdim=True)
    v = x.var(dim=axis, keepdim=True, unbiased=False)
    return (x - m) / torch.sqrt(v + eps)
def _scatter_add(tgt, idx, src, axis=0):
    return tgt.clone().index_add_(axis, idx, src)
def _segment_sum(src, seg, n):
    out = torch.zeros((n,) + src.shape[1:], dtype=src.dtype)
    return out.index_add_(0, seg, src)
def _pad(x, pw):
    flat = tuple(v for pair in reversed(pw) for v in pair)
    return torch.nn.functional.pad(x, flat)

kernel_name = 'parallel_sum__ssm_conv_h3__geglu_ffn__prompt'

def setup_inputs():

    _w = (lambda a: (lambda t: t.to(torch.bfloat16) if t.is_floating_point() else t)(torch.as_tensor(a)))
    def _A_setup():
        B, S, D = 1, 2048, 512
        ks = 4
        return {
            "q": _w((lambda *s: torch.randn(*s, dtype=torch.bfloat16) * 0.05)(B, S, D)),
            "k": _w((lambda *s: torch.randn(*s, dtype=torch.bfloat16) * 0.8)(B, S, D)),
            "v": _w((lambda *s: torch.randn(*s, dtype=torch.bfloat16) * 0.8)(B, S, D)),
            "shift_k": _w((lambda *s: torch.randn(*s, dtype=torch.bfloat16) * 0.8)(D, ks) * 0.5),
            "ssm_k": _w((lambda *s: torch.randn(*s, dtype=torch.bfloat16) * 0.8)(D, S) * (1.0 / S ** 0.5)),
            "ssm_D": _w((lambda *s: torch.randn(*s, dtype=torch.bfloat16) * 0.8)(D)),
        }
    _A_in = _A_setup()

    def _B_setup():
        B, S, D, F = 1, 2048, 512, 1376
        return {
            "x": _w((lambda *s: torch.randn(*s, dtype=torch.bfloat16) * 0.05)(B, S, D)),
            "w_gate": _w((lambda *s: torch.randn(*s, dtype=torch.bfloat16) * 0.05)(D, F)),
            "w_up": _w((lambda *s: torch.randn(*s, dtype=torch.bfloat16) * 0.05)(D, F)),
            "w_down": _w((lambda *s: torch.randn(*s, dtype=torch.bfloat16) * 0.05)(F, D)),
        }
    _B_in = _B_setup()

    return {
        "A_q": _A_in["q"],
        "A_k": _A_in["k"],
        "A_v": _A_in["v"],
        "A_shift_k": _A_in["shift_k"],
        "A_ssm_k": _A_in["ssm_k"],
        "A_ssm_D": _A_in["ssm_D"],
        "B_x": _B_in["x"],
        "B_w_gate": _B_in["w_gate"],
        "B_w_up": _B_in["w_up"],
        "B_w_down": _B_in["w_down"],
    }

def reference(A_q, A_k, A_v, A_shift_k, A_ssm_k, A_ssm_D, B_x, B_w_gate, B_w_up, B_w_down):
    def _A_ref(q, k, v, shift_k, ssm_k, ssm_D):
        B, S, D = q.shape
        ks = shift_k.shape[-1]
        k_pad = _pad(k, ((0, 0), (ks - 1, 0), (0, 0)))
        k_shift = torch.zeros_like(k)
        for t in range(ks):
            k_shift = k_shift + k_pad[:, t:t + S, :] * shift_k[:, ks - 1 - t][None, None, :]
        kv = k_shift * v
        L = 2 * S
        kv_pad = _pad(kv, ((0, 0), (0, S), (0, 0)))
        kf_pad = _pad(ssm_k, ((0, 0), (0, S)))
        kv_f = (lambda x, axes=(-2,-1): torch.fft.rfft2(x.float(), dim=axes))(kv_pad[:, None, :, :], axes=(1, 2))
        kf_f = (lambda x, axes=(-2,-1): torch.fft.rfft2(x.float(), dim=axes))(kf_pad[:, None, :], axes=(1, 2))
        kf_f = (lambda x, *d: x.permute(*d))(kf_f, 1, 2, 0)
        y_f = kv_f * kf_f[None, :, :, :]
        y = (lambda x, s, axes=(-2,-1): torch.fft.irfft2(x, s=s, dim=axes).to(torch.bfloat16))(y_f, (1, L), axes=(1, 2))[:, 0, :S, :]
        y = y + kv * ssm_D[None, None, :]
        return q * y

    def _B_ref(x, w_gate, w_up, w_down):
        return (_gelu(x @ w_gate) * (x @ w_up)) @ w_down

    x = A_q
    xn = _layernorm(x, axis=-1)
    return x + _A_ref(xn, A_k, A_v, A_shift_k, A_ssm_k, A_ssm_D) + _B_ref(xn, B_w_gate, B_w_up, B_w_down)
```

```cpp
#include <hip/hip_runtime.h>
#include <hip/hip_bf16.h>
#include <stdint.h>

typedef unsigned short u16;
constexpr int S = 2048, D = 512, F = 1376;

__device__ __forceinline__ float bf2f(u16 x) { return __uint_as_float(((unsigned)x) << 16); }
__device__ __forceinline__ u16 f2bf(float f) { __hip_bfloat16 b = __float2bfloat16(f); return *(u16*)&b; }
__device__ __forceinline__ float rbf(float f) { return bf2f(f2bf(f)); }

__global__ void k_pre(const u16* q, const u16* k, const u16* v, const u16* sk, u16* xn, u16* kv) {
  int s = blockIdx.x, t = threadIdx.x;
  __shared__ float red[16];
  float x = bf2f(q[s * D + t]);
  float sum = x;
  for (int o = 32; o; o >>= 1) sum += __shfl_xor(sum, o);
  if ((t & 63) == 0) red[t >> 6] = sum;
  __syncthreads();
  float tot = 0; for (int i = 0; i < 8; ++i) tot += red[i];
  float mean = tot / D;
  __syncthreads();
  float dd = (x - mean) * (x - mean);
  for (int o = 32; o; o >>= 1) dd += __shfl_xor(dd, o);
  if ((t & 63) == 0) red[t >> 6] = dd;
  __syncthreads();
  tot = 0; for (int i = 0; i < 8; ++i) tot += red[i];
  float var = rbf(tot / D);
  float m = rbf(mean);
  float sd = rbf(sqrtf(rbf(var + 1e-5f)));
  xn[s * D + t] = f2bf(rbf(x - m) / sd);
  float ks = 0.f;
  for (int tt = 0; tt < 4; ++tt) {
    int r = s + tt - 3;
    float kk = r >= 0 ? bf2f(k[r * D + t]) : 0.f;
    ks = rbf(ks + rbf(kk * bf2f(sk[t * 4 + 3 - tt])));
  }
  kv[s * D + t] = f2bf(ks * bf2f(v[s * D + t]));
}

__global__ void k_conv(const u16* kv, const u16* h, const u16* Dv, u16* z) {
  int s = blockIdx.x, d = threadIdx.x;
  float acc = 0.f;
  for (int u = 0; u <= s; ++u) acc += bf2f(kv[u * D + d]) * bf2f(h[d * S + (s - u)]);
  float y = rbf(acc);
  z[s * D + d] = f2bf(y + rbf(bf2f(kv[s * D + d]) * bf2f(Dv[d])));
}

__global__ void k_g1(const u16* xn, const u16* wg, const u16* wu, u16* H) {
  int s = blockIdx.x;
  __shared__ float xr[D];
  for (int i = threadIdx.x; i < D; i += blockDim.x) xr[i] = bf2f(xn[s * D + i]);
  __syncthreads();
  for (int f = threadIdx.x; f < F; f += blockDim.x) {
    float g = 0, u = 0;
    for (int d = 0; d < D; ++d) { g += xr[d] * bf2f(wg[d * F + f]); u += xr[d] * bf2f(wu[d * F + f]); }
    g = rbf(g); u = rbf(u);
    float inner = 0.7978845608028654f * (g + 0.044715f * g * g * g);
    float ge = rbf(0.5f * g * (1.f + tanhf(inner)));
    H[s * F + f] = f2bf(ge * u);
  }
}

__global__ void k_g2(const u16* H, const u16* wd, const u16* q, const u16* xn, const u16* z, u16* out) {
  int s = blockIdx.x, d = threadIdx.x;
  __shared__ float hr[F];
  for (int i = threadIdx.x; i < F; i += blockDim.x) hr[i] = bf2f(H[s * F + i]);
  __syncthreads();
  float acc = 0;
  for (int f = 0; f < F; ++f) acc += hr[f] * bf2f(wd[f * D + d]);
  float a = rbf(bf2f(xn[s * D + d]) * bf2f(z[s * D + d]));
  float r = rbf(bf2f(q[s * D + d]) + a);
  out[s * D + d] = f2bf(r + rbf(acc));
}

extern "C" void kernel_launch(void* const* d_in, const int* in_sizes, int n_in,
                              void* d_out, int out_size, void* d_ws, size_t ws_size,
                              hipStream_t stream) {
  const u16* q = (const u16*)d_in[0]; const u16* k = (const u16*)d_in[1]; const u16* v = (const u16*)d_in[2];
  const u16* sk = (const u16*)d_in[3]; const u16* h = (const u16*)d_in[4]; const u16* Dv = (const u16*)d_in[5];
  const u16* wg = (const u16*)d_in[7]; const u16* wu = (const u16*)d_in[8]; const u16* wd = (const u16*)d_in[9];
  u16* ws = (u16*)d_ws;
  u16* xn = ws; u16* kv = ws + S * D; u16* z = ws + 2 * S * D; u16* H = ws + 3 * S * D;
  k_pre<<<S, D, 0, stream>>>(q, k, v, sk, xn, kv);
  k_conv<<<S, D, 0, stream>>>(kv, h, Dv, z);
  k_g1<<<S, 256, 0, stream>>>(xn, wg, wu, H);
  k_g2<<<S, D, 0, stream>>>(H, wd, q, xn, z, (u16*)d_out);
}
```

```cpp
#include <hip/hip_runtime.h>
#include <stdint.h>
#include <utility>

typedef unsigned short u16;
typedef unsigned int u32;
typedef unsigned long long u64;
typedef __bf16 bf16x8 __attribute__((ext_vector_type(8)));
typedef __bf16 bf16x2 __attribute__((ext_vector_type(2)));
typedef short s16x4 __attribute__((ext_vector_type(4)));
typedef short s16x8 __attribute__((ext_vector_type(8)));
typedef float f32x16 __attribute__((ext_vector_type(16)));
typedef float f32x4 __attribute__((ext_vector_type(4)));
typedef float f32x2 __attribute__((ext_vector_type(2)));
typedef u32 u32x4 __attribute__((ext_vector_type(4)));
typedef u32 u32x2 __attribute__((ext_vector_type(2)));
typedef __amdgpu_buffer_rsrc_t rsrc_t;
#define DI __device__ __forceinline__
#define AI __attribute__((always_inline))
#define LAS __attribute__((address_space(3)))
#define GAS __attribute__((address_space(1)))
#define RLX_AGENT __ATOMIC_RELAXED, __HIP_MEMORY_SCOPE_AGENT
#define RLX_WG __ATOMIC_RELAXED, __HIP_MEMORY_SCOPE_WORKGROUP

constexpr int S = 2048, D = 512, F = 1376;
constexpr u32 SLOT = 40992, G_OFF = 16384, U_OFF = 16384 + 12288 + 32;
constexpr u32 CONV = 3 * SLOT;
constexpr u32 CH_SZ = 17520, HA = 64, HB = 4292, KA = 8400, KB = 12976, XB = 4304;
constexpr u32 MISC = CONV + 2 * CH_SZ;
constexpr u32 CS = MISC, ST = MISC + 1536, CNT = MISC + 2560;
constexpr u32 CKV = CNT + 8, EPO = CNT + 16, CVP = CNT + 24, FULL = CNT + 32, FREE = CNT + 36, STRDY = CNT + 40, WDRDY = CNT + 44;
constexpr u32 LDS_BYTES = MISC + 2816;
constexpr u32 ZST = 114688, PART = 32768;
constexpr u32 W_E = 0, W_PF = 4096, W_HF = W_PF + 16384, W_ZF = W_HF + 16384, W_SG = W_ZF + 16384, W_CG = W_SG + 16384;
constexpr u32 W_KV = 131072, W_ZT = W_KV + 2097152 + 4096, W_H = W_ZT + 2097152 + 4096, W_END = W_H + 64 * 86 * 1024;
constexpr u32 W_DBG = 16u << 20;
constexpr u32 CODE_BYTES = 22528;
#define PRL 1
#define PRS 0
#define CONVAFTER 9
#define LSLEEP 1
#define PRA 0
#define ROT 4
#define KVSTAG 0
#define SSTOP 9
#define POLLS 4
#define PD 10
#define WD1 6
#define WD2 7
#define KVAFTER 1
constexpr u32 SPIN_CAP = 1u << 18;

constexpr int cpair(int g) {
  int i = 0;
  for (int n = 0; n < 8; ++n) for (int m = 0; m <= n; ++m) if (m + n <= 7) {
    if (i++ == g) return n * 8 + m;
    if (m < n && i++ == g) return m * 8 + n;
  }
  return 0;
}
template <class Fn, int... I> DI void sfor_impl(Fn&& f, std::integer_sequence<int, I...>) { (f(std::integral_constant<int, I>{}), ...); }
template <int N, class Fn> DI void sfor(Fn&& f) { sfor_impl(f, std::make_integer_sequence<int, N>{}); }

DI rsrc_t mk_rsrc(const void* p, u32 bytes) { return __builtin_amdgcn_make_buffer_rsrc((void*)p, (short)0, (int)bytes, 0x00020000); }
DI void dma(u32 lds, u32 voff, rsrc_t r, u32 soff) {
  asm volatile("s_mov_b32 m0, %0\n\ts_nop 0\n\tbuffer_load_dwordx4 %1, %2, %3 offen lds" ::"s"(lds), "v"(voff), "s"(r), "s"(soff) : "memory");
}

DI void dma4(u32 lds, u32 v0, u32 v1, u32 v2, u32 v3, rsrc_t r, u32 soff) {
  asm volatile("s_mov_b32 m0, %0\n\ts_nop 0\n\tbuffer_load_dwordx4 %1, %5, %6 offen lds\n\tbuffer_load_dwordx4 %2, %5, %6 offen offset:1024 lds\n\t"
               "buffer_load_dwordx4 %3, %5, %6 offen offset:2048 lds\n\tbuffer_load_dwordx4 %4, %5, %6 offen offset:3072 lds"
               ::"s"(lds), "v"(v0), "v"(v1), "v"(v2), "v"(v3), "s"(r), "s"(soff) : "memory");
}
DI void dma3(u32 lds, u32 v0, u32 v1, u32 v2, rsrc_t r, u32 soff) {
  asm volatile("s_mov_b32 m0, %0\n\ts_nop 0\n\tbuffer_load_dwordx4 %1, %4, %5 offen lds\n\tbuffer_load_dwordx4 %2, %4, %5 offen offset:1024 lds\n\t"
               "buffer_load_dwordx4 %3, %4, %5 offen offset:2048 lds"
               ::"s"(lds), "v"(v0), "v"(v1), "v"(v2), "s"(r), "s"(soff) : "memory");
}
template <int OFF> DI void bld4_sc1(u32x4& d, u32 voff, rsrc_t r, u32 soff) {
  asm volatile("buffer_load_dwordx4 %0, %1, %2, %3 offen offset:%4 sc1" : "=v"(d) : "v"(voff), "s"(r), "s"(soff), "i"(OFF));
}
DI void bld4(u32x4& d, u32 voff, rsrc_t r, u32 soff) { asm volatile("buffer_load_dwordx4 %0, %1, %2, %3 offen" : "=v"(d) : "v"(voff), "s"(r), "s"(soff)); }
template <int OFF> DI void bld2(u32x2& d, u32 voff, rsrc_t r) { asm volatile("buffer_load_dwordx2 %0, %1, %2, 0 offen offset:%3" : "=v"(d) : "v"(voff), "s"(r), "i"(OFF)); }
template <int OFF> DI void gld4_sc1(u32x4& d, u32 voff, const char* base) {
  asm volatile("global_load_dwordx4 %0, %1, %2 offset:%3 sc1" : "=v"(d) : "v"(voff), "s"(base), "i"(OFF));
}
template <int N> DI void waitv_pin(u32x4& d) { asm volatile("s_waitcnt vmcnt(%1)" : "+v"(d) : "i"(N)); }
template <int N> DI void waitv() { asm volatile("s_waitcnt vmcnt(%0)" ::"i"(N) : "memory"); }
DI void bar() { asm volatile("s_waitcnt lgkmcnt(0)\n\ts_barrier" ::: "memory"); }
DI void waitl() { asm volatile("s_waitcnt lgkmcnt(0)" ::: "memory"); }
DI float bflo(u32 x) { return __uint_as_float(x << 16); }
DI float bfhi(u32 x) { return __uint_as_float(x & 0xffff0000u); }
DI u32 pk2(float lo, float hi) { f32x2 v = {lo, hi}; return __builtin_bit_cast(u32, __builtin_convertvector(v, bf16x2)); }
DI u32 pk2s(float lo, float hi) { asm("" : "+v"(lo)); asm("" : "+v"(hi)); return pk2(lo, hi); }
DI float rbf(float x) { return bflo(pk2(x, x)); }
template <class T> DI T ldsr(u32 a) { return *(const LAS T*)a; }
template <class T> DI void ldsw(u32 a, T v) { *(LAS T*)a = v; }
DI u32x2 ldtr(u32 a) { return __builtin_bit_cast(u32x2, __builtin_amdgcn_ds_read_tr16_b64_v4i16((LAS s16x4*)a)); }
DI bf16x8 cat(u32x2 a, u32x2 b) { u32x4 v = {a.x, a.y, b.x, b.y}; return __builtin_bit_cast(bf16x8, v); }
template <int CTRL, int RM> DI float dpp_add(float v) {
  return v + __builtin_bit_cast(float, __builtin_amdgcn_update_dpp(0, __builtin_bit_cast(int, v), CTRL, RM, 0xf, false));
}
DI float wave_sum(float v) {
  v = dpp_add<0x111, 0xf>(v); v = dpp_add<0x112, 0xf>(v); v = dpp_add<0x114, 0xf>(v); v = dpp_add<0x118, 0xf>(v);
  v = dpp_add<0x142, 0xa>(v); v = dpp_add<0x143, 0xc>(v);
  return __builtin_bit_cast(float, __builtin_amdgcn_readlane(__builtin_bit_cast(int, v), 63));
}
DI void wave_sum2(float& a, float& c) {
  a = dpp_add<0x111, 0xf>(a); c = dpp_add<0x111, 0xf>(c); a = dpp_add<0x112, 0xf>(a); c = dpp_add<0x112, 0xf>(c);
  a = dpp_add<0x114, 0xf>(a); c = dpp_add<0x114, 0xf>(c); a = dpp_add<0x118, 0xf>(a); c = dpp_add<0x118, 0xf>(c);
  a = dpp_add<0x142, 0xa>(a); c = dpp_add<0x142, 0xa>(c); a = dpp_add<0x143, 0xc>(a); c = dpp_add<0x143, 0xc>(c);
  a = __builtin_bit_cast(float, __builtin_amdgcn_readlane(__builtin_bit_cast(int, a), 63));
  c = __builtin_bit_cast(float, __builtin_amdgcn_readlane(__builtin_bit_cast(int, c), 63));
}
DI void gld8_sc1(u64& d, const char* p) { asm volatile("global_load_dwordx2 %0, %1, off sc1" : "=v"(d) : "v"(p)); }
DI void gld_u16(u32& d, u32 voff, const char* base) { asm volatile("global_load_ushort %0, %1, %2" : "=v"(d) : "v"(voff), "s"(base)); }
template <int SLEEP> DI void poll_flags(const char* ws, u32 off, int n, u64 epoch, int lane) {
  const GAS u64* p = (const GAS u64*)(ws + off + (u32)(lane < n ? lane : 0) * 64u);
#pragma unroll 1
  for (u32 it = 0; it < SPIN_CAP; ++it) {
    u64 v = __hip_atomic_load(p, RLX_AGENT);
    if (__all(v == epoch)) break;
    __builtin_amdgcn_s_sleep(SLEEP);
  }
  asm volatile("" ::: "memory");
}
DI u32 poll_granule(const char* ws, u32 row, u32 tag) {
  const GAS u64* p = (const GAS u64*)(ws + W_SG + row * 8u);
  u64 v = 0;
#pragma unroll 1
  for (u32 it = 0; it < SPIN_CAP; ++it) {
    v = __hip_atomic_load(p, RLX_AGENT);
    if (__all((u32)(v >> 32) == tag)) break;
    __builtin_amdgcn_s_sleep(1);
  }
  return (u32)v;
}
DI u32 lds_arrive(u32 a, int lane) {
  u32 r = 0;
  waitl();
  if (lane == 0) r = __hip_atomic_fetch_add((LAS u32*)a, 1u, RLX_WG);
  return (u32)__builtin_amdgcn_readfirstlane(r) + 1u;
}
DI void lds_wait(u32 a, u32 want) {
#pragma unroll 1
  for (u32 it = 0; it < SPIN_CAP * 16; ++it) {
    u32 v = __hip_atomic_load((LAS u32*)a, RLX_WG);
    if ((u32)__builtin_amdgcn_readfirstlane(v) >= want) break;
  }
  asm volatile("" ::: "memory");
}

DI int lane_id() { int l; asm volatile("v_mbcnt_lo_u32_b32 %0, -1, 0\n\tv_mbcnt_hi_u32_b32 %0, -1, %0" : "=v"(l)); return l; }
DI u32 rfl(u32 x) { return (u32)__builtin_amdgcn_readfirstlane(x); }
DI u32x2 lds_ld2(u32 a) { return __builtin_bit_cast(u32x2, __hip_atomic_load((LAS u64*)a, RLX_WG)); }
template <int SLEEP> DI void wait_min2(u32 a, u32 want) {
#pragma unroll 1
  for (u32 it = 0; it < SPIN_CAP * 16; ++it) {
    const u32x2 v = lds_ld2(a);
    if (rfl(min(v.x, v.y)) >= want) break;
    if (SLEEP) __builtin_amdgcn_s_sleep(SLEEP);
  }
  asm volatile("" ::: "memory");
}
DI u32 lds_ld1(u32 a) { return __hip_atomic_load((LAS u32*)a, RLX_WG); }
DI bool all_ge(u32 w, u32 want) { return ((w - want * 0x01010101u) & 0x80808080u) == 0u; }
template <int SLEEP> DI void wait_min4(u32 a, u32 want) {
#pragma unroll 1
  for (u32 it = 0; it < SPIN_CAP * 16; ++it) {
    if (all_ge(rfl(lds_ld1(a)), want)) break;
    if (SLEEP) __builtin_amdgcn_s_sleep(SLEEP);
  }
  asm volatile("" ::: "memory");
}
template <int SLEEP> DI void wait_byte(u32 a, u32 want) {
#pragma unroll 1
  for (u32 it = 0; it < SPIN_CAP * 16; ++it) {
    if (rfl(__hip_atomic_load((LAS unsigned char*)a, RLX_WG)) >= want) break;
    if (SLEEP) __builtin_amdgcn_s_sleep(SLEEP);
  }
  asm volatile("" ::: "memory");
}
DI u64 wait_epoch() {
  u32x2 e;
#pragma unroll 1
  for (u32 it = 0; it < SPIN_CAP * 16; ++it) {
    e = lds_ld2(EPO);
    if (rfl(e.x) != 0u) break;
    __builtin_amdgcn_s_sleep(2);
  }
  return ((u64)rfl(e.y) << 32) | rfl(e.x);
}
DI u64 gld_agent(const char* p) { return __hip_atomic_load((const GAS u64*)p, RLX_AGENT); }
DI void lds_set(u32 a, u32 v) { __hip_atomic_store((LAS unsigned char*)a, (unsigned char)v, RLX_WG); }
DI void gather(u32& junk, u32 voff, rsrc_t r, u32 soff) { asm volatile("buffer_load_dword %0, %1, %2, %3 offen" : "=v"(junk) : "v"(voff), "s"(r), "s"(soff)); }
template <int N> using IC = std::integral_constant<int, N>;

#define DONE() do {} while (0)
#define STAMPI(id) do {} while (0)
#define STAMP(id) do {} while (0)

struct Args { const u16 *q, *wg, *wu, *k, *v, *sk, *h, *dv, *wd; u16* out; char* ws; u64 pad; };
typedef u32 u32x16 __attribute__((ext_vector_type(16)));
typedef u32 u32x8 __attribute__((ext_vector_type(8)));
DI char* mkp(u32 lo, u32 hi) { return (char*)(((u64)hi << 32) | lo); }
#define GAP() __builtin_amdgcn_sched_barrier(0)
DI void barx() { asm volatile("s_barrier" ::: "memory"); }
template <class T> DI void pin(T& x) { asm volatile("" : "+v"(x)); }

__global__ void __launch_bounds__(768, 3) fused(Args) {
  u64 pc0; asm volatile("s_getpc_b64 %0" : "=s"(pc0));
  u32x16 ka; u32x8 kb;
  asm volatile("s_load_dwordx16 %0, %2, 0x0\n\ts_load_dwordx8 %1, %2, 0x40" : "=&s"(ka), "=&s"(kb) : "s"(__builtin_amdgcn_kernarg_segment_ptr()));
  GAP();
  const int tid = threadIdx.x, lane = tid & 63;
  const int wave = __builtin_amdgcn_readfirstlane(tid >> 6);
  const int b = blockIdx.x, xcd = b & 7, li = b >> 3;
  if (tid < 12) ldsw<u32>(CNT + tid * 4, 0u);
  const bool has = li < 30;
  const int id = 30 * xcd + (has ? li : 0), rr_ = id % 120, rb = 8 * (id / 120) + (rr_ & 7);
  const int cg = has ? rr_ >> 3 : 4 * (xcd & 3) + 2 * (xcd >> 2) + (li - 30);
  const bool csw = !has && cg < 15;
  const int rt = 4 * xcd + (li & 3), ct = li >> 2;
  const int j = wave & 3, l8 = lane >> 3, c8 = lane & 7;
  u32 xv[4], wv[3];
  sfor<4>([&](auto t) AI { xv[t] = (7 * t + l8) * 1024 + ((c8 ^ ((4 * t + (l8 >> 1)) & 7)) << 4); pin(xv[t]); });
  sfor<3>([&](auto t) AI {
    const u32 byte = t * 1024 + lane * 16, row = byte / 192u;
    u32 seg = byte - row * 192u;
    if (cg == 14) seg &= 48;
    wv[t] = (16 * j + row) * (F * 2) + cg * 192 + seg - t * 1024;
    pin(wv[t]);
  });
  asm volatile("s_waitcnt lgkmcnt(0)\n\ts_barrier" : "+s"(ka), "+s"(kb)::"memory");
  STAMP(9);
  char* const ws = mkp(kb[4], kb[5]);
  const rsrc_t rq = mk_rsrc(mkp(ka[0], ka[1]), S * D * 2);
  if (wave >= 8) {
    __builtin_amdgcn_s_setprio(3);
    u64 epoch;
    asm volatile("s_load_dwordx2 %0, %1, 0x0" : "=s"(epoch) : "s"(ws));
    const int lane = lane_id(), l8 = lane >> 3, c8 = lane & 7;
    const int ch = j & 1, cp = j >> 1, c16 = lane & 15, q = lane >> 4;
    const rsrc_t rws = mk_rsrc(ws, W_END);
    const u32 cb = CONV + ch * CH_SZ, hown = cb + (cp ? XB : HA), kown = cb + (cp ? KB : KA);
    const int srow = 256 * xcd + 8 * li + 2 * j;
    if (SSTOP == 0) wait_min4<8>(FREE, 8);
    u32x4 sx[2];
    sfor<2>([&](auto r) AI { bld4(sx[r], lane * 16, rq, (srow + r) * 1024); });
    STAMP(23);
    const int pa = b & 31, pg = b >> 5, s0 = 64 * pa + 16 * j;
    u32x2 skw[2];
    u32 dvv;
    auto kvload = [&]() AI {
      const rsrc_t rk = mk_rsrc(mkp(ka[6], ka[7]), S * D * 2), rv = mk_rsrc(mkp(ka[8], ka[9]), S * D * 2), rs_ = mk_rsrc(mkp(ka[10], ka[11]), D * 8);
      const u32 kvv = l8 * 1024 + c8 * 16 + pg * 128;
      sfor<3>([&](auto t) AI { const int r = s0 - 8 + 8 * t; dma(hown + t * 1024, kvv, rk, (r < 0 ? 0 : r) * 1024); });
      sfor<2>([&](auto t) AI { dma(kown + t * 1024, kvv, rv, (s0 + 8 * t) * 1024); });
      sfor<2>([&](auto t) AI { bld2<256 * t>(skw[t], (64 * pg + 16 * (q >> 1) + c16) * 8, rs_); });
      gld_u16(dvv, (2 * b + ch) * 2, mkp(ka[14], ka[15]));
    };
    asm volatile("s_waitcnt lgkmcnt(0)" : "+s"(epoch));
    epoch += 0x9E3779B97F4A7C15ull;
    epoch = (epoch ^ (epoch >> 30)) * 0xBF58476D1CE4E5B9ull; epoch = (epoch ^ (epoch >> 27)) * 0x94D049BB133111EBull;
    epoch = (epoch ^ (epoch >> 31)) | 1ull;
    ldsw<u64>(EPO, epoch);
    STAMP(18);
    const u32 tag = (u32)(epoch >> 17);
    if (has) wait_min4<2>(FULL, KVAFTER + KVSTAG * (li & 3));
    STAMP(19);
    kvload();
    STAMP(20);
    asm volatile("s_waitcnt vmcnt(8)" : "+v"(sx[0]), "+v"(sx[1]));
    STAMP(21);
    __builtin_amdgcn_s_setprio(0);
    {
      float x[2][8], mean[2], var[2];
      sfor<2>([&](auto r) AI {
        sfor<4>([&](auto i) AI { x[r][2 * i] = bflo(sx[r][(int)i]); x[r][2 * i + 1] = bfhi(sx[r][(int)i]); });
        mean[r] = x[r][0];
        sfor<7>([&](auto i) AI { mean[r] += x[r][i + 1]; });
      });
      wave_sum2(mean[0], mean[1]);
      sfor<2>([&](auto r) AI {
        mean[r] *= 1.0f / D; var[r] = 0.f;
        sfor<8>([&](auto i) AI { float d = x[r][i] - mean[r]; asm("" : "+v"(d)); var[r] += d * d; });
      });
      wave_sum2(var[0], var[1]);
      STAMP(22);
      const float mn = lane ? mean[1] : mean[0], vr = rbf((lane ? var[1] : var[0]) * (1.0f / D));
      const u32 g = pk2s(mn, __builtin_sqrtf(rbf(vr + 1e-5f)));
      if (lane < 2) __hip_atomic_store((GAS u64*)(ws + W_SG + (srow + lane) * 8), ((u64)tag << 32) | g, RLX_AGENT);
    }
    STAMP(1);
    asm volatile("s_waitcnt vmcnt(1)" : "+v"(skw[0]), "+v"(skw[1]), "+v"(dvv)::"memory");
    STAMP(2);
    {
      const bool first = s0 == 0 && (q & 1) == 0;
      u32x2 kr[2][5];
      sfor<2>([&](auto gi) AI {
        const u32 a = (8 * (q & 1) + (c16 >> 2)) * 128 + 64 * gi + 32 * (q >> 1) + 8 * (lane & 3);
        kr[gi][0] = ldtr(hown + a + 512); kr[gi][1] = ldtr(hown + a + 1024); kr[gi][2] = ldtr(hown + a + 1536); kr[gi][3] = ldtr(kown + a); kr[gi][4] = ldtr(kown + a + 512);
      });
      GAP();
      if (SSTOP == 1) wait_min4<8>(FREE, 8);
      sfor<2>([&](auto gi) AI {
        const float w0 = bflo(skw[gi].x), w1 = bfhi(skw[gi].x), w2 = bflo(skw[gi].y), w3 = bfhi(skw[gi].y);
        const u32x2 k0 = kr[gi][0], k1 = kr[gi][1], k2 = kr[gi][2], v0 = kr[gi][3], v1 = kr[gi][4];
        const float kk[11] = {first ? 0.f : bfhi(k0.x), first ? 0.f : bflo(k0.y), first ? 0.f : bfhi(k0.y), bflo(k1.x), bfhi(k1.x), bflo(k1.y), bfhi(k1.y), bflo(k2.x), bfhi(k2.x), bflo(k2.y), bfhi(k2.y)};
        const float vv[8] = {bflo(v0.x), bfhi(v0.x), bflo(v0.y), bfhi(v0.y), bflo(v1.x), bfhi(v1.x), bflo(v1.y), bfhi(v1.y)};
        u32 a4[4], m4[4];
        sfor<4>([&](auto t) AI { a4[t] = pk2s(w3 * kk[2 * t], w3 * kk[2 * t + 1]); m4[t] = pk2s(w2 * kk[2 * t + 1], w2 * kk[2 * t + 2]); });
        GAP();
        sfor<4>([&](auto t) AI { a4[t] = pk2s(bflo(a4[t]) + bflo(m4[t]), bfhi(a4[t]) + bfhi(m4[t])); m4[t] = pk2s(w1 * kk[2 * t + 2], w1 * kk[2 * t + 3]); });
        GAP();
        sfor<4>([&](auto t) AI { a4[t] = pk2s(bflo(a4[t]) + bflo(m4[t]), bfhi(a4[t]) + bfhi(m4[t])); m4[t] = pk2s(w0 * kk[2 * t + 3], w0 * kk[2 * t + 4]); });
        GAP();
        sfor<4>([&](auto t) AI { a4[t] = pk2s(bflo(a4[t]) + bflo(m4[t]), bfhi(a4[t]) + bfhi(m4[t])); });
        GAP();
        u32x4 ov;
        sfor<4>([&](auto t) AI { ov[3 - t] = pk2s(bfhi(a4[t]) * vv[2 * t + 1], bflo(a4[t]) * vv[2 * t]); });
        const int dg = 2 * gi + (q >> 1);
        ldsw<u32x4>(CONV + (dg & 1) * CH_SZ + (dg >> 1 ? KB : KA) + 2048 + c16 * 128 + (((7 - 2 * j - (q & 1)) ^ (c16 & 7)) << 4), ov);
      });
    }
    waitl();
    lds_set(CVP + j, 1);
    __builtin_amdgcn_s_setprio(3);
    STAMP(17);
    wait_min4<0>(CVP, 1);
    {
      u32x4 o[2];
      sfor<2>([&](auto t) AI { o[t] = ldsr<u32x4>(kown + 2048 + (8 * t + l8) * 128 + ((c8 ^ l8) << 4)); });
      sfor<2>([&](auto t) AI { __builtin_amdgcn_raw_buffer_store_b128(o[t], rws, W_KV + (64 * pg + 16 * j + 8 * t + l8) * 4096 + (1984 - 64 * pa) * 2 + c8 * 16, 0, 16); });
    }
    waitv<0>();
    if (lds_arrive(CKV, lane) == 4 && lane == 0) __hip_atomic_store((GAS u64*)(ws + W_PF + b * 64), epoch, RLX_AGENT);
    STAMP(3);
    const int mh = j & 1, db = j >> 1, s31 = lane & 31, hh = lane >> 5;
    const int orow = 64 * rt + 32 * mh + s31, ocol = 64 * ct + 32 * db;
    u32x2 xr[4];
    u32 sg = 0;
    if (has) wait_min4<4>(FULL, 8);
    STAMP(24);
    {
      const char* p1 = ws + W_SG + (hh ? orow : rb * 128 + 32 * j + s31) * 8;
      const char* p2 = ws + W_CG + ((has ? cg : 0) * 192 + 48 * j + (lane < 48 ? lane : 0)) * 8;
      const char* p3 = j == 0 ? ws + W_PF + ((b >> 5) * 32 + s31) * 64 : p1;
      u64 g1, g2, f;
      gld8_sc1(g1, p1); gld8_sc1(g2, p2); gld8_sc1(f, p3);
      dma4(cb + (cp ? HB : HA), lane * 16, lane * 16, lane * 16, lane * 16, mk_rsrc(mkp(ka[12], ka[13]), D * S * 2), (2 * b + ch) * 4096);
      if (lane < (cp ? 33 : 16)) ldsw<u32>(cb + (cp ? 4160 : 0) + lane * 4, 0u);
      if (lane < (cp ? 28 : 30)) ldsw<u32x4>(kown + 4096 + lane * 16, u32x4{0u, 0u, 0u, 0u});
      asm volatile("s_waitcnt vmcnt(4)" : "+v"(g1), "+v"(g2), "+v"(f));
      if (j == 0 && __all(f == epoch)) lds_set(CVP + 4, 1);
#pragma unroll 1
      for (u32 it = 0; it < SPIN_CAP; ++it) {
        if (__all((u32)(g1 >> 32) == tag && (u32)(g2 >> 32) == tag)) break;
        __builtin_amdgcn_s_sleep(2);
        g1 = gld_agent(p1); g2 = gld_agent(p2);
      }
      sg = (u32)g1;
      if (has) {
        if (lane < 32) ldsw<f32x2>(ST + (32 * j + lane) * 8, f32x2{bflo(sg), __builtin_amdgcn_rcpf(bfhi(sg))});
        if (lane < 48) ldsw<u32>(CS + (48 * j + lane) * 4, (u32)g2);
      }
      waitl();
      lds_set(STRDY + j, 1);
      STAMP(4);
      sg = (u32)__builtin_amdgcn_permlane32_swap(sg, sg, false, false)[1];
      if (j == 0 && !__all(f == epoch)) { poll_flags<2>(ws, W_PF + (b >> 5) * 32 * 64, 32, epoch, lane); lds_set(CVP + 4, 1); }
      wait_byte<1>(CVP + 4, 1);
      u32x4 kvr[4];
      const char* src = ws + W_KV + (2 * b + ch) * 4096 + 2 * cp;
      sfor<4>([&](auto t) AI { gld4_sc1<t * 1024>(kvr[t], lane * 16, src); });
      wait_min4<1>(FREE, 6);
      sfor<2>([&](auto t) AI {
        const int r = 16 * t + (lane >> 2);
        dma(ZST + j * 2048 + t * 1024, r * 1024 + (((lane & 3) ^ ((r >> 1) & 3)) << 4), rq, (64 * rt + 32 * mh) * 1024 + ocol * 2);
      });
      asm volatile("s_waitcnt vmcnt(0)" : "+v"(kvr[0]), "+v"(kvr[1]), "+v"(kvr[2]), "+v"(kvr[3])::"memory");
      if (cp && lane == 63) kvr[3].w &= 0xffffu;
      sfor<4>([&](auto t) AI { ldsw<u32x4>(kown + t * 1024 + lane * 16, kvr[t]); });
      sfor<4>([&](auto g) AI { xr[g] = ldsr<u32x2>(ZST + j * 2048 + s31 * 64 + ((g ^ ((s31 >> 1) & 3)) << 4) + 8 * hh); });
      waitl();
      pin(xr[0]); pin(xr[1]); pin(xr[2]); pin(xr[3]);
      lds_set(CVP + j, 2);
      STAMP(5);
    }
    wait_min4<2>(FREE, CONVAFTER);
    wait_byte<0>(CVP + (j ^ 2), 2);
    __builtin_amdgcn_s_setprio(0);
    STAMP(6);
    f32x4 dacc[8];
    sfor<8>([&](auto i) AI { dacc[i] = f32x4{0.f, 0.f, 0.f, 0.f}; });
    u32 kv2[8];
    {
      u32 hb = cb + ((c16 & 1) ? HA : HB) + 4 * (c16 - 15) + 16 * q + 256 * cp;
      u32 kb_ = cb + ((c16 & 1) ? KA : KB) + 448 - 64 * (c16 >> 1) + 16 * q + 256 * cp;
      u32 hb2 = hb + 8;
      pin(hb); pin(hb2); pin(kb_);
      bf16x8 ta[12], kf[12];
      auto ld1 = [&](auto odd, auto n, auto w, auto off) AI {
        constexpr int r = (n < 4 && odd) ? 8 + n : n;
        if constexpr (w == 0) kf[r] = ldsr<bf16x8>(kb_ + 512 * (7 - n) + off);
        if constexpr (w == 1) { const u32x2 v = ldsr<u32x2>(hb + 512 * n + off); u32x4 t = __builtin_bit_cast(u32x4, ta[r]); t.x = v.x; t.y = v.y; ta[r] = __builtin_bit_cast(bf16x8, t); }
        if constexpr (w == 2) { const u32x2 v = ldsr<u32x2>(hb2 + 512 * n + off); u32x4 t = __builtin_bit_cast(u32x4, ta[r]); t.z = v.x; t.w = v.y; ta[r] = __builtin_bit_cast(bf16x8, t); }
      };
      auto cstep = [&](auto odd, auto off) AI {
        sfor<36>([&](auto g) AI {
          constexpr int c = cpair(35 - g) >> 3, dl = cpair(35 - g) & 7;
          constexpr int rc = (c < 4 && odd) ? 8 + c : c, rd_ = (dl < 4 && odd) ? 8 + dl : dl;
          dacc[c + dl] = __builtin_amdgcn_mfma_f32_16x16x32_bf16(kf[rc], ta[rd_], dacc[c + dl], 0, 0, 0);
          if constexpr (g < 12) ld1(IC<1 - odd>{}, IC<g / 3>{}, IC<g % 3>{}, off);
          else if constexpr (g < 24) ld1(IC<1 - odd>{}, IC<7 - (g - 12) / 3>{}, IC<g % 3>{}, off);
          GAP();
        });
      };
      sfor<24>([&](auto i) AI { ld1(IC<0>{}, IC<(i < 3 ? 0 : i < 6 ? 7 : i < 9 ? 1 : i < 12 ? 6 : i < 15 ? 2 : i < 18 ? 5 : i < 21 ? 3 : 4)>{}, IC<i % 3>{}, IC<0>{}); GAP(); });
#pragma unroll 1
      for (int i = 0; i < 2; ++i) {
        cstep(IC<0>{}, IC<64>{});
        cstep(IC<1>{}, IC<128>{});
        hb += 128; hb2 += 128; kb_ += 128;
      }
    }
    sfor<8>([&](auto i) AI { kv2[i] = ldsr<u32>(cb + KA + 2 * (2046 - (2 * c16 + 64 * q + 32 * (i & 1) + 256 * (i >> 1) + 1024 * cp))); });
    waitl();
    lds_set(CVP + j, 3);
    __builtin_amdgcn_s_setprio(3);
    STAMP(7);
    {
      const float dv = bflo(dvv);
      sfor<8>([&](auto i) AI { kv2[i] = pk2s(bfhi(kv2[i]) * dv, bflo(kv2[i]) * dv); });
      wait_byte<0>(CVP + (j ^ 2), 3);
      sfor<4>([&](auto i) AI { ldsw<f32x4>(hown + i * 1024 + lane * 16, cp ? dacc[i] : dacc[4 + i]); });
      waitl();
      lds_set(CVP + j, 4);
      wait_byte<0>(CVP + (j ^ 2), 4);
      f32x4 o[4];
      sfor<4>([&](auto i) AI { o[i] = ldsr<f32x4>(cb + (cp ? HA : XB) + i * 1024 + lane * 16); });
      waitl();
      lds_set(CVP + j, 5);
      sfor<4>([&](auto i) AI {
        const f32x4 m = cp ? dacc[4 + i] : dacc[i];
        sfor<2>([&](auto rr) AI {
          const u32 yb = pk2s(m[2 * rr] + o[i][2 * rr], m[2 * rr + 1] + o[i][2 * rr + 1]), kd = kv2[2 * i + rr];
          ldsw<u32>(ZST + j * 2048 + 4 * c16 + 128 * q + 64 * rr + 512 * i, pk2s(bflo(yb) + bflo(kd), bfhi(yb) + bfhi(kd)));
        });
      });
      waitl();
      const u32x4 zo[2] = {ldsr<u32x4>(ZST + j * 2048 + lane * 16), ldsr<u32x4>(ZST + j * 2048 + 1024 + lane * 16)};
      sfor<2>([&](auto t) AI {
        __builtin_amdgcn_raw_buffer_store_b128(zo[t], rws, lane * 16 + W_ZT + (2 * b + ch) * 4096 + cp * 2048 + t * 1024, 0, 16);
      });
      STAMP(16);
      waitv<0>();
      if (lds_arrive(CNT + 4, lane) == 4 && lane == 0) __hip_atomic_store((GAS u64*)(ws + W_ZF + b * 64), epoch, RLX_AGENT);
    }
    STAMP(8);
    const float mean = bflo(sg), rs = __builtin_amdgcn_rcpf(bfhi(sg));
    u32 xn[8];
    sfor<8>([&](auto i) AI {
      const u32 x2 = xr[i >> 1][i & 1], n2 = pk2s(bflo(x2) - mean, bfhi(x2) - mean);
      xn[i] = pk2s(bflo(n2) * rs, bfhi(n2) * rs);
      pin(xn[i]);
    });
    if (b == 0 && j == 3) {
      sfor<4>([&](auto t) AI { poll_flags<1>(ws, W_PF + t * 4096, 64, epoch, lane); });
      if (lane == 0) __hip_atomic_store((GAS u64*)(ws + W_E), epoch, RLX_AGENT);
    }
    if (j == 0) { poll_flags<2>(ws, W_ZF + ct * 32 * 64, 32, epoch, lane); lds_set(CVP + 5, 1); }
    else wait_byte<1>(CVP + 5, 1);
    STAMP(10);
    u32x4 zr[2];
    sfor<2>([&](auto t) AI {
      bld4_sc1<0>(zr[t], (lane >> 2) * 4096 + (lane & 3) * 16, rws, W_ZT + (ocol + 16 * t) * 4096 + (64 * rt + 32 * mh) * 2);
    });
    asm volatile("s_waitcnt vmcnt(0)" : "+v"(zr[0]), "+v"(zr[1])::"memory");
    sfor<2>([&](auto t) AI { ldsw<u32x4>(ZST + j * 2048 + t * 1024 + lane * 16, zr[t]); });
    waitl();
    float tt[16];
    sfor<4>([&](auto g) AI {
      const u32x2 z4 = ldtr(ZST + j * 2048 + (8 * g + 4 * hh + ((lane & 15) >> 2)) * 64 + 32 * ((lane >> 4) & 1) + 8 * (lane & 3));
      sfor<2>([&](auto e) AI {
        const u32 x2 = xr[g][(int)e], z2 = z4[(int)e];
        u32 n2 = pk2s(bflo(xn[2 * g + e]) * bflo(z2), bfhi(xn[2 * g + e]) * bfhi(z2));
        n2 = pk2s(bflo(x2) + bflo(n2), bfhi(x2) + bfhi(n2));
        tt[4 * g + 2 * e] = bflo(n2); tt[4 * g + 2 * e + 1] = bfhi(n2);
      });
    });
    sfor<16>([&](auto i) AI { pin(tt[i]); });
    STAMP(11);
    wait_min4<1>(FREE, 12);
    STAMP(12);
    u32x2 ob[4];
    sfor<4>([&](auto g) AI {
      const f32x4 v = ldsr<f32x4>(PART + mh * 8192 + db * 4096 + g * 1024 + lane * 16) + ldsr<f32x4>(PART + (2 + mh) * 8192 + db * 4096 + g * 1024 + lane * 16);
      ob[g].x = pk2(tt[4 * g] + rs * v.x, tt[4 * g + 1] + rs * v.y);
      ob[g].y = pk2(tt[4 * g + 2] + rs * v.z, tt[4 * g + 3] + rs * v.w);
    });
    sfor<2>([&](auto gp) AI {
      auto s0 = __builtin_amdgcn_permlane32_swap(ob[2 * gp].x, ob[2 * gp + 1].x, false, false);
      auto s1 = __builtin_amdgcn_permlane32_swap(ob[2 * gp].y, ob[2 * gp + 1].y, false, false);
      const u32x4 v = {(u32)s0[0], (u32)s1[0], (u32)s0[1], (u32)s1[1]};
      *(GAS u32x4*)((GAS char*)mkp(kb[2], kb[3]) + orow * 1024 + ocol * 2 + 32 * gp + 16 * hh) = v;
    });
    STAMP(13);
    DONE();
  } else if (wave < 4) {
    u32 junk, junk2;
    asm volatile("s_load_dword %0, %2, %3\n\ts_load_dword %1, %2, %4" : "=&s"(junk), "=&s"(junk2)
                 : "s"(pc0 & ~127ull), "s"((u32)(li + 32 * j) * 128u), "s"(min((u32)(li + 32 * (4 + j)) * 128u, CODE_BYTES)));
    const rsrc_t rws = mk_rsrc(ws, W_END);
    const int wm = wave >> 1, wn = wave & 1;
    const int l0 = lane_id();
    u32 wa = G_OFF + ((l0 >> 4) & 1) * (12288 + 32) + (8 * (l0 >> 5) + ((l0 & 15) >> 2)) * 192 + 96 * wn + 8 * (l0 & 3);
    u64 epoch;
    f32x16 acc[2][3];
    sfor<2>([&](auto m) AI { sfor<3>([&](auto n) AI { sfor<16>([&](auto r) AI { acc[m][n][(int)r] = 0.f; }); pin(acc[m][n]); }); });
    u32 xa[4];
    sfor<4>([&](auto ks) AI { xa[ks] = (64 * wm + (l0 & 31)) * 128 + (((2 * ks + (l0 >> 5)) ^ ((l0 >> 1) & 7)) << 4); pin(xa[ks]); });
    asm volatile("s_waitcnt lgkmcnt(0)" : "+s"(junk), "+s"(junk2));
    STAMP(10);
    if (has) {
      bf16x8 xf[2][2], wf[2][3];
      auto ldw = [&](auto st, auto n) AI { const u32 a = wa + (st & 3) * 3072 + n * 32; wf[st & 1][n] = cat(ldtr(a), ldtr(a + 768)); };
      auto ldx = [&](auto st, auto m) AI { xf[st & 1][m] = ldsr<bf16x8>(xa[st & 3] + m * 4096); };
      wait_min4<1>(FULL, 1);
      STAMP(1);
      ldw(IC<0>{}, IC<0>{}); ldx(IC<0>{}, IC<0>{}); ldx(IC<0>{}, IC<1>{}); ldw(IC<0>{}, IC<1>{}); ldw(IC<0>{}, IC<2>{});
      ldw(IC<1>{}, IC<0>{}); ldw(IC<1>{}, IC<1>{}); ldx(IC<1>{}, IC<0>{});
      GAP();
#pragma unroll 1
      for (int c = 0; c < 8; ++c) {
        const u32 adv = (c == 2 || c == 5) ? (u32)(-2 * (int)SLOT) : SLOT;
        const u32 need = c == 7 ? 0u : (u32)(c + 2);
        u32 fl;
        sfor<24>([&](auto gg) AI {
          constexpr int st = gg / 6, g = gg % 6, n = g >> 1, m = g & 1;
          acc[m][n] = __builtin_amdgcn_mfma_f32_32x32x16_bf16(wf[st & 1][n], xf[st & 1][m], acc[m][n], 0, 0, 0);
          if constexpr (g == 0) { ldw(IC<st + 1>{}, IC<2>{}); ldx(IC<st + 1>{}, IC<1>{}); }
          if constexpr (g == 2) ldw(IC<st + 2>{}, IC<0>{});
          if constexpr (g == 4) ldw(IC<st + 2>{}, IC<1>{});
          if constexpr (g == 5) ldx(IC<st + 2>{}, IC<0>{});
          if constexpr (st == 1 && g == 1) fl = lds_ld1(FULL);
          if constexpr (st == 1 && g == 3) { xa[0] += adv; xa[1] += adv; xa[2] += adv; pin(xa[0]); pin(xa[1]); pin(xa[2]); }
          if constexpr (st == 2 && g == 1) {
            xa[3] += adv; wa += adv; pin(xa[3]); pin(wa);
            if (!all_ge(rfl(fl), need)) wait_min4<0>(FULL, need);
          }
          if constexpr (st == 3 && g == 1) lds_set(FREE + wave, c + 1);
          GAP();
        });
      }
      STAMP(2);
      __builtin_amdgcn_s_setprio(2);
      const int lane = lane_id(), s31 = lane & 31, hh = lane >> 5;
      wait_min4<0>(STRDY, 1);
      epoch = wait_epoch();
      STAMP(11);
      f32x2 st[2];
      sfor<2>([&](auto m) AI { st[m] = ldsr<f32x2>(ST + (64 * wm + 32 * m + s31) * 8); });
      f32x4 c4[3][4];
      sfor<12>([&](auto i) AI { c4[i >> 2][i & 3] = ldsr<f32x4>(CS + (wn * 96 + (i >> 2) * 32 + 8 * (i & 3) + 4 * hh) * 4); });
      sfor<3>([&](auto n) AI {
        const int ks = 6 * cg + 3 * wn + n;
        if (ks < 86) {
          f32x2 g[8], u[8], e[8];
          sfor<8>([&](auto i) AI {
            constexpr int m = i >> 2, r = 2 * (i & 3);
            const f32x2 cg2 = {c4[n][r >> 2][r & 3], c4[n][r >> 2][(r & 3) + 1]};
            g[i] = f32x2{acc[m][n][r], acc[m][n][r + 1]} - st[m].x * cg2;
          });
          GAP();
          sfor<8>([&](auto i) AI { g[i] = g[i] * st[i >> 2].y; });
          GAP();
          sfor<8>([&](auto i) AI { e[i] = g[i] * g[i]; });
          GAP();
          sfor<8>([&](auto i) AI { e[i] = e[i] * -0.1029432397f - 2.3022081985f; });
          GAP();
          sfor<8>([&](auto i) AI { e[i] = e[i] * g[i]; });
          GAP();
          sfor<8>([&](auto i) AI {
            constexpr int m = i >> 2, r = 2 * (i & 3);
            const f32x2 cu2 = {c4[n][2 + (r >> 2)][r & 3], c4[n][2 + (r >> 2)][(r & 3) + 1]};
            e[i] = f32x2{__builtin_amdgcn_exp2f(e[i].x), __builtin_amdgcn_exp2f(e[i].y)};
            u[i] = f32x2{acc[m][n][r + 8], acc[m][n][r + 9]} - st[m].x * cu2;
            GAP();
          });
          sfor<8>([&](auto i) AI { e[i] = e[i] + 1.0f; });
          GAP();
          sfor<8>([&](auto i) AI {
            e[i] = f32x2{__builtin_amdgcn_rcpf(e[i].x), __builtin_amdgcn_rcpf(e[i].y)};
            g[i] = g[i] * u[i];
            GAP();
          });
          sfor<8>([&](auto i) AI { g[i] = g[i] * e[i]; });
          GAP();
          sfor<2>([&](auto m) AI {
            const u32x4 o = {pk2(g[4 * m].x, g[4 * m].y), pk2(g[4 * m + 1].x, g[4 * m + 1].y), pk2(g[4 * m + 2].x, g[4 * m + 2].y), pk2(g[4 * m + 3].x, g[4 * m + 3].y)};
            __builtin_amdgcn_raw_buffer_store_b128(o, rws, lane * 16 + W_H + ((4 * rb + 2 * wm + m) * 86 + ks) * 1024, 0, 16);
          });
          GAP();
        }
      });
      lds_set(FREE + wave, 9);
      STAMP(3);
      waitv<0>();
      if (lds_arrive(CNT + 0, lane) == 4 && lane == 0) __hip_atomic_store((GAS u64*)(ws + W_HF + (rb * 16 + cg) * 64), epoch, RLX_AGENT);
      STAMP(4);
    } else {
      if (csw) {
        f32x4 cs[3];
        sfor<3>([&](auto n) AI { cs[n] = f32x4{0.f, 0.f, 0.f, 0.f}; });
        const u32 o2_ = (l0 & 15) == ((l0 >> 4) & 1) ? 0x3f803f80u : 0u;
        const bf16x8 sel = __builtin_bit_cast(bf16x8, u32x4{o2_, o2_, o2_, o2_});
        wa += wm * 6144;
#pragma unroll 1
        for (int c = 0; c < 8; ++c) {
          wait_min4<1>(FULL, c + 1);
          bf16x8 w[6];
          sfor<6>([&](auto i) AI { const u32 a = wa + (i / 3) * 3072 + (i % 3) * 32; w[i] = cat(ldtr(a), ldtr(a + 768)); });
          waitl();
          lds_set(FREE + wave, c + 1);
          sfor<6>([&](auto i) AI { cs[i % 3] = __builtin_amdgcn_mfma_f32_16x16x32_bf16(w[i], sel, cs[i % 3], 0, 0, 0); });
          wa += (c == 2 || c == 5) ? (u32)(-2 * (int)SLOT) : SLOT;
        }
        if ((l0 & 15) < 2) sfor<3>([&](auto n) AI { ldsw<f32x4>(CS + (wm * 192 + wn * 96 + n * 32 + (l0 & 15) * 16 + 4 * (l0 >> 4)) * 4, cs[n]); });
        waitl();
      }
      lds_set(FREE + wave, 9);
      epoch = wait_epoch();
      if (csw && wave < 3) {
        wait_min4<1>(FREE, 9);
        const u32 i = 64 * wave + l0;
        const float v = ldsr<float>(CS + i * 4) + ldsr<float>(CS + 768 + i * 4);
        __hip_atomic_store((GAS u64*)(ws + W_CG + (cg * 192 + i) * 8), ((u64)(u32)(epoch >> 17) << 32) | __float_as_uint(v), RLX_AGENT);
      }
      STAMP(4);
    }
    const int lane = lane_id(), hh = lane >> 5, g16 = lane >> 4, q4 = (lane & 15) >> 2, p4 = lane & 3;
    const int kh = wave >> 1, mh = wave & 1;
    u32x4 hf[43];
    const u32 hso = W_H + ((2 * rt + mh) * 86 + 43 * kh) * 1024, hvo = lane * 16;
    f32x16 o2[2];
    sfor<2>([&](auto n) AI { sfor<16>([&](auto r) AI { o2[n][(int)r] = 0.f; }); pin(o2[n]); });
    u32 db_[2][3];
    sfor<2>([&](auto n) AI {
      const u32 da = kh * 8192 + (4 * hh + q4) * 128 + ((4 * ((int)n ^ (q4 >> 1)) + 2 * (g16 & 1) + (p4 >> 1)) << 4) + 8 * (p4 & 1);
      sfor<3>([&](auto w) AI { db_[n][w] = da + w * 61440; pin(db_[n][w]); });
    });
    u32 gvo = (lane < 43 ? lane : 42) * 1024 + ct * 128;
    pin(gvo);
    u32 junkv = 0;
    bf16x8 df[3][2];
    u32 fl;
    auto ldd = [&](auto k) AI {
      constexpr int c = k >> 2, ks = k & 3;
      constexpr u32 wb = (c < 7 ? 16384u * c : c == 7 ? CONV : c == 8 ? CONV + CH_SZ : 16384u * (c - 9)) + ks * 2048;
      constexpr int w = wb / 61440 > 2 ? 2 : wb / 61440;
      sfor<2>([&](auto n) AI { const u32 a = db_[n][w] + (wb - w * 61440); df[k % 3][n] = cat(ldtr(a), ldtr(a + 1024)); });
    };
    if (wave == 0) { poll_flags<1>(ws, W_HF + (rt >> 1) * 16 * 64, 15, epoch, lane); lds_set(CVP + 6, 1); }
    else wait_byte<0>(CVP + 6, 1);
    STAMP(5);
    asm volatile("buffer_load_dword %0, %1, %2, %3 offen sc1" : "=v"(junkv) : "v"(gvo), "s"(rws), "s"(hso));
    auto hload = [&](auto k) AI { bld4_sc1<(k & 3) * 1024>(hf[k], hvo, rws, hso + (k >> 2) * 4096); };
    sfor<PD>([&](auto k) AI { hload(k); });
    wait_min4<0>(WDRDY, 1);
    STAMP(6);
    ldd(IC<0>{}); ldd(IC<1>{});
    GAP();
    sfor<43>([&](auto k) AI {
      if constexpr (k == 10) lds_set(FREE + wave, 10);
      if constexpr (k == 18) lds_set(FREE + wave, 11);
      if constexpr (k == 24 || k == 32) fl = lds_ld1(WDRDY);
      if constexpr (k == 40) fl = lds_ld1(FREE);
      if constexpr (k == 26 || k == 34) { STAMP(k == 26 ? 12 : 14); if (!all_ge(rfl(fl), k == 26 ? 2u : 3u)) wait_min4<0>(WDRDY, k == 26 ? 2 : 3); STAMP(k == 26 ? 13 : 15); GAP(); }
      constexpr int younger = (k + PD <= 43 ? PD : 43 - k) - 1;
      waitv_pin<younger>(hf[k]);
      if constexpr (k == 0) { pin(junkv); STAMP(24); }
      if constexpr (k == 8) STAMP(25);
      if constexpr (k == 16) STAMP(26);
      o2[0] = __builtin_amdgcn_mfma_f32_32x32x16_bf16(df[k % 3][0], __builtin_bit_cast(bf16x8, hf[k]), o2[0], 0, 0, 0);
      if constexpr (k + 2 < 43) ldd(IC<k + 2>{});
      GAP();
      o2[1] = __builtin_amdgcn_mfma_f32_32x32x16_bf16(df[k % 3][1], __builtin_bit_cast(bf16x8, hf[k]), o2[1], 0, 0, 0);
      if constexpr (k + PD < 43) hload(IC<k + PD>{});
      GAP();
    });
    STAMP(7);
    if (!all_ge(rfl(fl), 11u)) wait_min4<0>(FREE, 11);
    sfor<2>([&](auto n) AI { sfor<4>([&](auto g) AI {
      f32x4 v = {o2[n][4 * g], o2[n][4 * g + 1], o2[n][4 * g + 2], o2[n][4 * g + 3]};
      ldsw<f32x4>(PART + wave * 8192 + n * 4096 + g * 1024 + lane * 16, v);
    }); });
    waitl();
    lds_set(FREE + wave, 12);
    STAMP(8);
    DONE();
  } else {
    __builtin_amdgcn_s_setprio(PRL);
    const rsrc_t rg = mk_rsrc(mkp(ka[2], ka[3]), D * F * 2), ru = mk_rsrc(mkp(ka[4], ka[5]), D * F * 2);
    const int crot = ROT * ((rb + cg) & 1);
    auto chx = [&](int c, u32 sb) AI { dma4(sb + 4 * j * 1024, xv[0], xv[1], xv[2], xv[3], rq, (rb * 128 + 32 * j) * 1024 + (c ^ crot) * 128); };
    auto chw = [&](int c, u32 sb) AI {
      dma3(sb + G_OFF + 3 * j * 1024, wv[0], wv[1], wv[2], rg, (c ^ crot) * 64 * F * 2);
      dma3(sb + U_OFF + 3 * j * 1024, wv[0], wv[1], wv[2], ru, (c ^ crot) * 64 * F * 2);
    };
    if (has) {
      chx(0, 0); chw(0, 0); chx(1, SLOT); chw(1, SLOT);
      waitv<10>(); lds_set(FULL + j, 1);
      STAMP(1);
      u32 sb = 2 * SLOT;
#pragma unroll 1
      for (int c = 2; c < 8; ++c) {
        wait_min4<1>(FREE, c - 2);
        chx(c, sb);
        waitv<4>(); lds_set(FULL + j, c);
        chw(c, sb);
        sb = sb == 2 * SLOT ? 0 : sb + SLOT;
      }
      waitv<0>(); lds_set(FULL + j, 8);
    } else if (csw) {
      chw(0, 0); chw(1, SLOT);
      u32 sb = 2 * SLOT;
#pragma unroll 1
      for (int c = 2; c < 8; ++c) {
        wait_min4<1>(FREE, c - 2);
        chw(c, sb);
        waitv<12>(); lds_set(FULL + j, c - 1);
        sb = sb == 2 * SLOT ? 0 : sb + SLOT;
      }
      waitv<0>(); lds_set(FULL + j, 8);
    }
    STAMP(2);
    const rsrc_t rd = mk_rsrc(mkp(kb[0], kb[1]), F * D * 2);
    u32 dv[4];
    sfor<4>([&](auto t) AI { dv[t] = (7 * t + l8) * 1024 + ((c8 ^ (((l8 >> 1) & 1) << 2)) << 4) + ct * 128; });
    auto wd_groups = [&](int lo, int hi, u32 ldelta) AI {
#pragma unroll 1
      for (int g = lo + j; g < hi; g += 4) dma4(g * 4096 + ldelta, dv[0], dv[1], dv[2], dv[3], rd, (((g >> 1) & 1) * 688 + (g >> 2) * 64 + (g & 1) * 32) * 1024);
    };
    wait_min4<1>(FREE, WD1);
    wd_groups(21, 28, 0);
    wait_min4<1>(FREE, WD2);
    wd_groups(0, 10, 0);
    wait_min4<1>(FREE, 8);
    wd_groups(10, 21, 0);
    u32 junk = 0;
    if (j < 2) gather(junk, ((j ? 688 : 0) + 448 + min(4 * lane + (li & 3), 239)) * 1024 + ct * 128, rd, 0);
    asm volatile("s_waitcnt vmcnt(0)" : "+v"(junk)::"memory");
    lds_set(WDRDY + j, 1);
    STAMP(3);
    wait_min4<2>(CVP, 5);
    wd_groups(28, 32, CONV - 28 * 4096);
    wd_groups(32, 36, CONV + CH_SZ - 32 * 4096);
    waitv<0>(); lds_set(WDRDY + j, 2);
    STAMP(4);
    wait_min4<1>(FREE, 10);
    wd_groups(36, 44, 0u - 36 * 4096);
    waitv<0>(); lds_set(WDRDY + j, 3);
    STAMP(5);
    DONE();
  }
}

extern "C" void kernel_launch(void* const* d_in, const int* in_sizes, int n_in, void* d_out, int out_size, void* d_ws, size_t ws_size,
                              hipStream_t stream) {
  Args a;
  a.q = (const u16*)d_in[0]; a.k = (const u16*)d_in[1]; a.v = (const u16*)d_in[2]; a.sk = (const u16*)d_in[3];
  a.h = (const u16*)d_in[4]; a.dv = (const u16*)d_in[5];
  a.wg = (const u16*)d_in[7]; a.wu = (const u16*)d_in[8]; a.wd = (const u16*)d_in[9];
  a.out = (u16*)d_out; a.ws = (char*)d_ws; a.pad = 0;
  (void)hipFuncSetAttribute((const void*)fused, hipFuncAttributeMaxDynamicSharedMemorySize, LDS_BYTES);
  fused<<<256, 768, LDS_BYTES, stream>>>(a);
}
```

```cpp
#include <hip/hip_runtime.h>
#include <stdint.h>
#include <utility>

typedef unsigned short u16;
typedef unsigned int u32;
typedef unsigned long long u64;
typedef __bf16 bf16x8 __attribute__((ext_vector_type(8)));
typedef __bf16 bf16x2 __attribute__((ext_vector_type(2)));
typedef short s16x4 __attribute__((ext_vector_type(4)));
typedef short s16x8 __attribute__((ext_vector_type(8)));
typedef float f32x16 __attribute__((ext_vector_type(16)));
typedef float f32x4 __attribute__((ext_vector_type(4)));
typedef float f32x2 __attribute__((ext_vector_type(2)));
typedef u32 u32x4 __attribute__((ext_vector_type(4)));
typedef u32 u32x2 __attribute__((ext_vector_type(2)));
typedef __amdgpu_buffer_rsrc_t rsrc_t;
#define DI __device__ __forceinline__
#define AI __attribute__((always_inline))
#define LAS __attribute__((address_space(3)))
#define GAS __attribute__((address_space(1)))
#define RLX_AGENT __ATOMIC_RELAXED, __HIP_MEMORY_SCOPE_AGENT
#define RLX_WG __ATOMIC_RELAXED, __HIP_MEMORY_SCOPE_WORKGROUP

constexpr int S = 2048, D = 512, F = 1376;
constexpr u32 SLOT = 40992, G_OFF = 16384, U_OFF = 16384 + 12288 + 32;
constexpr u32 CONV = 3 * SLOT;
constexpr u32 CH_SZ = 17520, HA = 64, HB = 4292, KA = 8400, KB = 12976, XB = 4304;
constexpr u32 MISC = CONV + 2 * CH_SZ;
constexpr u32 CS = MISC, ST = MISC + 1536, CNT = MISC + 2560;
constexpr u32 CKV = CNT + 8, EPO = CNT + 16, CVP = CNT + 24, FULL = CNT + 32, FREE = CNT + 36, STRDY = CNT + 40, WDRDY = CNT + 44;
constexpr u32 LDS_BYTES = MISC + 2816;
constexpr u32 ZST = 114688, PART = 32768;
constexpr u32 W_E = 0, W_PF = 4096, W_HF = W_PF + 16384, W_ZF = W_HF + 16384, W_SG = W_ZF + 16384, W_CG = W_SG + 16384;
constexpr u32 W_KV = 131072, W_ZT = W_KV + 2097152 + 4096, W_H = W_ZT + 2097152 + 4096, W_END = W_H + 64 * 86 * 1024;
constexpr u32 W_DBG = 16u << 20;
constexpr u32 CODE_BYTES = 22528;
#define PRL 1
#define PRS 0
#define CONVAFTER 9
#define LSLEEP 1
#define PRA 0
#define ROT 4
#define KVSTAG 0
#define SSTOP 9
#define POLLS 4
#define PD 10
#define WD1 6
#define WD2 7
#define KVAFTER 1
constexpr u32 SPIN_CAP = 1u << 18;

constexpr int cpair(int g) {
  int i = 0;
  for (int n = 0; n < 8; ++n) for (int m = 0; m <= n; ++m) if (m + n <= 7) {
    if (i++ == g) return n * 8 + m;
    if (m < n && i++ == g) return m * 8 + n;
  }
  return 0;
}
template <class Fn, int... I> DI void sfor_impl(Fn&& f, std::integer_sequence<int, I...>) { (f(std::integral_constant<int, I>{}), ...); }
template <int N, class Fn> DI void sfor(Fn&& f) { sfor_impl(f, std::make_integer_sequence<int, N>{}); }

DI rsrc_t mk_rsrc(const void* p, u32 bytes) { return __builtin_amdgcn_make_buffer_rsrc((void*)p, (short)0, (int)bytes, 0x00020000); }
DI void dma(u32 lds, u32 voff, rsrc_t r, u32 soff) {
  asm volatile("s_mov_b32 m0, %0\n\ts_nop 0\n\tbuffer_load_dwordx4 %1, %2, %3 offen lds" ::"s"(lds), "v"(voff), "s"(r), "s"(soff) : "memory");
}

DI void dma4(u32 lds, u32 v0, u32 v1, u32 v2, u32 v3, rsrc_t r, u32 soff) {
  asm volatile("s_mov_b32 m0, %0\n\ts_nop 0\n\tbuffer_load_dwordx4 %1, %5, %6 offen lds\n\tbuffer_load_dwordx4 %2, %5, %6 offen offset:1024 lds\n\t"
               "buffer_load_dwordx4 %3, %5, %6 offen offset:2048 lds\n\tbuffer_load_dwordx4 %4, %5, %6 offen offset:3072 lds"
               ::"s"(lds), "v"(v0), "v"(v1), "v"(v2), "v"(v3), "s"(r), "s"(soff) : "memory");
}
DI void dma3(u32 lds, u32 v0, u32 v1, u32 v2, rsrc_t r, u32 soff) {
  asm volatile("s_mov_b32 m0, %0\n\ts_nop 0\n\tbuffer_load_dwordx4 %1, %4, %5 offen lds\n\tbuffer_load_dwordx4 %2, %4, %5 offen offset:1024 lds\n\t"
               "buffer_load_dwordx4 %3, %4, %5 offen offset:2048 lds"
               ::"s"(lds), "v"(v0), "v"(v1), "v"(v2), "s"(r), "s"(soff) : "memory");
}
template <int OFF> DI void bld4_sc1(u32x4& d, u32 voff, rsrc_t r, u32 soff) {
  asm volatile("buffer_load_dwordx4 %0, %1, %2, %3 offen offset:%4 sc1" : "=v"(d) : "v"(voff), "s"(r), "s"(soff), "i"(OFF));
}
DI void bld4(u32x4& d, u32 voff, rsrc_t r, u32 soff) { asm volatile("buffer_load_dwordx4 %0, %1, %2, %3 offen" : "=v"(d) : "v"(voff), "s"(r), "s"(soff)); }
template <int OFF> DI void bld2(u32x2& d, u32 voff, rsrc_t r) { asm volatile("buffer_load_dwordx2 %0, %1, %2, 0 offen offset:%3" : "=v"(d) : "v"(voff), "s"(r), "i"(OFF)); }
template <int OFF> DI void gld4_sc1(u32x4& d, u32 voff, const char* base) {
  asm volatile("global_load_dwordx4 %0, %1, %2 offset:%3 sc1" : "=v"(d) : "v"(voff), "s"(base), "i"(OFF));
}
template <int N> DI void waitv_pin(u32x4& d) { asm volatile("s_waitcnt vmcnt(%1)" : "+v"(d) : "i"(N)); }
template <int N> DI void waitv() { asm volatile("s_waitcnt vmcnt(%0)" ::"i"(N) : "memory"); }
DI void bar() { asm volatile("s_waitcnt lgkmcnt(0)\n\ts_barrier" ::: "memory"); }
DI void waitl() { asm volatile("s_waitcnt lgkmcnt(0)" ::: "memory"); }
DI float bflo(u32 x) { return __uint_as_float(x << 16); }
DI float bfhi(u32 x) { return __uint_as_float(x & 0xffff0000u); }
DI u32 pk2(float lo, float hi) { f32x2 v = {lo, hi}; return __builtin_bit_cast(u32, __builtin_convertvector(v, bf16x2)); }
DI u32 pk2s(float lo, float hi) { asm("" : "+v"(lo)); asm("" : "+v"(hi)); return pk2(lo, hi); }
DI float rbf(float x) { return bflo(pk2(x, x)); }
template <class T> DI T ldsr(u32 a) { return *(const LAS T*)a; }
template <class T> DI void ldsw(u32 a, T v) { *(LAS T*)a = v; }
DI u32x2 ldtr(u32 a) { return __builtin_bit_cast(u32x2, __builtin_amdgcn_ds_read_tr16_b64_v4i16((LAS s16x4*)a)); }
DI bf16x8 cat(u32x2 a, u32x2 b) { u32x4 v = {a.x, a.y, b.x, b.y}; return __builtin_bit_cast(bf16x8, v); }
template <int CTRL, int RM> DI float dpp_add(float v) {
  return v + __builtin_bit_cast(float, __builtin_amdgcn_update_dpp(0, __builtin_bit_cast(int, v), CTRL, RM, 0xf, false));
}
DI float wave_sum(float v) {
  v = dpp_add<0x111, 0xf>(v); v = dpp_add<0x112, 0xf>(v); v = dpp_add<0x114, 0xf>(v); v = dpp_add<0x118, 0xf>(v);
  v = dpp_add<0x142, 0xa>(v); v = dpp_add<0x143, 0xc>(v);
  return __builtin_bit_cast(float, __builtin_amdgcn_readlane(__builtin_bit_cast(int, v), 63));
}
DI void wave_sum2(float& a, float& c) {
  a = dpp_add<0x111, 0xf>(a); c = dpp_add<0x111, 0xf>(c); a = dpp_add<0x112, 0xf>(a); c = dpp_add<0x112, 0xf>(c);
  a = dpp_add<0x114, 0xf>(a); c = dpp_add<0x114, 0xf>(c); a = dpp_add<0x118, 0xf>(a); c = dpp_add<0x118, 0xf>(c);
  a = dpp_add<0x142, 0xa>(a); c = dpp_add<0x142, 0xa>(c); a = dpp_add<0x143, 0xc>(a); c = dpp_add<0x143, 0xc>(c);
  a = __builtin_bit_cast(float, __builtin_amdgcn_readlane(__builtin_bit_cast(int, a), 63));
  c = __builtin_bit_cast(float, __builtin_amdgcn_readlane(__builtin_bit_cast(int, c), 63));
}
DI void gld8_sc1(u64& d, const char* p) { asm volatile("global_load_dwordx2 %0, %1, off sc1" : "=v"(d) : "v"(p)); }
DI void gld_u16(u32& d, u32 voff, const char* base) { asm volatile("global_load_ushort %0, %1, %2" : "=v"(d) : "v"(voff), "s"(base)); }
template <int SLEEP> DI void poll_flags(const char* ws, u32 off, int n, u64 epoch, int lane) {
  const GAS u64* p = (const GAS u64*)(ws + off + (u32)(lane < n ? lane : 0) * 64u);
#pragma unroll 1
  for (u32 it = 0; it < SPIN_CAP; ++it) {
    u64 v = __hip_atomic_load(p, RLX_AGENT);
    if (__all(v == epoch)) break;
    __builtin_amdgcn_s_sleep(SLEEP);
  }
  asm volatile("" ::: "memory");
}
DI u32 poll_granule(const char* ws, u32 row, u32 tag) {
  const GAS u64* p = (const GAS u64*)(ws + W_SG + row * 8u);
  u64 v = 0;
#pragma unroll 1
  for (u32 it = 0; it < SPIN_CAP; ++it) {
    v = __hip_atomic_load(p, RLX_AGENT);
    if (__all((u32)(v >> 32) == tag)) break;
    __builtin_amdgcn_s_sleep(1);
  }
  return (u32)v;
}
DI u32 lds_arrive(u32 a, int lane) {
  u32 r = 0;
  waitl();
  if (lane == 0) r = __hip_atomic_fetch_add((LAS u32*)a, 1u, RLX_WG);
  return (u32)__builtin_amdgcn_readfirstlane(r) + 1u;
}
DI void lds_wait(u32 a, u32 want) {
#pragma unroll 1
  for (u32 it = 0; it < SPIN_CAP * 16; ++it) {
    u32 v = __hip_atomic_load((LAS u32*)a, RLX_WG);
    if ((u32)__builtin_amdgcn_readfirstlane(v) >= want) break;
  }
  asm volatile("" ::: "memory");
}

DI int lane_id() { int l; asm volatile("v_mbcnt_lo_u32_b32 %0, -1, 0\n\tv_mbcnt_hi_u32_b32 %0, -1, %0" : "=v"(l)); return l; }
DI u32 rfl(u32 x) { return (u32)__builtin_amdgcn_readfirstlane(x); }
DI u32x2 lds_ld2(u32 a) { return __builtin_bit_cast(u32x2, __hip_atomic_load((LAS u64*)a, RLX_WG)); }
template <int SLEEP> DI void wait_min2(u32 a, u32 want) {
#pragma unroll 1
  for (u32 it = 0; it < SPIN_CAP * 16; ++it) {
    const u32x2 v = lds_ld2(a);
    if (rfl(min(v.x, v.y)) >= want) break;
    if (SLEEP) __builtin_amdgcn_s_sleep(SLEEP);
  }
  asm volatile("" ::: "memory");
}
DI u32 lds_ld1(u32 a) { return __hip_atomic_load((LAS u32*)a, RLX_WG); }
DI bool all_ge(u32 w, u32 want) { return ((w - want * 0x01010101u) & 0x80808080u) == 0u; }
template <int SLEEP> DI void wait_min4(u32 a, u32 want) {
#pragma unroll 1
  for (u32 it = 0; it < SPIN_CAP * 16; ++it) {
    if (all_ge(rfl(lds_ld1(a)), want)) break;
    if (SLEEP) __builtin_amdgcn_s_sleep(SLEEP);
  }
  asm volatile("" ::: "memory");
}
template <int SLEEP> DI void wait_byte(u32 a, u32 want) {
#pragma unroll 1
  for (u32 it = 0; it < SPIN_CAP * 16; ++it) {
    if (rfl(__hip_atomic_load((LAS unsigned char*)a, RLX_WG)) >= want) break;
    if (SLEEP) __builtin_amdgcn_s_sleep(SLEEP);
  }
  asm volatile("" ::: "memory");
}
DI u64 wait_epoch() {
  u32x2 e;
#pragma unroll 1
  for (u32 it = 0; it < SPIN_CAP * 16; ++it) {
    e = lds_ld2(EPO);
    if (rfl(e.x) != 0u) break;
    __builtin_amdgcn_s_sleep(2);
  }
  return ((u64)rfl(e.y) << 32) | rfl(e.x);
}
DI u64 gld_agent(const char* p) { return __hip_atomic_load((const GAS u64*)p, RLX_AGENT); }
DI void lds_set(u32 a, u32 v) { __hip_atomic_store((LAS unsigned char*)a, (unsigned char)v, RLX_WG); }
DI void gather(u32& junk, u32 voff, rsrc_t r, u32 soff) { asm volatile("buffer_load_dword %0, %1, %2, %3 offen" : "=v"(junk) : "v"(voff), "s"(r), "s"(soff)); }
template <int N> using IC = std::integral_constant<int, N>;

#define DONE() do {} while (0)
#define STAMPI(id) do {} while (0)
#define STAMP(id) do {} while (0)

struct Args { const u16 *q, *wg, *wu, *k, *v, *sk, *h, *dv, *wd; u16* out; char* ws; u64 pad; };
typedef u32 u32x16 __attribute__((ext_vector_type(16)));
typedef u32 u32x8 __attribute__((ext_vector_type(8)));
DI char* mkp(u32 lo, u32 hi) { return (char*)(((u64)hi << 32) | lo); }
#define GAP() __builtin_amdgcn_sched_barrier(0)
DI void barx() { asm volatile("s_barrier" ::: "memory"); }
template <class T> DI void pin(T& x) { asm volatile("" : "+v"(x)); }

__global__ void __launch_bounds__(768, 3) fused(Args) {
  u64 pc0; asm volatile("s_getpc_b64 %0" : "=s"(pc0));
  const u64 tstart_ = __builtin_amdgcn_s_memrealtime();
  const u64 cstart_ = __builtin_amdgcn_s_memtime();
  asm volatile("s_waitcnt lgkmcnt(0)");
  u32x16 ka; u32x8 kb;
  asm volatile("s_load_dwordx16 %0, %2, 0x0\n\ts_load_dwordx8 %1, %2, 0x40" : "=&s"(ka), "=&s"(kb) : "s"(__builtin_amdgcn_kernarg_segment_ptr()));
  GAP();
  const int tid = threadIdx.x, lane = tid & 63;
  const int wave = __builtin_amdgcn_readfirstlane(tid >> 6);
  const int b = blockIdx.x, xcd = b & 7, li = b >> 3;
  if (tid < 12) ldsw<u32>(CNT + tid * 4, 0u);
  const bool has = li < 30;
  const int id = 30 * xcd + (has ? li : 0), rr_ = id % 120, rb = 8 * (id / 120) + (rr_ & 7);
  const int cg = has ? rr_ >> 3 : 4 * (xcd & 3) + 2 * (xcd >> 2) + (li - 30);
  const bool csw = !has && cg < 15;
  const int rt = 4 * xcd + (li & 3), ct = li >> 2;
  const int j = wave & 3, l8 = lane >> 3, c8 = lane & 7;
  u32 xv[4], wv[3];
  sfor<4>([&](auto t) AI { xv[t] = (7 * t + l8) * 1024 + ((c8 ^ ((4 * t + (l8 >> 1)) & 7)) << 4); pin(xv[t]); });
  sfor<3>([&](auto t) AI {
    const u32 byte = t * 1024 + lane * 16, row = byte / 192u;
    u32 seg = byte - row * 192u;
    if (cg == 14) seg &= 48;
    wv[t] = (16 * j + row) * (F * 2) + cg * 192 + seg - t * 1024;
    pin(wv[t]);
  });
  asm volatile("s_waitcnt lgkmcnt(0)\n\ts_barrier" : "+s"(ka), "+s"(kb)::"memory");
  STAMP(9);
  char* const ws = mkp(kb[4], kb[5]);
  const rsrc_t rq = mk_rsrc(mkp(ka[0], ka[1]), S * D * 2);
  if (wave >= 8) {
    __builtin_amdgcn_s_setprio(3);
    u64 epoch;
    asm volatile("s_load_dwordx2 %0, %1, 0x0" : "=s"(epoch) : "s"(ws));
    const int lane = lane_id(), l8 = lane >> 3, c8 = lane & 7;
    const int ch = j & 1, cp = j >> 1, c16 = lane & 15, q = lane >> 4;
    const rsrc_t rws = mk_rsrc(ws, W_END);
    const u32 cb = CONV + ch * CH_SZ, hown = cb + (cp ? XB : HA), kown = cb + (cp ? KB : KA);
    const int srow = 256 * xcd + 8 * li + 2 * j;
    if (SSTOP == 0) wait_min4<8>(FREE, 8);
    u32x4 sx[2];
    sfor<2>([&](auto r) AI { bld4(sx[r], lane * 16, rq, (srow + r) * 1024); });
    STAMP(23);
    const int pa = b & 31, pg = b >> 5, s0 = 64 * pa + 16 * j;
    u32x2 skw[2];
    u32 dvv;
    auto kvload = [&]() AI {
      const rsrc_t rk = mk_rsrc(mkp(ka[6], ka[7]), S * D * 2), rv = mk_rsrc(mkp(ka[8], ka[9]), S * D * 2), rs_ = mk_rsrc(mkp(ka[10], ka[11]), D * 8);
      const u32 kvv = l8 * 1024 + c8 * 16 + pg * 128;
      sfor<3>([&](auto t) AI { const int r = s0 - 8 + 8 * t; dma(hown + t * 1024, kvv, rk, (r < 0 ? 0 : r) * 1024); });
      sfor<2>([&](auto t) AI { dma(kown + t * 1024, kvv, rv, (s0 + 8 * t) * 1024); });
      sfor<2>([&](auto t) AI { bld2<256 * t>(skw[t], (64 * pg + 16 * (q >> 1) + c16) * 8, rs_); });
      gld_u16(dvv, (2 * b + ch) * 2, mkp(ka[14], ka[15]));
    };
    asm volatile("s_waitcnt lgkmcnt(0)" : "+s"(epoch));
    epoch += 0x9E3779B97F4A7C15ull;
    epoch = (epoch ^ (epoch >> 30)) * 0xBF58476D1CE4E5B9ull; epoch = (epoch ^ (epoch >> 27)) * 0x94D049BB133111EBull;
    epoch = (epoch ^ (epoch >> 31)) | 1ull;
    ldsw<u64>(EPO, epoch);
    STAMP(18);
    const u32 tag = (u32)(epoch >> 17);
    if (has) wait_min4<2>(FULL, KVAFTER + KVSTAG * (li & 3));
    STAMP(19);
    kvload();
    STAMP(20);
    asm volatile("s_waitcnt vmcnt(8)" : "+v"(sx[0]), "+v"(sx[1]));
    STAMP(21);
    __builtin_amdgcn_s_setprio(0);
    {
      float x[2][8], mean[2], var[2];
      sfor<2>([&](auto r) AI {
        sfor<4>([&](auto i) AI { x[r][2 * i] = bflo(sx[r][(int)i]); x[r][2 * i + 1] = bfhi(sx[r][(int)i]); });
        mean[r] = x[r][0];
        sfor<7>([&](auto i) AI { mean[r] += x[r][i + 1]; });
      });
      wave_sum2(mean[0], mean[1]);
      sfor<2>([&](auto r) AI {
        mean[r] *= 1.0f / D; var[r] = 0.f;
        sfor<8>([&](auto i) AI { float d = x[r][i] - mean[r]; asm("" : "+v"(d)); var[r] += d * d; });
      });
      wave_sum2(var[0], var[1]);
      STAMP(22);
      const float mn = lane ? mean[1] : mean[0], vr = rbf((lane ? var[1] : var[0]) * (1.0f / D));
      const u32 g = pk2s(mn, __builtin_sqrtf(rbf(vr + 1e-5f)));
      if (lane < 2) __hip_atomic_store((GAS u64*)(ws + W_SG + (srow + lane) * 8), ((u64)tag << 32) | g, RLX_AGENT);
    }
    STAMP(1);
    asm volatile("s_waitcnt vmcnt(1)" : "+v"(skw[0]), "+v"(skw[1]), "+v"(dvv)::"memory");
    STAMP(2);
    {
      const bool first = s0 == 0 && (q & 1) == 0;
      u32x2 kr[2][5];
      sfor<2>([&](auto gi) AI {
        const u32 a = (8 * (q & 1) + (c16 >> 2)) * 128 + 64 * gi + 32 * (q >> 1) + 8 * (lane & 3);
        kr[gi][0] = ldtr(hown + a + 512); kr[gi][1] = ldtr(hown + a + 1024); kr[gi][2] = ldtr(hown + a + 1536); kr[gi][3] = ldtr(kown + a); kr[gi][4] = ldtr(kown + a + 512);
      });
      GAP();
      if (SSTOP == 1) wait_min4<8>(FREE, 8);
      sfor<2>([&](auto gi) AI {
        const float w0 = bflo(skw[gi].x), w1 = bfhi(skw[gi].x), w2 = bflo(skw[gi].y), w3 = bfhi(skw[gi].y);
        const u32x2 k0 = kr[gi][0], k1 = kr[gi][1], k2 = kr[gi][2], v0 = kr[gi][3], v1 = kr[gi][4];
        const float kk[11] = {first ? 0.f : bfhi(k0.x), first ? 0.f : bflo(k0.y), first ? 0.f : bfhi(k0.y), bflo(k1.x), bfhi(k1.x), bflo(k1.y), bfhi(k1.y), bflo(k2.x), bfhi(k2.x), bflo(k2.y), bfhi(k2.y)};
        const float vv[8] = {bflo(v0.x), bfhi(v0.x), bflo(v0.y), bfhi(v0.y), bflo(v1.x), bfhi(v1.x), bflo(v1.y), bfhi(v1.y)};
        u32 a4[4], m4[4];
        sfor<4>([&](auto t) AI { a4[t] = pk2s(w3 * kk[2 * t], w3 * kk[2 * t + 1]); m4[t] = pk2s(w2 * kk[2 * t + 1], w2 * kk[2 * t + 2]); });
        GAP();
        sfor<4>([&](auto t) AI { a4[t] = pk2s(bflo(a4[t]) + bflo(m4[t]), bfhi(a4[t]) + bfhi(m4[t])); m4[t] = pk2s(w1 * kk[2 * t + 2], w1 * kk[2 * t + 3]); });
        GAP();
        sfor<4>([&](auto t) AI { a4[t] = pk2s(bflo(a4[t]) + bflo(m4[t]), bfhi(a4[t]) + bfhi(m4[t])); m4[t] = pk2s(w0 * kk[2 * t + 3], w0 * kk[2 * t + 4]); });
        GAP();
        sfor<4>([&](auto t) AI { a4[t] = pk2s(bflo(a4[t]) + bflo(m4[t]), bfhi(a4[t]) + bfhi(m4[t])); });
        GAP();
        u32x4 ov;
        sfor<4>([&](auto t) AI { ov[3 - t] = pk2s(bfhi(a4[t]) * vv[2 * t + 1], bflo(a4[t]) * vv[2 * t]); });
        const int dg = 2 * gi + (q >> 1);
        ldsw<u32x4>(CONV + (dg & 1) * CH_SZ + (dg >> 1 ? KB : KA) + 2048 + c16 * 128 + (((7 - 2 * j - (q & 1)) ^ (c16 & 7)) << 4), ov);
      });
    }
    waitl();
    lds_set(CVP + j, 1);
    __builtin_amdgcn_s_setprio(3);
    STAMP(17);
    wait_min4<0>(CVP, 1);
    {
      u32x4 o[2];
      sfor<2>([&](auto t) AI { o[t] = ldsr<u32x4>(kown + 2048 + (8 * t + l8) * 128 + ((c8 ^ l8) << 4)); });
      sfor<2>([&](auto t) AI { __builtin_amdgcn_raw_buffer_store_b128(o[t], rws, W_KV + (64 * pg + 16 * j + 8 * t + l8) * 4096 + (1984 - 64 * pa) * 2 + c8 * 16, 0, 16); });
    }
    waitv<0>();
    if (lds_arrive(CKV, lane) == 4 && lane == 0) __hip_atomic_store((GAS u64*)(ws + W_PF + b * 64), epoch, RLX_AGENT);
    STAMP(3);
    const int mh = j & 1, db = j >> 1, s31 = lane & 31, hh = lane >> 5;
    const int orow = 64 * rt + 32 * mh + s31, ocol = 64 * ct + 32 * db;
    u32x2 xr[4];
    u32 sg = 0;
    if (has) wait_min4<4>(FULL, 8);
    STAMP(24);
    {
      const char* p1 = ws + W_SG + (hh ? orow : rb * 128 + 32 * j + s31) * 8;
      const char* p2 = ws + W_CG + ((has ? cg : 0) * 192 + 48 * j + (lane < 48 ? lane : 0)) * 8;
      const char* p3 = j == 0 ? ws + W_PF + ((b >> 5) * 32 + s31) * 64 : p1;
      u64 g1, g2, f;
      gld8_sc1(g1, p1); gld8_sc1(g2, p2); gld8_sc1(f, p3);
      dma4(cb + (cp ? HB : HA), lane * 16, lane * 16, lane * 16, lane * 16, mk_rsrc(mkp(ka[12], ka[13]), D * S * 2), (2 * b + ch) * 4096);
      if (lane < (cp ? 33 : 16)) ldsw<u32>(cb + (cp ? 4160 : 0) + lane * 4, 0u);
      if (lane < (cp ? 28 : 30)) ldsw<u32x4>(kown + 4096 + lane * 16, u32x4{0u, 0u, 0u, 0u});
      asm volatile("s_waitcnt vmcnt(4)" : "+v"(g1), "+v"(g2), "+v"(f));
      if (j == 0 && __all(f == epoch)) lds_set(CVP + 4, 1);
#pragma unroll 1
      for (u32 it = 0; it < SPIN_CAP; ++it) {
        if (__all((u32)(g1 >> 32) == tag && (u32)(g2 >> 32) == tag)) break;
        __builtin_amdgcn_s_sleep(2);
        g1 = gld_agent(p1); g2 = gld_agent(p2);
      }
      sg = (u32)g1;
      if (has) {
        if (lane < 32) ldsw<f32x2>(ST + (32 * j + lane) * 8, f32x2{bflo(sg), __builtin_amdgcn_rcpf(bfhi(sg))});
        if (lane < 48) ldsw<u32>(CS + (48 * j + lane) * 4, (u32)g2);
      }
      waitl();
      lds_set(STRDY + j, 1);
      STAMP(4);
      sg = (u32)__builtin_amdgcn_permlane32_swap(sg, sg, false, false)[1];
      if (j == 0 && !__all(f == epoch)) { poll_flags<2>(ws, W_PF + (b >> 5) * 32 * 64, 32, epoch, lane); lds_set(CVP + 4, 1); }
      wait_byte<1>(CVP + 4, 1);
      u32x4 kvr[4];
      const char* src = ws + W_KV + (2 * b + ch) * 4096 + 2 * cp;
      sfor<4>([&](auto t) AI { gld4_sc1<t * 1024>(kvr[t], lane * 16, src); });
      wait_min4<1>(FREE, 6);
      sfor<2>([&](auto t) AI {
        const int r = 16 * t + (lane >> 2);
        dma(ZST + j * 2048 + t * 1024, r * 1024 + (((lane & 3) ^ ((r >> 1) & 3)) << 4), rq, (64 * rt + 32 * mh) * 1024 + ocol * 2);
      });
      asm volatile("s_waitcnt vmcnt(0)" : "+v"(kvr[0]), "+v"(kvr[1]), "+v"(kvr[2]), "+v"(kvr[3])::"memory");
      if (cp && lane == 63) kvr[3].w &= 0xffffu;
      sfor<4>([&](auto t) AI { ldsw<u32x4>(kown + t * 1024 + lane * 16, kvr[t]); });
      sfor<4>([&](auto g) AI { xr[g] = ldsr<u32x2>(ZST + j * 2048 + s31 * 64 + ((g ^ ((s31 >> 1) & 3)) << 4) + 8 * hh); });
      waitl();
      pin(xr[0]); pin(xr[1]); pin(xr[2]); pin(xr[3]);
      lds_set(CVP + j, 2);
      STAMP(5);
    }
    wait_min4<2>(FREE, CONVAFTER);
    wait_byte<0>(CVP + (j ^ 2), 2);
    __builtin_amdgcn_s_setprio(0);
    STAMP(6);
    f32x4 dacc[8];
    sfor<8>([&](auto i) AI { dacc[i] = f32x4{0.f, 0.f, 0.f, 0.f}; });
    u32 kv2[8];
    {
      u32 hb = cb + ((c16 & 1) ? HA : HB) + 4 * (c16 - 15) + 16 * q + 256 * cp;
      u32 kb_ = cb + ((c16 & 1) ? KA : KB) + 448 - 64 * (c16 >> 1) + 16 * q + 256 * cp;
      u32 hb2 = hb + 8;
      pin(hb); pin(hb2); pin(kb_);
      bf16x8 ta[12], kf[12];
      auto ld1 = [&](auto odd, auto n, auto w, auto off) AI {
        constexpr int r = (n < 4 && odd) ? 8 + n : n;
        if constexpr (w == 0) kf[r] = ldsr<bf16x8>(kb_ + 512 * (7 - n) + off);
        if constexpr (w == 1) { const u32x2 v = ldsr<u32x2>(hb + 512 * n + off); u32x4 t = __builtin_bit_cast(u32x4, ta[r]); t.x = v.x; t.y = v.y; ta[r] = __builtin_bit_cast(bf16x8, t); }
        if constexpr (w == 2) { const u32x2 v = ldsr<u32x2>(hb2 + 512 * n + off); u32x4 t = __builtin_bit_cast(u32x4, ta[r]); t.z = v.x; t.w = v.y; ta[r] = __builtin_bit_cast(bf16x8, t); }
      };
      auto cstep = [&](auto odd, auto off) AI {
        sfor<36>([&](auto g) AI {
          constexpr int c = cpair(35 - g) >> 3, dl = cpair(35 - g) & 7;
          constexpr int rc = (c < 4 && odd) ? 8 + c : c, rd_ = (dl < 4 && odd) ? 8 + dl : dl;
          dacc[c + dl] = __builtin_amdgcn_mfma_f32_16x16x32_bf16(kf[rc], ta[rd_], dacc[c + dl], 0, 0, 0);
          if constexpr (g < 12) ld1(IC<1 - odd>{}, IC<g / 3>{}, IC<g % 3>{}, off);
          else if constexpr (g < 24) ld1(IC<1 - odd>{}, IC<7 - (g - 12) / 3>{}, IC<g % 3>{}, off);
          GAP();
        });
      };
      sfor<24>([&](auto i) AI { ld1(IC<0>{}, IC<(i < 3 ? 0 : i < 6 ? 7 : i < 9 ? 1 : i < 12 ? 6 : i < 15 ? 2 : i < 18 ? 5 : i < 21 ? 3 : 4)>{}, IC<i % 3>{}, IC<0>{}); GAP(); });
#pragma unroll 1
      for (int i = 0; i < 2; ++i) {
        cstep(IC<0>{}, IC<64>{});
        cstep(IC<1>{}, IC<128>{});
        hb += 128; hb2 += 128; kb_ += 128;
      }
    }
    sfor<8>([&](auto i) AI { kv2[i] = ldsr<u32>(cb + KA + 2 * (2046 - (2 * c16 + 64 * q + 32 * (i & 1) + 256 * (i >> 1) + 1024 * cp))); });
    waitl();
    lds_set(CVP + j, 3);
    __builtin_amdgcn_s_setprio(3);
    STAMP(7);
    {
      const float dv = bflo(dvv);
      sfor<8>([&](auto i) AI { kv2[i] = pk2s(bfhi(kv2[i]) * dv, bflo(kv2[i]) * dv); });
      wait_byte<0>(CVP + (j ^ 2), 3);
      sfor<4>([&](auto i) AI { ldsw<f32x4>(hown + i * 1024 + lane * 16, cp ? dacc[i] : dacc[4 + i]); });
      waitl();
      lds_set(CVP + j, 4);
      wait_byte<0>(CVP + (j ^ 2), 4);
      f32x4 o[4];
      sfor<4>([&](auto i) AI { o[i] = ldsr<f32x4>(cb + (cp ? HA : XB) + i * 1024 + lane * 16); });
      waitl();
      lds_set(CVP + j, 5);
      sfor<4>([&](auto i) AI {
        const f32x4 m = cp ? dacc[4 + i] : dacc[i];
        sfor<2>([&](auto rr) AI {
          const u32 yb = pk2s(m[2 * rr] + o[i][2 * rr], m[2 * rr + 1] + o[i][2 * rr + 1]), kd = kv2[2 * i + rr];
          ldsw<u32>(ZST + j * 2048 + 4 * c16 + 128 * q + 64 * rr + 512 * i, pk2s(bflo(yb) + bflo(kd), bfhi(yb) + bfhi(kd)));
        });
      });
      waitl();
      const u32x4 zo[2] = {ldsr<u32x4>(ZST + j * 2048 + lane * 16), ldsr<u32x4>(ZST + j * 2048 + 1024 + lane * 16)};
      sfor<2>([&](auto t) AI {
        __builtin_amdgcn_raw_buffer_store_b128(zo[t], rws, lane * 16 + W_ZT + (2 * b + ch) * 4096 + cp * 2048 + t * 1024, 0, 16);
      });
      STAMP(16);
      waitv<0>();
      if (lds_arrive(CNT + 4, lane) == 4 && lane == 0) __hip_atomic_store((GAS u64*)(ws + W_ZF + b * 64), epoch, RLX_AGENT);
    }
    STAMP(8);
    const float mean = bflo(sg), rs = __builtin_amdgcn_rcpf(bfhi(sg));
    u32 xn[8];
    sfor<8>([&](auto i) AI {
      const u32 x2 = xr[i >> 1][i & 1], n2 = pk2s(bflo(x2) - mean, bfhi(x2) - mean);
      xn[i] = pk2s(bflo(n2) * rs, bfhi(n2) * rs);
      pin(xn[i]);
    });
    if (b == 0 && j == 3) {
      sfor<4>([&](auto t) AI { poll_flags<1>(ws, W_PF + t * 4096, 64, epoch, lane); });
      if (lane == 0) __hip_atomic_store((GAS u64*)(ws + W_E), epoch, RLX_AGENT);
    }
    if (j == 0) { poll_flags<2>(ws, W_ZF + ct * 32 * 64, 32, epoch, lane); lds_set(CVP + 5, 1); }
    else wait_byte<1>(CVP + 5, 1);
    STAMP(10);
    u32x4 zr[2];
    sfor<2>([&](auto t) AI {
      bld4_sc1<0>(zr[t], (lane >> 2) * 4096 + (lane & 3) * 16, rws, W_ZT + (ocol + 16 * t) * 4096 + (64 * rt + 32 * mh) * 2);
    });
    asm volatile("s_waitcnt vmcnt(0)" : "+v"(zr[0]), "+v"(zr[1])::"memory");
    sfor<2>([&](auto t) AI { ldsw<u32x4>(ZST + j * 2048 + t * 1024 + lane * 16, zr[t]); });
    waitl();
    float tt[16];
    sfor<4>([&](auto g) AI {
      const u32x2 z4 = ldtr(ZST + j * 2048 + (8 * g + 4 * hh + ((lane & 15) >> 2)) * 64 + 32 * ((lane >> 4) & 1) + 8 * (lane & 3));
      sfor<2>([&](auto e) AI {
        const u32 x2 = xr[g][(int)e], z2 = z4[(int)e];
        u32 n2 = pk2s(bflo(xn[2 * g + e]) * bflo(z2), bfhi(xn[2 * g + e]) * bfhi(z2));
        n2 = pk2s(bflo(x2) + bflo(n2), bfhi(x2) + bfhi(n2));
        tt[4 * g + 2 * e] = bflo(n2); tt[4 * g + 2 * e + 1] = bfhi(n2);
      });
    });
    sfor<16>([&](auto i) AI { pin(tt[i]); });
    STAMP(11);
    wait_min4<1>(FREE, 12);
    STAMP(12);
    u32x2 ob[4];
    sfor<4>([&](auto g) AI {
      const f32x4 v = ldsr<f32x4>(PART + mh * 8192 + db * 4096 + g * 1024 + lane * 16) + ldsr<f32x4>(PART + (2 + mh) * 8192 + db * 4096 + g * 1024 + lane * 16);
      ob[g].x = pk2(tt[4 * g] + rs * v.x, tt[4 * g + 1] + rs * v.y);
      ob[g].y = pk2(tt[4 * g + 2] + rs * v.z, tt[4 * g + 3] + rs * v.w);
    });
    sfor<2>([&](auto gp) AI {
      auto s0 = __builtin_amdgcn_permlane32_swap(ob[2 * gp].x, ob[2 * gp + 1].x, false, false);
      auto s1 = __builtin_amdgcn_permlane32_swap(ob[2 * gp].y, ob[2 * gp + 1].y, false, false);
      const u32x4 v = {(u32)s0[0], (u32)s1[0], (u32)s0[1], (u32)s1[1]};
      *(GAS u32x4*)((GAS char*)mkp(kb[2], kb[3]) + orow * 1024 + ocol * 2 + 32 * gp + 16 * hh) = v;
    });
    STAMP(13);
    while (__builtin_amdgcn_s_memrealtime() - tstart_ < 6000) __builtin_amdgcn_s_sleep(1);
    DONE();
  } else if (wave < 4) {
    u32 junk, junk2;
    asm volatile("s_load_dword %0, %2, %3\n\ts_load_dword %1, %2, %4" : "=&s"(junk), "=&s"(junk2)
                 : "s"(pc0 & ~127ull), "s"((u32)(li + 32 * j) * 128u), "s"(min((u32)(li + 32 * (4 + j)) * 128u, CODE_BYTES)));
    const rsrc_t rws = mk_rsrc(ws, W_END);
    const int wm = wave >> 1, wn = wave & 1;
    const int l0 = lane_id();
    u32 wa = G_OFF + ((l0 >> 4) & 1) * (12288 + 32) + (8 * (l0 >> 5) + ((l0 & 15) >> 2)) * 192 + 96 * wn + 8 * (l0 & 3);
    u64 epoch;
    f32x16 acc[2][3];
    sfor<2>([&](auto m) AI { sfor<3>([&](auto n) AI { sfor<16>([&](auto r) AI { acc[m][n][(int)r] = 0.f; }); pin(acc[m][n]); }); });
    u32 xa[4];
    sfor<4>([&](auto ks) AI { xa[ks] = (64 * wm + (l0 & 31)) * 128 + (((2 * ks + (l0 >> 5)) ^ ((l0 >> 1) & 7)) << 4); pin(xa[ks]); });
    asm volatile("s_waitcnt lgkmcnt(0)" : "+s"(junk), "+s"(junk2));
    STAMP(10);
    if (has) {
      bf16x8 xf[2][2], wf[2][3];
      auto ldw = [&](auto st, auto n) AI { const u32 a = wa + (st & 3) * 3072 + n * 32; wf[st & 1][n] = cat(ldtr(a), ldtr(a + 768)); };
      auto ldx = [&](auto st, auto m) AI { xf[st & 1][m] = ldsr<bf16x8>(xa[st & 3] + m * 4096); };
      wait_min4<1>(FULL, 1);
      STAMP(1);
      ldw(IC<0>{}, IC<0>{}); ldx(IC<0>{}, IC<0>{}); ldx(IC<0>{}, IC<1>{}); ldw(IC<0>{}, IC<1>{}); ldw(IC<0>{}, IC<2>{});
      ldw(IC<1>{}, IC<0>{}); ldw(IC<1>{}, IC<1>{}); ldx(IC<1>{}, IC<0>{});
      GAP();
#pragma unroll 1
      for (int c = 0; c < 8; ++c) {
        const u32 adv = (c == 2 || c == 5) ? (u32)(-2 * (int)SLOT) : SLOT;
        const u32 need = c == 7 ? 0u : (u32)(c + 2);
        u32 fl;
        sfor<24>([&](auto gg) AI {
          constexpr int st = gg / 6, g = gg % 6, n = g >> 1, m = g & 1;
          acc[m][n] = __builtin_amdgcn_mfma_f32_32x32x16_bf16(wf[st & 1][n], xf[st & 1][m], acc[m][n], 0, 0, 0);
          if constexpr (g == 0) { ldw(IC<st + 1>{}, IC<2>{}); ldx(IC<st + 1>{}, IC<1>{}); }
          if constexpr (g == 2) ldw(IC<st + 2>{}, IC<0>{});
          if constexpr (g == 4) ldw(IC<st + 2>{}, IC<1>{});
          if constexpr (g == 5) ldx(IC<st + 2>{}, IC<0>{});
          if constexpr (st == 1 && g == 1) fl = lds_ld1(FULL);
          if constexpr (st == 1 && g == 3) { xa[0] += adv; xa[1] += adv; xa[2] += adv; pin(xa[0]); pin(xa[1]); pin(xa[2]); }
          if constexpr (st == 2 && g == 1) {
            xa[3] += adv; wa += adv; pin(xa[3]); pin(wa);
            if (!all_ge(rfl(fl), need)) wait_min4<0>(FULL, need);
          }
          if constexpr (st == 3 && g == 1) lds_set(FREE + wave, c + 1);
          GAP();
        });
      }
      STAMP(2);
      __builtin_amdgcn_s_setprio(2);
      const int lane = lane_id(), s31 = lane & 31, hh = lane >> 5;
      wait_min4<0>(STRDY, 1);
      epoch = wait_epoch();
      STAMP(11);
      f32x2 st[2];
      sfor<2>([&](auto m) AI { st[m] = ldsr<f32x2>(ST + (64 * wm + 32 * m + s31) * 8); });
      f32x4 c4[3][4];
      sfor<12>([&](auto i) AI { c4[i >> 2][i & 3] = ldsr<f32x4>(CS + (wn * 96 + (i >> 2) * 32 + 8 * (i & 3) + 4 * hh) * 4); });
      sfor<3>([&](auto n) AI {
        const int ks = 6 * cg + 3 * wn + n;
        if (ks < 86) {
          f32x2 g[8], u[8], e[8];
          sfor<8>([&](auto i) AI {
            constexpr int m = i >> 2, r = 2 * (i & 3);
            const f32x2 cg2 = {c4[n][r >> 2][r & 3], c4[n][r >> 2][(r & 3) + 1]};
            g[i] = f32x2{acc[m][n][r], acc[m][n][r + 1]} - st[m].x * cg2;
          });
          GAP();
          sfor<8>([&](auto i) AI { g[i] = g[i] * st[i >> 2].y; });
          GAP();
          sfor<8>([&](auto i) AI { e[i] = g[i] * g[i]; });
          GAP();
          sfor<8>([&](auto i) AI { e[i] = e[i] * -0.1029432397f - 2.3022081985f; });
          GAP();
          sfor<8>([&](auto i) AI { e[i] = e[i] * g[i]; });
          GAP();
          sfor<8>([&](auto i) AI {
            constexpr int m = i >> 2, r = 2 * (i & 3);
            const f32x2 cu2 = {c4[n][2 + (r >> 2)][r & 3], c4[n][2 + (r >> 2)][(r & 3) + 1]};
            e[i] = f32x2{__builtin_amdgcn_exp2f(e[i].x), __builtin_amdgcn_exp2f(e[i].y)};
            u[i] = f32x2{acc[m][n][r + 8], acc[m][n][r + 9]} - st[m].x * cu2;
            GAP();
          });
          sfor<8>([&](auto i) AI { e[i] = e[i] + 1.0f; });
          GAP();
          sfor<8>([&](auto i) AI {
            e[i] = f32x2{__builtin_amdgcn_rcpf(e[i].x), __builtin_amdgcn_rcpf(e[i].y)};
            g[i] = g[i] * u[i];
            GAP();
          });
          sfor<8>([&](auto i) AI { g[i] = g[i] * e[i]; });
          GAP();
          sfor<2>([&](auto m) AI {
            const u32x4 o = {pk2(g[4 * m].x, g[4 * m].y), pk2(g[4 * m + 1].x, g[4 * m + 1].y), pk2(g[4 * m + 2].x, g[4 * m + 2].y), pk2(g[4 * m + 3].x, g[4 * m + 3].y)};
            __builtin_amdgcn_raw_buffer_store_b128(o, rws, lane * 16 + W_H + ((4 * rb + 2 * wm + m) * 86 + ks) * 1024, 0, 16);
          });
          GAP();
        }
      });
      lds_set(FREE + wave, 9);
      STAMP(3);
      waitv<0>();
      if (lds_arrive(CNT + 0, lane) == 4 && lane == 0) __hip_atomic_store((GAS u64*)(ws + W_HF + (rb * 16 + cg) * 64), epoch, RLX_AGENT);
      STAMP(4);
    } else {
      if (csw) {
        f32x4 cs[3];
        sfor<3>([&](auto n) AI { cs[n] = f32x4{0.f, 0.f, 0.f, 0.f}; });
        const u32 o2_ = (l0 & 15) == ((l0 >> 4) & 1) ? 0x3f803f80u : 0u;
        const bf16x8 sel = __builtin_bit_cast(bf16x8, u32x4{o2_, o2_, o2_, o2_});
        wa += wm * 6144;
#pragma unroll 1
        for (int c = 0; c < 8; ++c) {
          wait_min4<1>(FULL, c + 1);
          bf16x8 w[6];
          sfor<6>([&](auto i) AI { const u32 a = wa + (i / 3) * 3072 + (i % 3) * 32; w[i] = cat(ldtr(a), ldtr(a + 768)); });
          waitl();
          lds_set(FREE + wave, c + 1);
          sfor<6>([&](auto i) AI { cs[i % 3] = __builtin_amdgcn_mfma_f32_16x16x32_bf16(w[i], sel, cs[i % 3], 0, 0, 0); });
          wa += (c == 2 || c == 5) ? (u32)(-2 * (int)SLOT) : SLOT;
        }
        if ((l0 & 15) < 2) sfor<3>([&](auto n) AI { ldsw<f32x4>(CS + (wm * 192 + wn * 96 + n * 32 + (l0 & 15) * 16 + 4 * (l0 >> 4)) * 4, cs[n]); });
        waitl();
      }
      lds_set(FREE + wave, 9);
      epoch = wait_epoch();
      if (csw && wave < 3) {
        wait_min4<1>(FREE, 9);
        const u32 i = 64 * wave + l0;
        const float v = ldsr<float>(CS + i * 4) + ldsr<float>(CS + 768 + i * 4);
        __hip_atomic_store((GAS u64*)(ws + W_CG + (cg * 192 + i) * 8), ((u64)(u32)(epoch >> 17) << 32) | __float_as_uint(v), RLX_AGENT);
      }
      STAMP(4);
    }
    const int lane = lane_id(), hh = lane >> 5, g16 = lane >> 4, q4 = (lane & 15) >> 2, p4 = lane & 3;
    const int kh = wave >> 1, mh = wave & 1;
    u32x4 hf[43];
    const u32 hso = W_H + ((2 * rt + mh) * 86 + 43 * kh) * 1024, hvo = lane * 16;
    f32x16 o2[2];
    sfor<2>([&](auto n) AI { sfor<16>([&](auto r) AI { o2[n][(int)r] = 0.f; }); pin(o2[n]); });
    u32 db_[2][3];
    sfor<2>([&](auto n) AI {
      const u32 da = kh * 8192 + (4 * hh + q4) * 128 + ((4 * ((int)n ^ (q4 >> 1)) + 2 * (g16 & 1) + (p4 >> 1)) << 4) + 8 * (p4 & 1);
      sfor<3>([&](auto w) AI { db_[n][w] = da + w * 61440; pin(db_[n][w]); });
    });
    u32 gvo = (lane < 43 ? lane : 42) * 1024 + ct * 128;
    pin(gvo);
    u32 junkv = 0;
    bf16x8 df[3][2];
    u32 fl;
    auto ldd = [&](auto k) AI {
      constexpr int c = k >> 2, ks = k & 3;
      constexpr u32 wb = (c < 7 ? 16384u * c : c == 7 ? CONV : c == 8 ? CONV + CH_SZ : 16384u * (c - 9)) + ks * 2048;
      constexpr int w = wb / 61440 > 2 ? 2 : wb / 61440;
      sfor<2>([&](auto n) AI { const u32 a = db_[n][w] + (wb - w * 61440); df[k % 3][n] = cat(ldtr(a), ldtr(a + 1024)); });
    };
    if (wave == 0) { poll_flags<1>(ws, W_HF + (rt >> 1) * 16 * 64, 15, epoch, lane); lds_set(CVP + 6, 1); }
    else wait_byte<0>(CVP + 6, 1);
    STAMP(5);
    asm volatile("buffer_load_dword %0, %1, %2, %3 offen sc1" : "=v"(junkv) : "v"(gvo), "s"(rws), "s"(hso));
    auto hload = [&](auto k) AI { bld4_sc1<(k & 3) * 1024>(hf[k], hvo, rws, hso + (k >> 2) * 4096); };
    sfor<PD>([&](auto k) AI { hload(k); });
    wait_min4<0>(WDRDY, 1);
    STAMP(6);
    ldd(IC<0>{}); ldd(IC<1>{});
    GAP();
    sfor<43>([&](auto k) AI {
      if constexpr (k == 10) lds_set(FREE + wave, 10);
      if constexpr (k == 18) lds_set(FREE + wave, 11);
      if constexpr (k == 24 || k == 32) fl = lds_ld1(WDRDY);
      if constexpr (k == 40) fl = lds_ld1(FREE);
      if constexpr (k == 26 || k == 34) { STAMP(k == 26 ? 12 : 14); if (!all_ge(rfl(fl), k == 26 ? 2u : 3u)) wait_min4<0>(WDRDY, k == 26 ? 2 : 3); STAMP(k == 26 ? 13 : 15); GAP(); }
      constexpr int younger = (k + PD <= 43 ? PD : 43 - k) - 1;
      waitv_pin<younger>(hf[k]);
      if constexpr (k == 0) { pin(junkv); STAMP(24); }
      if constexpr (k == 8) STAMP(25);
      if constexpr (k == 16) STAMP(26);
      o2[0] = __builtin_amdgcn_mfma_f32_32x32x16_bf16(df[k % 3][0], __builtin_bit_cast(bf16x8, hf[k]), o2[0], 0, 0, 0);
      if constexpr (k + 2 < 43) ldd(IC<k + 2>{});
      GAP();
      o2[1] = __builtin_amdgcn_mfma_f32_32x32x16_bf16(df[k % 3][1], __builtin_bit_cast(bf16x8, hf[k]), o2[1], 0, 0, 0);
      if constexpr (k + PD < 43) hload(IC<k + PD>{});
      GAP();
    });
    STAMP(7);
    if (!all_ge(rfl(fl), 11u)) wait_min4<0>(FREE, 11);
    sfor<2>([&](auto n) AI { sfor<4>([&](auto g) AI {
      f32x4 v = {o2[n][4 * g], o2[n][4 * g + 1], o2[n][4 * g + 2], o2[n][4 * g + 3]};
      ldsw<f32x4>(PART + wave * 8192 + n * 4096 + g * 1024 + lane * 16, v);
    }); });
    waitl();
    lds_set(FREE + wave, 12);
    STAMP(8);
    DONE();
  } else {
    __builtin_amdgcn_s_setprio(PRL);
    const rsrc_t rg = mk_rsrc(mkp(ka[2], ka[3]), D * F * 2), ru = mk_rsrc(mkp(ka[4], ka[5]), D * F * 2);
    const int crot = ROT * ((rb + cg) & 1);
    auto chx = [&](int c, u32 sb) AI { dma4(sb + 4 * j * 1024, xv[0], xv[1], xv[2], xv[3], rq, (rb * 128 + 32 * j) * 1024 + (c ^ crot) * 128); };
    auto chw = [&](int c, u32 sb) AI {
      dma3(sb + G_OFF + 3 * j * 1024, wv[0], wv[1], wv[2], rg, (c ^ crot) * 64 * F * 2);
      dma3(sb + U_OFF + 3 * j * 1024, wv[0], wv[1], wv[2], ru, (c ^ crot) * 64 * F * 2);
    };
    if (has) {
      chx(0, 0); chw(0, 0); chx(1, SLOT); chw(1, SLOT);
      waitv<10>(); lds_set(FULL + j, 1);
      STAMP(1);
      u32 sb = 2 * SLOT;
#pragma unroll 1
      for (int c = 2; c < 8; ++c) {
        wait_min4<1>(FREE, c - 2);
        chx(c, sb);
        waitv<4>(); lds_set(FULL + j, c);
        chw(c, sb);
        sb = sb == 2 * SLOT ? 0 : sb + SLOT;
      }
      waitv<0>(); lds_set(FULL + j, 8);
    } else if (csw) {
      chw(0, 0); chw(1, SLOT);
      u32 sb = 2 * SLOT;
#pragma unroll 1
      for (int c = 2; c < 8; ++c) {
        wait_min4<1>(FREE, c - 2);
        chw(c, sb);
        waitv<12>(); lds_set(FULL + j, c - 1);
        sb = sb == 2 * SLOT ? 0 : sb + SLOT;
      }
      waitv<0>(); lds_set(FULL + j, 8);
    }
    STAMP(2);
    const rsrc_t rd = mk_rsrc(mkp(kb[0], kb[1]), F * D * 2);
    u32 dv[4];
    sfor<4>([&](auto t) AI { dv[t] = (7 * t + l8) * 1024 + ((c8 ^ (((l8 >> 1) & 1) << 2)) << 4) + ct * 128; });
    auto wd_groups = [&](int lo, int hi, u32 ldelta) AI {
#pragma unroll 1
      for (int g = lo + j; g < hi; g += 4) dma4(g * 4096 + ldelta, dv[0], dv[1], dv[2], dv[3], rd, (((g >> 1) & 1) * 688 + (g >> 2) * 64 + (g & 1) * 32) * 1024);
    };
    wait_min4<1>(FREE, WD1);
    wd_groups(21, 28, 0);
    wait_min4<1>(FREE, WD2);
    wd_groups(0, 10, 0);
    wait_min4<1>(FREE, 8);
    wd_groups(10, 21, 0);
    u32 junk = 0;
    if (j < 2) gather(junk, ((j ? 688 : 0) + 448 + min(4 * lane + (li & 3), 239)) * 1024 + ct * 128, rd, 0);
    asm volatile("s_waitcnt vmcnt(0)" : "+v"(junk)::"memory");
    lds_set(WDRDY + j, 1);
    STAMP(3);
    wait_min4<2>(CVP, 5);
    wd_groups(28, 32, CONV - 28 * 4096);
    wd_groups(32, 36, CONV + CH_SZ - 32 * 4096);
    waitv<0>(); lds_set(WDRDY + j, 2);
    STAMP(4);
    wait_min4<1>(FREE, 10);
    wd_groups(36, 44, 0u - 36 * 4096);
    waitv<0>(); lds_set(WDRDY + j, 3);
    STAMP(5);
    DONE();
  }
}

extern "C" void kernel_launch(void* const* d_in, const int* in_sizes, int n_in, void* d_out, int out_size, void* d_ws, size_t ws_size,
                              hipStream_t stream) {
  Args a;
  a.q = (const u16*)d_in[0]; a.k = (const u16*)d_in[1]; a.v = (const u16*)d_in[2]; a.sk = (const u16*)d_in[3];
  a.h = (const u16*)d_in[4]; a.dv = (const u16*)d_in[5];
  a.wg = (const u16*)d_in[7]; a.wu = (const u16*)d_in[8]; a.wd = (const u16*)d_in[9];
  a.out = (u16*)d_out; a.ws = (char*)d_ws; a.pad = 0;
  (void)hipFuncSetAttribute((const void*)fused, hipFuncAttributeMaxDynamicSharedMemorySize, LDS_BYTES);
  fused<<<256, 768, LDS_BYTES, stream>>>(a);
}
```

```cpp
#include <hip/hip_runtime.h>
#include <stdint.h>
#include <utility>

typedef unsigned short u16;
typedef unsigned int u32;
typedef unsigned long long u64;
typedef __bf16 bf16x8 __attribute__((ext_vector_type(8)));
typedef __bf16 bf16x2 __attribute__((ext_vector_type(2)));
typedef short s16x4 __attribute__((ext_vector_type(4)));
typedef short s16x8 __attribute__((ext_vector_type(8)));
typedef float f32x16 __attribute__((ext_vector_type(16)));
typedef float f32x4 __attribute__((ext_vector_type(4)));
typedef float f32x2 __attribute__((ext_vector_type(2)));
typedef u32 u32x4 __attribute__((ext_vector_type(4)));
typedef u32 u32x2 __attribute__((ext_vector_type(2)));
typedef __amdgpu_buffer_rsrc_t rsrc_t;
#define DI __device__ __forceinline__
#define AI __attribute__((always_inline))
#define LAS __attribute__((address_space(3)))
#define GAS __attribute__((address_space(1)))
#define RLX_AGENT __ATOMIC_RELAXED, __HIP_MEMORY_SCOPE_AGENT
#define RLX_WG __ATOMIC_RELAXED, __HIP_MEMORY_SCOPE_WORKGROUP

constexpr int S = 2048, D = 512, F = 1376;
#define UPAD 32
#define SLOTB (40960 + UPAD)
constexpr u32 SLOT = SLOTB, G_OFF = 16384, U_OFF = 16384 + 12288 + UPAD;
constexpr u32 CSLOT = 2 * (12288 + UPAD) + 64 - 2 * UPAD % 64;
constexpr u32 CONV = 3 * 41216;
constexpr u32 CH_SZ = 17520, HA = 64, HB = 4292, KA = 8400, KB = 12976, XB = 4304;
constexpr u32 MISC = CONV + 2 * CH_SZ;
constexpr u32 CS = MISC, ST = MISC + 1536, CNT = MISC + 2560;
constexpr u32 CKV = CNT + 8, CSP = CNT + 12, EPO = CNT + 16, CVP = CNT + 24, FULL = CNT + 32, FREE = CNT + 36, STRDY = CNT + 40, WDRDY = CNT + 44;
constexpr u32 LDS_BYTES = MISC + 2816;
static_assert(LDS_BYTES <= 163840, "LDS");
constexpr u32 XCH = 98304, ZST = 114688, PART = 32768;
constexpr u32 W_E = 0, W_PF = 4096, W_HF = W_PF + 16384, W_ZF = W_HF + 16384, W_SG = W_ZF + 16384, W_CG = W_SG + 16384;
constexpr u32 W_KV = 131072, W_ZT = W_KV + 2097152 + 4096, W_H = W_ZT + 4194304, W_END = W_H + 64 * 86 * 1024;
constexpr u32 W_DBG = 16u << 20;
constexpr u32 CODE_BYTES = 26112;
#define PRL 1
#define PRS 0
#define CONVAFTER 9
#define LSLEEP 1
#define PRA 0
#define ROT 0
#define S2AFTER 8
#define WDFL 2
#define KVSTAG 0
#define SSTOP 9
#define POLLS 4
#define PD 10
#define WD1 6
#define WD2 7
#define PFLO 8
#define PFPRIO 0
#define PFWAIT 0
#define KVMODE 0
#define WD3 8
#define PRE 2
#define PRW 0
#define WSL 1
#define PRC 3
#define ZSLEEP 4
#define ZAFTER 11
#define KVAFTER 1
constexpr u32 SPIN_CAP = 1u << 18;

constexpr int cpair(int g) {
  int i = 0;
  for (int n = 0; n < 8; ++n) for (int m = 0; m <= n; ++m) if (m + n <= 7) {
    if (i++ == g) return n * 8 + m;
    if (m < n && i++ == g) return m * 8 + n;
  }
  return 0;
}
template <class Fn, int... I> DI void sfor_impl(Fn&& f, std::integer_sequence<int, I...>) { (f(std::integral_constant<int, I>{}), ...); }
template <int N, class Fn> DI void sfor(Fn&& f) { sfor_impl(f, std::make_integer_sequence<int, N>{}); }

DI rsrc_t mk_rsrc(const void* p, u32 bytes) { return __builtin_amdgcn_make_buffer_rsrc((void*)p, (short)0, (int)bytes, 0x00020000); }
DI void dma(u32 lds, u32 voff, rsrc_t r, u32 soff) {
  asm volatile("s_mov_b32 m0, %0\n\ts_nop 0\n\tbuffer_load_dwordx4 %1, %2, %3 offen lds" ::"s"(lds), "v"(voff), "s"(r), "s"(soff) : "memory");
}

DI void dma4(u32 lds, u32 v0, u32 v1, u32 v2, u32 v3, rsrc_t r, u32 soff) {
  asm volatile("s_mov_b32 m0, %0\n\ts_nop 0\n\tbuffer_load_dwordx4 %1, %5, %6 offen lds\n\tbuffer_load_dwordx4 %2, %5, %6 offen offset:1024 lds\n\t"
               "buffer_load_dwordx4 %3, %5, %6 offen offset:2048 lds\n\tbuffer_load_dwordx4 %4, %5, %6 offen offset:3072 lds"
               ::"s"(lds), "v"(v0), "v"(v1), "v"(v2), "v"(v3), "s"(r), "s"(soff) : "memory");
}
DI void dma3(u32 lds, u32 v0, u32 v1, u32 v2, rsrc_t r, u32 soff) {
  asm volatile("s_mov_b32 m0, %0\n\ts_nop 0\n\tbuffer_load_dwordx4 %1, %4, %5 offen lds\n\tbuffer_load_dwordx4 %2, %4, %5 offen offset:1024 lds\n\t"
               "buffer_load_dwordx4 %3, %4, %5 offen offset:2048 lds"
               ::"s"(lds), "v"(v0), "v"(v1), "v"(v2), "s"(r), "s"(soff) : "memory");
}
template <int OFF> DI void bld4_sc1(u32x4& d, u32 voff, rsrc_t r, u32 soff) {
  asm volatile("buffer_load_dwordx4 %0, %1, %2, %3 offen offset:%4 sc1" : "=v"(d) : "v"(voff), "s"(r), "s"(soff), "i"(OFF));
}
template <int OFF> DI void bld4o(u32x4& d, u32 voff, rsrc_t r, u32 soff) { asm volatile("buffer_load_dwordx4 %0, %1, %2, %3 offen offset:%4" : "=v"(d) : "v"(voff), "s"(r), "s"(soff), "i"(OFF)); }
DI void bld4(u32x4& d, u32 voff, rsrc_t r, u32 soff) { asm volatile("buffer_load_dwordx4 %0, %1, %2, %3 offen" : "=v"(d) : "v"(voff), "s"(r), "s"(soff)); }
template <int OFF> DI void bld2(u32x2& d, u32 voff, rsrc_t r) { asm volatile("buffer_load_dwordx2 %0, %1, %2, 0 offen offset:%3" : "=v"(d) : "v"(voff), "s"(r), "i"(OFF)); }
template <int OFF> DI void gld4_sc1(u32x4& d, u32 voff, const char* base) {
  asm volatile("global_load_dwordx4 %0, %1, %2 offset:%3 sc1" : "=v"(d) : "v"(voff), "s"(base), "i"(OFF));
}
template <int N> DI void waitv_pin(u32x4& d) { asm volatile("s_waitcnt vmcnt(%1)" : "+v"(d) : "i"(N)); }
template <int N> DI void waitv() { asm volatile("s_waitcnt vmcnt(%0)" ::"i"(N) : "memory"); }
DI u32 vm_pending() { u32 r; asm volatile("s_getreg_b32 %0, hwreg(HW_REG_IB_STS)" : "=s"(r)::"memory"); return (r & 15u) | ((r >> 18) & 48u); }
DI void bar() { asm volatile("s_waitcnt lgkmcnt(0)\n\ts_barrier" ::: "memory"); }
DI void waitl() { asm volatile("s_waitcnt lgkmcnt(0)" ::: "memory"); }
DI float bflo(u32 x) { return __uint_as_float(x << 16); }
DI float bfhi(u32 x) { return __uint_as_float(x & 0xffff0000u); }
DI u32 pk2(float lo, float hi) { f32x2 v = {lo, hi}; return __builtin_bit_cast(u32, __builtin_convertvector(v, bf16x2)); }
DI u32 pk2s(float lo, float hi) { asm("" : "+v"(lo)); asm("" : "+v"(hi)); return pk2(lo, hi); }
DI float rbf(float x) { return bflo(pk2(x, x)); }
template <class T> DI T ldsr(u32 a) { return *(const LAS T*)a; }
template <class T> DI void ldsw(u32 a, T v) { *(LAS T*)a = v; }
DI u32x2 ldtr(u32 a) { return __builtin_bit_cast(u32x2, __builtin_amdgcn_ds_read_tr16_b64_v4i16((LAS s16x4*)a)); }
DI bf16x8 cat(u32x2 a, u32x2 b) { u32x4 v = {a.x, a.y, b.x, b.y}; return __builtin_bit_cast(bf16x8, v); }
template <int CTRL, int RM> DI float dpp_add(float v) {
  return v + __builtin_bit_cast(float, __builtin_amdgcn_update_dpp(0, __builtin_bit_cast(int, v), CTRL, RM, 0xf, false));
}
DI float wave_sum(float v) {
  v = dpp_add<0x111, 0xf>(v); v = dpp_add<0x112, 0xf>(v); v = dpp_add<0x114, 0xf>(v); v = dpp_add<0x118, 0xf>(v);
  v = dpp_add<0x142, 0xa>(v); v = dpp_add<0x143, 0xc>(v);
  return __builtin_bit_cast(float, __builtin_amdgcn_readlane(__builtin_bit_cast(int, v), 63));
}
DI void wave_sum2(float& a, float& c) {
  a = dpp_add<0x111, 0xf>(a); c = dpp_add<0x111, 0xf>(c); a = dpp_add<0x112, 0xf>(a); c = dpp_add<0x112, 0xf>(c);
  a = dpp_add<0x114, 0xf>(a); c = dpp_add<0x114, 0xf>(c); a = dpp_add<0x118, 0xf>(a); c = dpp_add<0x118, 0xf>(c);
  a = dpp_add<0x142, 0xa>(a); c = dpp_add<0x142, 0xa>(c); a = dpp_add<0x143, 0xc>(a); c = dpp_add<0x143, 0xc>(c);
  a = __builtin_bit_cast(float, __builtin_amdgcn_readlane(__builtin_bit_cast(int, a), 63));
  c = __builtin_bit_cast(float, __builtin_amdgcn_readlane(__builtin_bit_cast(int, c), 63));
}
DI void gld8_sc1(u64& d, const char* p) { asm volatile("global_load_dwordx2 %0, %1, off sc1" : "=v"(d) : "v"(p)); }
DI void gld_u16(u32& d, u32 voff, const char* base) { asm volatile("global_load_ushort %0, %1, %2" : "=v"(d) : "v"(voff), "s"(base)); }
template <int SLEEP> DI void poll_flags(const char* ws, u32 off, int n, u64 epoch, int lane) {
  const GAS u64* p = (const GAS u64*)(ws + off + (u32)(lane < n ? lane : 0) * 64u);
#pragma unroll 1
  for (u32 it = 0; it < SPIN_CAP; ++it) {
    u64 v = __hip_atomic_load(p, RLX_AGENT);
    if (__all(v == epoch)) break;
    __builtin_amdgcn_s_sleep(SLEEP);
  }
  asm volatile("" ::: "memory");
}
DI u32 poll_granule(const char* ws, u32 row, u32 tag) {
  const GAS u64* p = (const GAS u64*)(ws + W_SG + row * 8u);
  u64 v = 0;
#pragma unroll 1
  for (u32 it = 0; it < SPIN_CAP; ++it) {
    v = __hip_atomic_load(p, RLX_AGENT);
    if (__all((u32)(v >> 32) == tag)) break;
    __builtin_amdgcn_s_sleep(1);
  }
  return (u32)v;
}
DI u32 lds_arrive(u32 a, int lane) {
  u32 r = 0;
  waitl();
  if (lane == 0) r = __hip_atomic_fetch_add((LAS u32*)a, 1u, RLX_WG);
  return (u32)__builtin_amdgcn_readfirstlane(r) + 1u;
}
DI void lds_wait(u32 a, u32 want) {
#pragma unroll 1
  for (u32 it = 0; it < SPIN_CAP * 16; ++it) {
    u32 v = __hip_atomic_load((LAS u32*)a, RLX_WG);
    if ((u32)__builtin_amdgcn_readfirstlane(v) >= want) break;
  }
  asm volatile("" ::: "memory");
}

DI int lane_id() { int l; asm volatile("v_mbcnt_lo_u32_b32 %0, -1, 0\n\tv_mbcnt_hi_u32_b32 %0, -1, %0" : "=v"(l)); return l; }
DI u32 rfl(u32 x) { return (u32)__builtin_amdgcn_readfirstlane(x); }
DI u32x2 lds_ld2(u32 a) { return __builtin_bit_cast(u32x2, __hip_atomic_load((LAS u64*)a, RLX_WG)); }
template <int SLEEP> DI void wait_min2(u32 a, u32 want) {
#pragma unroll 1
  for (u32 it = 0; it < SPIN_CAP * 16; ++it) {
    const u32x2 v = lds_ld2(a);
    if (rfl(min(v.x, v.y)) >= want) break;
    if (SLEEP) __builtin_amdgcn_s_sleep(SLEEP);
  }
  asm volatile("" ::: "memory");
}
DI u32 lds_ld1(u32 a) { return __hip_atomic_load((LAS u32*)a, RLX_WG); }
DI bool all_ge(u32 w, u32 want) { return ((w - want * 0x01010101u) & 0x80808080u) == 0u; }
template <int SLEEP> DI void wait_min4(u32 a, u32 want) {
#pragma unroll 1
  for (u32 it = 0; it < SPIN_CAP * 16; ++it) {
    if (all_ge(rfl(lds_ld1(a)), want)) break;
    if (SLEEP) __builtin_amdgcn_s_sleep(SLEEP);
  }
  asm volatile("" ::: "memory");
}
template <int SLEEP> DI void wait_byte(u32 a, u32 want) {
#pragma unroll 1
  for (u32 it = 0; it < SPIN_CAP * 16; ++it) {
    if (rfl(__hip_atomic_load((LAS unsigned char*)a, RLX_WG)) >= want) break;
    if (SLEEP) __builtin_amdgcn_s_sleep(SLEEP);
  }
  asm volatile("" ::: "memory");
}
DI u64 wait_epoch() {
  u32x2 e;
#pragma unroll 1
  for (u32 it = 0; it < SPIN_CAP * 16; ++it) {
    e = lds_ld2(EPO);
    if (rfl(e.x) != 0u) break;
    __builtin_amdgcn_s_sleep(2);
  }
  return ((u64)rfl(e.y) << 32) | rfl(e.x);
}
DI u64 gld_agent(const char* p) { return __hip_atomic_load((const GAS u64*)p, RLX_AGENT); }
DI void lds_set(u32 a, u32 v) { __hip_atomic_store((LAS unsigned char*)a, (unsigned char)v, RLX_WG); }
DI void gather(u32& junk, u32 voff, rsrc_t r, u32 soff) { asm volatile("buffer_load_dword %0, %1, %2, %3 offen" : "=v"(junk) : "v"(voff), "s"(r), "s"(soff)); }
template <int N> using IC = std::integral_constant<int, N>;

#define DONE() do {} while (0)
#define STAMP(id) do {} while (0)

struct Args { const u16 *q, *wg, *wu, *k, *v, *sk, *h, *dv, *wd; u16* out; char* ws; u64 pad; };
typedef u32 u32x16 __attribute__((ext_vector_type(16)));
typedef u32 u32x8 __attribute__((ext_vector_type(8)));
DI char* mkp(u32 lo, u32 hi) { return (char*)(((u64)hi << 32) | lo); }
#define GAP() __builtin_amdgcn_sched_barrier(0)
DI void barx() { asm volatile("s_barrier" ::: "memory"); }
template <class T> DI void spin(T& x) { asm volatile("" : "+s"(x)); }
template <class T> DI void pin(T& x) { asm volatile("" : "+v"(x)); }

__global__ void __launch_bounds__(768, 3) fused(Args) {
  u64 pc0; asm volatile("s_getpc_b64 %0" : "=s"(pc0));
  u32x16 ka; u32x8 kb;
  asm volatile("s_load_dwordx16 %0, %2, 0x0\n\ts_load_dwordx8 %1, %2, 0x40" : "=&s"(ka), "=&s"(kb) : "s"(__builtin_amdgcn_kernarg_segment_ptr()));
  GAP();
  const int tid = threadIdx.x, lane = tid & 63;
  const int wave = __builtin_amdgcn_readfirstlane(tid >> 6);
  const int b = blockIdx.x, xcd = b & 7, li = b >> 3;
  if (tid < 12) ldsw<u32>(CNT + tid * 4, 0u);
  const bool has = li < 30;
  const int id = 30 * xcd + (has ? li : 0), rr_ = id % 120, rb = 8 * (id / 120) + (rr_ & 7);
  const int cg = has ? rr_ >> 3 : 4 * (xcd & 3) + 2 * (xcd >> 2) + (li - 30);
  const bool csw = !has && cg < 15;
  const int rt = 4 * xcd + (li & 3), ct = li >> 2;
  const int j = wave & 3, l8 = lane >> 3, c8 = lane & 7;
  u32 xv[4], wv[3];
  sfor<4>([&](auto t) AI { xv[t] = (7 * t + l8) * 1024 + ((c8 ^ ((4 * t + (l8 >> 1)) & 7)) << 4); pin(xv[t]); });
  sfor<3>([&](auto t) AI {
    const u32 byte = t * 1024 + lane * 16, row = byte / 192u;
    const u32 seg = byte - row * 192u;
    wv[t] = cg * 192 + seg >= F * 2 ? 0x40000000u : (16 * j + row) * (F * 2) + cg * 192 + seg - t * 1024;
    pin(wv[t]);
  });
  asm volatile("s_waitcnt lgkmcnt(0)\n\ts_barrier" : "+s"(ka), "+s"(kb)::"memory");
  STAMP(9);
  char* const ws = mkp(kb[4], kb[5]);
  const rsrc_t rq = mk_rsrc(mkp(ka[0], ka[1]), S * D * 2);
  if (wave >= 8) {
    __builtin_amdgcn_s_setprio(3);
    u64 epoch;
    asm volatile("s_load_dwordx2 %0, %1, 0x0" : "=s"(epoch) : "s"(ws));
    const int lane = lane_id(), l8 = lane >> 3, c8 = lane & 7;
    const int ch = j & 1, cp = j >> 1, c16 = lane & 15, q = lane >> 4;
    const rsrc_t rws = mk_rsrc(ws, W_END);
    const u32 cb = CONV + ch * CH_SZ, hown = cb + (cp ? XB : HA), kown = cb + (cp ? KB : KA);
    const int srow = 256 * xcd + 8 * li + 2 * j;
    if (SSTOP == 0) wait_min4<8>(FREE, 8);
    u32x4 sx[2];
    sfor<2>([&](auto r) AI { bld4(sx[r], lane * 16, rq, (srow + r) * 1024); });
    STAMP(23);
    const int pa = b & 31, pg = b >> 5, s0 = 64 * pa + 16 * j;
    u32x2 skw[2];
    u32 dvv;
    auto kvload = [&]() AI {
      const rsrc_t rk = mk_rsrc(mkp(ka[6], ka[7]), S * D * 2), rv = mk_rsrc(mkp(ka[8], ka[9]), S * D * 2), rs_ = mk_rsrc(mkp(ka[10], ka[11]), D * 8);
      const u32 kvv = l8 * 1024 + c8 * 16 + pg * 128;
      sfor<3>([&](auto t) AI { const int r = s0 - 8 + 8 * t; dma(hown + t * 1024, kvv, rk, (r < 0 ? 0 : r) * 1024); });
      sfor<2>([&](auto t) AI { dma(kown + t * 1024, kvv, rv, (s0 + 8 * t) * 1024); });
      sfor<2>([&](auto t) AI { bld2<256 * t>(skw[t], (64 * pg + 16 * (q >> 1) + c16) * 8, rs_); });
      gld_u16(dvv, (2 * b + ch) * 2, mkp(ka[14], ka[15]));
    };
    asm volatile("s_waitcnt lgkmcnt(0)" : "+s"(epoch));
    epoch += 0x9E3779B97F4A7C15ull;
    epoch = (epoch ^ (epoch >> 30)) * 0xBF58476D1CE4E5B9ull; epoch = (epoch ^ (epoch >> 27)) * 0x94D049BB133111EBull;
    epoch = (epoch ^ (epoch >> 31)) | 1ull;
    ldsw<u64>(EPO, epoch);
    STAMP(18);
    const u32 tag = (u32)(epoch >> 17);
    if (has) wait_min4<2>(FULL, KVAFTER + KVSTAG * (li & 3));
    STAMP(19);
    kvload();
    STAMP(20);
    asm volatile("s_waitcnt vmcnt(8)" : "+v"(sx[0]), "+v"(sx[1]));
    STAMP(21);
    __builtin_amdgcn_s_setprio(0);
    {
      float x[2][8], mean[2], var[2];
      sfor<2>([&](auto r) AI {
        sfor<4>([&](auto i) AI { x[r][2 * i] = bflo(sx[r][(int)i]); x[r][2 * i + 1] = bfhi(sx[r][(int)i]); });
        mean[r] = x[r][0];
        sfor<7>([&](auto i) AI { mean[r] += x[r][i + 1]; });
      });
      wave_sum2(mean[0], mean[1]);
      sfor<2>([&](auto r) AI {
        mean[r] *= 1.0f / D; var[r] = 0.f;
        sfor<8>([&](auto i) AI { float d = x[r][i] - mean[r]; asm("" : "+v"(d)); var[r] += d * d; });
      });
      wave_sum2(var[0], var[1]);
      STAMP(22);
      const float mn = lane ? mean[1] : mean[0], vr = rbf((lane ? var[1] : var[0]) * (1.0f / D));
      const u32 g = pk2s(mn, __builtin_sqrtf(rbf(vr + 1e-5f)));
      if (lane < 2) __hip_atomic_store((GAS u64*)(ws + W_SG + (srow + lane) * 8), ((u64)tag << 32) | g, RLX_AGENT);
    }
    STAMP(1);
    asm volatile("s_waitcnt vmcnt(1)" : "+v"(skw[0]), "+v"(skw[1]), "+v"(dvv)::"memory");
    STAMP(2);
    {
      const bool first = s0 == 0 && (q & 1) == 0;
      u32x2 kr[2][5];
      sfor<2>([&](auto gi) AI {
        const u32 a = (8 * (q & 1) + (c16 >> 2)) * 128 + 64 * gi + 32 * (q >> 1) + 8 * (lane & 3);
        kr[gi][0] = ldtr(hown + a + 512); kr[gi][1] = ldtr(hown + a + 1024); kr[gi][2] = ldtr(hown + a + 1536); kr[gi][3] = ldtr(kown + a); kr[gi][4] = ldtr(kown + a + 512);
      });
      GAP();
      if (SSTOP == 1) wait_min4<8>(FREE, 8);
      sfor<2>([&](auto gi) AI {
        const float w0 = bflo(skw[gi].x), w1 = bfhi(skw[gi].x), w2 = bflo(skw[gi].y), w3 = bfhi(skw[gi].y);
        const u32x2 k0 = kr[gi][0], k1 = kr[gi][1], k2 = kr[gi][2], v0 = kr[gi][3], v1 = kr[gi][4];
        const float kk[11] = {first ? 0.f : bfhi(k0.x), first ? 0.f : bflo(k0.y), first ? 0.f : bfhi(k0.y), bflo(k1.x), bfhi(k1.x), bflo(k1.y), bfhi(k1.y), bflo(k2.x), bfhi(k2.x), bflo(k2.y), bfhi(k2.y)};
        const float vv[8] = {bflo(v0.x), bfhi(v0.x), bflo(v0.y), bfhi(v0.y), bflo(v1.x), bfhi(v1.x), bflo(v1.y), bfhi(v1.y)};
        u32 a4[4], m4[4];
        sfor<4>([&](auto t) AI { a4[t] = pk2s(w3 * kk[2 * t], w3 * kk[2 * t + 1]); m4[t] = pk2s(w2 * kk[2 * t + 1], w2 * kk[2 * t + 2]); });
        GAP();
        sfor<4>([&](auto t) AI { a4[t] = pk2s(bflo(a4[t]) + bflo(m4[t]), bfhi(a4[t]) + bfhi(m4[t])); m4[t] = pk2s(w1 * kk[2 * t + 2], w1 * kk[2 * t + 3]); });
        GAP();
        sfor<4>([&](auto t) AI { a4[t] = pk2s(bflo(a4[t]) + bflo(m4[t]), bfhi(a4[t]) + bfhi(m4[t])); m4[t] = pk2s(w0 * kk[2 * t + 3], w0 * kk[2 * t + 4]); });
        GAP();
        sfor<4>([&](auto t) AI { a4[t] = pk2s(bflo(a4[t]) + bflo(m4[t]), bfhi(a4[t]) + bfhi(m4[t])); });
        GAP();
        u32x4 ov;
        sfor<4>([&](auto t) AI { ov[3 - t] = pk2s(bfhi(a4[t]) * vv[2 * t + 1], bflo(a4[t]) * vv[2 * t]); });
        const int dg = 2 * gi + (q >> 1);
        ldsw<u32x4>(CONV + (dg & 1) * CH_SZ + (dg >> 1 ? KB : KA) + 2048 + c16 * 128 + (((7 - 2 * j - (q & 1)) ^ (c16 & 7)) << 4), ov);
      });
    }
    waitl();
    lds_set(CVP + j, 1);
    __builtin_amdgcn_s_setprio(3);
    STAMP(17);
    wait_min4<0>(CVP, 1);
    {
      u32x4 o[2];
      sfor<2>([&](auto t) AI { o[t] = ldsr<u32x4>(kown + 2048 + (8 * t + l8) * 128 + ((c8 ^ l8) << 4)); });
      sfor<2>([&](auto t) AI { __builtin_amdgcn_raw_buffer_store_b128(o[t], rws, W_KV + (64 * pg + 16 * j + 8 * t + l8) * 4096 + (1984 - 64 * pa) * 2 + c8 * 16, 0, 16); });
    }
    const int mh = j & 1, db = j >> 1, s31 = lane & 31, hh = lane >> 5;
    const int orow = 64 * rt + 32 * mh + s31, ocol = 64 * ct + 32 * db;
    u32x2 xr[4];
    u32 sg;
    {
      const char* p1 = ws + W_SG + (hh ? orow : rb * 128 + 32 * j + s31) * 8;
      const char* p2 = ws + W_CG + ((has ? cg : 0) * 192 + 48 * j + (lane < 48 ? lane : 0)) * 8;
      u64 g1, g2;
      gld8_sc1(g1, p1); gld8_sc1(g2, p2);
      asm volatile("s_waitcnt vmcnt(0)" : "+v"(g1), "+v"(g2)::"memory");
      if (lds_arrive(CKV, lane) == 4 && lane == 0) __hip_atomic_store((GAS u64*)(ws + W_PF + b * 64), epoch, RLX_AGENT);
      STAMP(3);
#pragma unroll 1
      for (u32 it = 0; it < SPIN_CAP; ++it) {
        if (__all((u32)(g1 >> 32) == tag && (u32)(g2 >> 32) == tag)) break;
        __builtin_amdgcn_s_sleep(4);
        g1 = gld_agent(p1); g2 = gld_agent(p2);
      }
      sg = (u32)g1;
      if (has) {
        if (lane < 32) ldsw<f32x2>(ST + (32 * j + lane) * 8, f32x2{bflo(sg), __builtin_amdgcn_rcpf(bfhi(sg))});
        if (lane < 48) ldsw<u32>(CS + (48 * j + lane) * 4, (u32)g2);
      }
      lds_set(STRDY + j, 1);
      STAMP(4);
    }
    if (has) wait_min4<4>(FULL, S2AFTER);
    STAMP(24);
    {
      u64 f;
      gld8_sc1(f, ws + W_PF + ((b >> 5) * 32 + (j == 0 ? s31 : 0)) * 64);
      dma4(cb + (cp ? HB : HA), lane * 16, lane * 16, lane * 16, lane * 16, mk_rsrc(mkp(ka[12], ka[13]), D * S * 2), (2 * b + ch) * 4096);
      if (lane < (cp ? 33 : 16)) ldsw<u32>(cb + (cp ? 4160 : 0) + lane * 4, 0u);
      if (lane < (cp ? 28 : 30)) ldsw<u32x4>(kown + 4096 + lane * 16, u32x4{0u, 0u, 0u, 0u});
      asm volatile("s_waitcnt vmcnt(4)" : "+v"(f));
      sg = (u32)__builtin_amdgcn_permlane32_swap(sg, sg, false, false)[1];
      if (j == 0) { if (!__all(f == epoch)) poll_flags<2>(ws, W_PF + (b >> 5) * 32 * 64, 32, epoch, lane); lds_set(CVP + 4, 1); }
      wait_byte<1>(CVP + 4, 1);
      u32x4 kvr[4];
      const char* src = ws + W_KV + (2 * b + ch) * 4096 + 2 * cp;
      sfor<4>([&](auto t) AI { gld4_sc1<t * 1024>(kvr[t], lane * 16, src); });
      wait_min4<1>(FREE, 6);
      sfor<2>([&](auto t) AI {
        const int r = 16 * t + (lane >> 2);
        dma(ZST + j * 2048 + t * 1024, r * 1024 + (((lane & 3) ^ ((r >> 1) & 3)) << 4), rq, (64 * rt + 32 * mh) * 1024 + ocol * 2);
      });
      asm volatile("s_waitcnt vmcnt(0)" : "+v"(kvr[0]), "+v"(kvr[1]), "+v"(kvr[2]), "+v"(kvr[3])::"memory");
      if (cp && lane == 63) kvr[3].w &= 0xffffu;
      sfor<4>([&](auto t) AI { ldsw<u32x4>(kown + t * 1024 + lane * 16, kvr[t]); });
      sfor<4>([&](auto g) AI { xr[g] = ldsr<u32x2>(ZST + j * 2048 + s31 * 64 + ((g ^ ((s31 >> 1) & 3)) << 4) + 8 * hh); });
      waitl();
      pin(xr[0]); pin(xr[1]); pin(xr[2]); pin(xr[3]);
      lds_set(CVP + j, 2);
      STAMP(5);
    }
    __builtin_amdgcn_s_setprio(0);
    wait_min4<1>(FREE, CONVAFTER);
    wait_byte<0>(CVP + (j ^ 2), 2);
    __builtin_amdgcn_s_setprio(PRC);
    STAMP(6);
    f32x4 dacc[8];
    sfor<8>([&](auto i) AI { dacc[i] = f32x4{0.f, 0.f, 0.f, 0.f}; });
    u32 kv2[8];
    {
      u32 hb = cb + ((c16 & 1) ? HA : HB) + 4 * (c16 - 15) + 16 * q + 256 * cp;
      u32 kb_ = cb + ((c16 & 1) ? KA : KB) + 448 - 64 * (c16 >> 1) + 16 * q + 256 * cp;
      u32 hb2 = hb + 8;
      pin(hb); pin(hb2); pin(kb_);
      bf16x8 ta[12], kf[12];
      auto ld1 = [&](auto odd, auto n, auto w, auto off) AI {
        constexpr int r = (n < 4 && odd) ? 8 + n : n;
        if constexpr (w == 0) kf[r] = ldsr<bf16x8>(kb_ + 512 * (7 - n) + off);
        if constexpr (w == 1) { const u32x2 v = ldsr<u32x2>(hb + 512 * n + off); u32x4 t = __builtin_bit_cast(u32x4, ta[r]); t.x = v.x; t.y = v.y; ta[r] = __builtin_bit_cast(bf16x8, t); }
        if constexpr (w == 2) { const u32x2 v = ldsr<u32x2>(hb2 + 512 * n + off); u32x4 t = __builtin_bit_cast(u32x4, ta[r]); t.z = v.x; t.w = v.y; ta[r] = __builtin_bit_cast(bf16x8, t); }
      };
      auto cstep = [&](auto odd, auto off) AI {
        sfor<36>([&](auto g) AI {
          constexpr int c = cpair(35 - g) >> 3, dl = cpair(35 - g) & 7;
          constexpr int rc = (c < 4 && odd) ? 8 + c : c, rd_ = (dl < 4 && odd) ? 8 + dl : dl;
          dacc[c + dl] = __builtin_amdgcn_mfma_f32_16x16x32_bf16(kf[rc], ta[rd_], dacc[c + dl], 0, 0, 0);
          if constexpr (g < 12) ld1(IC<1 - odd>{}, IC<g / 3>{}, IC<g % 3>{}, off);
          else if constexpr (g < 24) ld1(IC<1 - odd>{}, IC<7 - (g - 12) / 3>{}, IC<g % 3>{}, off);
          GAP();
        });
      };
      sfor<24>([&](auto i) AI { ld1(IC<0>{}, IC<(i < 3 ? 0 : i < 6 ? 7 : i < 9 ? 1 : i < 12 ? 6 : i < 15 ? 2 : i < 18 ? 5 : i < 21 ? 3 : 4)>{}, IC<i % 3>{}, IC<0>{}); GAP(); });
#pragma unroll 1
      for (int i = 0; i < 2; ++i) {
        cstep(IC<0>{}, IC<64>{});
        cstep(IC<1>{}, IC<128>{});
        hb += 128; hb2 += 128; kb_ += 128;
      }
    }
    __builtin_amdgcn_s_setprio(3);
    STAMP(25);
    sfor<4>([&](auto i) AI { ldsw<f32x4>(XCH + j * 4096 + i * 1024 + lane * 16, cp ? dacc[i] : dacc[4 + i]); });
    lds_set(CVP + j, 4);
    {
      const int l = lane_id();
      u32 a = cb + KA + 2 * (2046 - 2 * (l & 15) - 64 * (l >> 4) - 1024 * cp) - 1600;
      pin(a);
      sfor<8>([&](auto i) AI { kv2[i] = ldsr<u32>(a + 64 * (1 - (i & 1)) + 512 * (3 - (i >> 1))); });
    }
    STAMP(7);
    {
      const float dv = bflo(dvv);
      sfor<8>([&](auto i) AI { kv2[i] = pk2s(bfhi(kv2[i]) * dv, bflo(kv2[i]) * dv); pin(kv2[i]); });
      wait_byte<0>(CVP + (j ^ 2), 4);
      f32x4 o[4];
      sfor<4>([&](auto i) AI { o[i] = ldsr<f32x4>(XCH + (j ^ 2) * 4096 + i * 1024 + lane * 16); });
      asm volatile("" ::: "memory");
      lds_set(CVP + j, 5);
      sfor<4>([&](auto i) AI {
        const f32x4 m = cp ? dacc[4 + i] : dacc[i];
        sfor<2>([&](auto rr) AI {
          const u32 yb = pk2s(m[2 * rr] + o[i][2 * rr], m[2 * rr + 1] + o[i][2 * rr + 1]), kd = kv2[2 * i + rr];
          ldsw<u32>(ZST + j * 2048 + 4 * c16 + 128 * q + 64 * rr + 512 * i, pk2s(bflo(yb) + bflo(kd), bfhi(yb) + bfhi(kd)));
        });
      });
      waitl();
      u32x2 zo[4];
      sfor<4>([&](auto t) AI { zo[t] = ldsr<u32x2>(ZST + j * 2048 + t * 512 + lane * 8); });
      sfor<4>([&](auto t) AI {
        __builtin_amdgcn_raw_buffer_store_b128(u32x4{zo[t].x, tag, zo[t].y, tag}, rws, lane * 16 + W_ZT + (2 * b + ch) * 8192 + cp * 4096 + t * 1024, 0, 16);
      });
    }
    STAMP(8);
    const float mean = bflo(sg), rs = __builtin_amdgcn_rcpf(bfhi(sg));
    u32 xn[8];
    sfor<8>([&](auto i) AI {
      const u32 x2 = xr[i >> 1][i & 1], n2 = pk2s(bflo(x2) - mean, bfhi(x2) - mean);
      xn[i] = pk2s(bflo(n2) * rs, bfhi(n2) * rs);
      pin(xn[i]);
    });
    if (b == 0 && j == 3) {
      sfor<4>([&](auto t) AI { poll_flags<1>(ws, W_PF + t * 4096, 64, epoch, lane); });
      if (lane == 0) __hip_atomic_store((GAS u64*)(ws + W_E), epoch, RLX_AGENT);
    }
    wait_min4<2>(FREE, ZAFTER);
    u32x4 zr[4];
#pragma unroll 1
    for (u32 it = 0; it < SPIN_CAP; ++it) {
      sfor<4>([&](auto t) AI { bld4_sc1<0>(zr[t], l8 * 8192 + c8 * 16, rws, W_ZT + (ocol + 8 * t) * 8192 + (64 * rt + 32 * mh) * 4); });
      asm volatile("s_waitcnt vmcnt(0)" : "+v"(zr[0]), "+v"(zr[1]), "+v"(zr[2]), "+v"(zr[3])::"memory");
      u32 bad = 0;
      sfor<4>([&](auto t) AI { bad |= (zr[t].y ^ tag) | (zr[t].w ^ tag); });
      if (__all(bad == 0u)) break;
      __builtin_amdgcn_s_sleep(ZSLEEP);
    }
    STAMP(10);
    sfor<4>([&](auto t) AI { ldsw<u32x2>(ZST + j * 2048 + t * 512 + lane * 8, u32x2{zr[t].x, zr[t].z}); });
    waitl();
    float tt[16];
    sfor<4>([&](auto g) AI {
      const u32x2 z4 = ldtr(ZST + j * 2048 + (8 * g + 4 * hh + ((lane & 15) >> 2)) * 64 + 32 * ((lane >> 4) & 1) + 8 * (lane & 3));
      sfor<2>([&](auto e) AI {
        const u32 x2 = xr[g][(int)e], z2 = z4[(int)e];
        u32 n2 = pk2s(bflo(xn[2 * g + e]) * bflo(z2), bfhi(xn[2 * g + e]) * bfhi(z2));
        n2 = pk2s(bflo(x2) + bflo(n2), bfhi(x2) + bfhi(n2));
        tt[4 * g + 2 * e] = bflo(n2); tt[4 * g + 2 * e + 1] = bfhi(n2);
      });
    });
    sfor<16>([&](auto i) AI { pin(tt[i]); });
    STAMP(11);
    wait_min4<0>(FREE, 12);
    STAMP(12);
    u32x2 ob[4];
    sfor<4>([&](auto g) AI {
      const f32x4 v = ldsr<f32x4>(PART + mh * 8192 + db * 4096 + g * 1024 + lane * 16) + ldsr<f32x4>(PART + (2 + mh) * 8192 + db * 4096 + g * 1024 + lane * 16);
      ob[g].x = pk2(tt[4 * g] + rs * v.x, tt[4 * g + 1] + rs * v.y);
      ob[g].y = pk2(tt[4 * g + 2] + rs * v.z, tt[4 * g + 3] + rs * v.w);
    });
    sfor<2>([&](auto gp) AI {
      auto s0 = __builtin_amdgcn_permlane32_swap(ob[2 * gp].x, ob[2 * gp + 1].x, false, false);
      auto s1 = __builtin_amdgcn_permlane32_swap(ob[2 * gp].y, ob[2 * gp + 1].y, false, false);
      const u32x4 v = {(u32)s0[0], (u32)s1[0], (u32)s0[1], (u32)s1[1]};
      *(GAS u32x4*)((GAS char*)mkp(kb[2], kb[3]) + orow * 1024 + ocol * 2 + 32 * gp + 16 * hh) = v;
    });
    STAMP(13);
    DONE();
  } else if (wave < 4) {
    u32 junk, junk2;
    asm volatile("s_load_dword %0, %2, %3\n\ts_load_dword %1, %2, %4" : "=&s"(junk), "=&s"(junk2)
                 : "s"(pc0 & ~127ull), "s"((u32)(li + 32 * j) * 128u), "s"(min((u32)(li + 32 * (4 + j)) * 128u, CODE_BYTES)));
    const rsrc_t rws = mk_rsrc(ws, W_END);
    const int wm = wave >> 1, wn = wave & 1;
    const int l0 = lane_id();
    u32 wa = ((l0 >> 4) & 1) * (12288 + UPAD) + (8 * (l0 >> 5) + ((l0 & 15) >> 2)) * 192 + 96 * wn + 8 * (l0 & 3);
    u64 epoch;
    f32x16 acc[2][3];
    sfor<2>([&](auto m) AI { sfor<3>([&](auto n) AI { sfor<16>([&](auto r) AI { acc[m][n][(int)r] = 0.f; }); pin(acc[m][n]); }); });
    u32 xa[4];
    sfor<4>([&](auto ks) AI { xa[ks] = (64 * wm + (l0 & 31)) * 128 + (((2 * ks + (l0 >> 5)) ^ ((l0 >> 1) & 7)) << 4); pin(xa[ks]); });
    asm volatile("s_waitcnt lgkmcnt(0)" : "+s"(junk), "+s"(junk2));
    STAMP(10);
    if (has) {
      bf16x8 xf[2][2], wf[2][3];
      auto ldw = [&](auto st, auto n) AI { const u32 a = wa + G_OFF + (st & 3) * 3072 + n * 32; wf[st & 1][n] = cat(ldtr(a), ldtr(a + 768)); };
      auto ldx = [&](auto st, auto m) AI { xf[st & 1][m] = ldsr<bf16x8>(xa[st & 3] + m * 4096); };
      wait_min4<1>(FULL, 1);
      u32 accw = 0, cntw = 0;
      STAMP(1);
      ldw(IC<0>{}, IC<0>{}); ldx(IC<0>{}, IC<0>{}); ldx(IC<0>{}, IC<1>{}); ldw(IC<0>{}, IC<1>{}); ldw(IC<0>{}, IC<2>{});
      ldw(IC<1>{}, IC<0>{}); ldw(IC<1>{}, IC<1>{}); ldx(IC<1>{}, IC<0>{});
      GAP();
      u32 needw = 0x01010101u, off = 0;
#pragma unroll 1
      for (int c = 0; c < 8; ++c) {
        u32 fl, adv, have;
        sfor<24>([&](auto gg) AI {
          constexpr int st = gg / 6, g = gg % 6, n = g >> 1, m = g & 1;
          acc[m][n] = __builtin_amdgcn_mfma_f32_32x32x16_bf16(wf[st & 1][n], xf[st & 1][m], acc[m][n], 0, 0, 0);
          if constexpr (g == 0) { ldw(IC<st + 1>{}, IC<2>{}); ldx(IC<st + 1>{}, IC<1>{}); }
          if constexpr (g == 2) ldw(IC<st + 2>{}, IC<0>{});
          if constexpr (g == 4) ldw(IC<st + 2>{}, IC<1>{});
          if constexpr (g == 5) ldx(IC<st + 2>{}, IC<0>{});
          if constexpr (st == 0 && g == 3) { const u32 o2 = off == 2 * SLOT ? 0u : off + SLOT; adv = o2 - off; off = o2; needw += 0x01010101u; spin(adv); spin(off); spin(needw); }
          if constexpr (st == 1 && g == 1) fl = lds_ld1(FULL);
          if constexpr (st == 1 && g == 3) { xa[0] += adv; xa[1] += adv; xa[2] += adv; pin(xa[0]); pin(xa[1]); pin(xa[2]); }
          if constexpr (st == 2 && g == 0) { have = (rfl(fl) - needw) & 0x80808080u; spin(have); }
          if constexpr (st == 2 && g == 1) {
            xa[3] += adv; wa += adv; pin(xa[3]); pin(wa);
            if (__builtin_expect(have != 0u, 0)) wait_min4<0>(FULL, c + 2);
          }
          if constexpr (st == 3 && g == 1) lds_set(FREE + wave, c + 1);
          GAP();
        });
      }
      STAMP(2);
      __builtin_amdgcn_s_setprio(PRE);
      const int lane = lane_id(), s31 = lane & 31, hh = lane >> 5;
      f32x2 st[2];
      f32x4 ca[4];
      auto ldc = [&](auto n) AI { sfor<4>([&](auto g) AI { const u32 a = CS + (wn * 96 + n * 32 + 8 * g + 4 * hh) * 4; ca[g] = ldsr<f32x4>(a); }); };
#pragma unroll 1
      for (u32 it = 0; it < SPIN_CAP * 16; ++it) {
        const u32 r1 = lds_ld1(STRDY);
        asm volatile("" ::: "memory");
        const u32x2 e = lds_ld2(EPO);
        sfor<2>([&](auto m) AI { st[m] = ldsr<f32x2>(ST + (64 * wm + 32 * m + s31) * 8); });
        ldc(IC<0>{});
        asm volatile("" ::: "memory");
        epoch = ((u64)rfl(e.y) << 32) | rfl(e.x);
        if (__builtin_expect(all_ge(rfl(r1), 1), 1)) break;
      }
      STAMP(11);
      sfor<3>([&](auto n) AI {
        const int ks = 6 * cg + 3 * wn + n;
        if (ks < 86) {
          f32x4 c4[4];
          sfor<4>([&](auto g) AI { c4[g] = ca[g]; });
          f32x2 g[8], u[8], e[8];
          sfor<8>([&](auto i) AI {
            constexpr int m = i >> 2, r = 2 * (i & 3);
            const f32x2 cg2 = {c4[r >> 2][r & 3], c4[r >> 2][(r & 3) + 1]};
            g[i] = f32x2{acc[m][n][r], acc[m][n][r + 1]} - st[m].x * cg2;
          });
          GAP();
          sfor<8>([&](auto i) AI { g[i] = g[i] * st[i >> 2].y; });
          GAP();
          sfor<8>([&](auto i) AI { e[i] = g[i] * g[i]; });
          GAP();
          sfor<8>([&](auto i) AI { e[i] = e[i] * -0.1029432397f - 2.3022081985f; });
          GAP();
          sfor<8>([&](auto i) AI { e[i] = e[i] * g[i]; });
          GAP();
          sfor<8>([&](auto i) AI {
            constexpr int m = i >> 2, r = 2 * (i & 3);
            const f32x2 cu2 = {c4[2 + (r >> 2)][r & 3], c4[2 + (r >> 2)][(r & 3) + 1]};
            e[i] = f32x2{__builtin_amdgcn_exp2f(e[i].x), __builtin_amdgcn_exp2f(e[i].y)};
            u[i] = f32x2{acc[m][n][r + 8], acc[m][n][r + 9]} - st[m].x * cu2;
            GAP();
          });
          if constexpr (n < 2) ldc(IC<n + 1>{});
          sfor<8>([&](auto i) AI { e[i] = e[i] + 1.0f; });
          GAP();
          sfor<8>([&](auto i) AI {
            e[i] = f32x2{__builtin_amdgcn_rcpf(e[i].x), __builtin_amdgcn_rcpf(e[i].y)};
            g[i] = g[i] * u[i];
            GAP();
          });
          sfor<8>([&](auto i) AI { g[i] = g[i] * e[i]; });
          GAP();
          sfor<2>([&](auto m) AI {
            const u32x4 o = {pk2(g[4 * m].x, g[4 * m].y), pk2(g[4 * m + 1].x, g[4 * m + 1].y), pk2(g[4 * m + 2].x, g[4 * m + 2].y), pk2(g[4 * m + 3].x, g[4 * m + 3].y)};
            __builtin_amdgcn_raw_buffer_store_b128(o, rws, lane * 16 + W_H + ((4 * rb + 2 * wm + m) * 86 + ks) * 1024, 0, 16);
          });
          GAP();
          if constexpr (n < 2) STAMP(20 + n);
        }
      });
      lds_set(FREE + wave, 9);
      STAMP(3);
      waitv<0>();
      __builtin_amdgcn_s_setprio(PRW);
      if (lds_arrive(CNT + 0, lane) == 4 && lane == 0) __hip_atomic_store((GAS u64*)(ws + W_HF + (rb * 16 + cg) * 64), epoch, RLX_AGENT);
      STAMP(4);
    } else {
      if (csw) {
        f32x4 cs[3];
        sfor<3>([&](auto n) AI { cs[n] = f32x4{0.f, 0.f, 0.f, 0.f}; });
        const u32 o2_ = (l0 & 15) == ((l0 >> 4) & 1) ? 0x3f803f80u : 0u;
        const bf16x8 sel = __builtin_bit_cast(bf16x8, u32x4{o2_, o2_, o2_, o2_});
        wa += wm * 6144;
#pragma unroll 1
        for (int c = 0; c < 8; ++c) {
          wait_min4<1>(FULL, c + 1);
          bf16x8 w[6];
          sfor<6>([&](auto i) AI { const u32 a = wa + (i / 3) * 3072 + (i % 3) * 32; w[i] = cat(ldtr(a), ldtr(a + 768)); });
          asm volatile("" ::: "memory");
          lds_set(FREE + wave, c < 3 ? c + 1 : 3);
          sfor<6>([&](auto i) AI { cs[i % 3] = __builtin_amdgcn_mfma_f32_16x16x32_bf16(w[i], sel, cs[i % 3], 0, 0, 0); });
          wa += c == 4 ? (u32)(-4 * (int)CSLOT) : CSLOT;
        }
        if ((l0 & 15) < 2) sfor<3>([&](auto n) AI { ldsw<f32x4>(CS + (wm * 192 + wn * 96 + n * 32 + (l0 & 15) * 16 + 4 * (l0 >> 4)) * 4, cs[n]); });
      }
      lds_set(FREE + wave, 9);
      epoch = wait_epoch();
      if (csw && wave < 3) {
        wait_min4<1>(FREE, 9);
        const u32 i = 64 * wave + l0;
        const float v = ldsr<float>(CS + i * 4) + ldsr<float>(CS + 768 + i * 4);
        __hip_atomic_store((GAS u64*)(ws + W_CG + (cg * 192 + i) * 8), ((u64)(u32)(epoch >> 17) << 32) | __float_as_uint(v), RLX_AGENT);
      }
      STAMP(4);
    }
    const int lane = lane_id(), hh = lane >> 5, g16 = lane >> 4, q4 = (lane & 15) >> 2, p4 = lane & 3;
    const int kh = wave >> 1, mh = wave & 1;
    u32x4 hf[43];
    const u32 hso = W_H + ((2 * rt + mh) * 86 + 43 * kh) * 1024, hvo = lane * 16;
    f32x16 o2[2];
    sfor<2>([&](auto n) AI { sfor<16>([&](auto r) AI { o2[n][(int)r] = 0.f; }); pin(o2[n]); });
    u32 db_[2][3];
    sfor<2>([&](auto n) AI {
      const u32 da = kh * 8192 + (4 * hh + q4) * 128 + ((4 * ((int)n ^ (q4 >> 1)) + 2 * (g16 & 1) + (p4 >> 1)) << 4) + 8 * (p4 & 1);
      sfor<3>([&](auto w) AI { db_[n][w] = da + w * 61440; pin(db_[n][w]); });
    });
    bf16x8 df[3][2];
    u32 fl;
    auto ldd = [&](auto k) AI {
      constexpr int c = k >> 2, ks = k & 3;
      constexpr u32 wb = (c < 7 ? 16384u * c : c == 7 ? CONV : c == 8 ? CONV + CH_SZ : 16384u * (c - 9)) + ks * 2048;
      constexpr int w = wb / 61440 > 2 ? 2 : wb / 61440;
      sfor<2>([&](auto n) AI { const u32 a = db_[n][w] + (wb - w * 61440); df[k % 3][n] = cat(ldtr(a), ldtr(a + 1024)); });
    };
    if (wave == 0) { poll_flags<1>(ws, W_HF + (rt >> 1) * 16 * 64, 15, epoch, lane); lds_set(CVP + 6, 1); }
    else wait_byte<WSL>(CVP + 6, 1);
    __builtin_amdgcn_s_setprio(2);
    STAMP(5);
    auto hload = [&](auto k) AI { bld4_sc1<(k & 3) * 1024>(hf[k], hvo, rws, hso + (k >> 2) * 4096); };
    sfor<PD>([&](auto k) AI { hload(k); });
    wait_min4<0>(WDRDY, 1);
    STAMP(6);
    ldd(IC<0>{}); ldd(IC<1>{});
    GAP();
    sfor<43>([&](auto k) AI {
      if constexpr (k == 10) lds_set(FREE + wave, 10);
      if constexpr (k == 18) lds_set(FREE + wave, 11);
      if constexpr (k == 20 || k == 32) fl = lds_ld1(WDRDY);
      if constexpr (k == 40) fl = lds_ld1(FREE);
      if constexpr (k == 22 || k == 34) { STAMP(k == 22 ? 12 : 14); if (__builtin_expect(!all_ge(rfl(fl), k == 22 ? 2u : 3u), 0)) wait_min4<0>(WDRDY, k == 22 ? 2 : 3); STAMP(k == 22 ? 13 : 15); GAP(); }
      constexpr int younger = (k + PD <= 43 ? PD : 43 - k) - 1;
      waitv_pin<younger>(hf[k]);
      if constexpr (k == 0) STAMP(24);
      if constexpr (k == 8) STAMP(25);
      if constexpr (k == 16) STAMP(26);
      o2[0] = __builtin_amdgcn_mfma_f32_32x32x16_bf16(df[k % 3][0], __builtin_bit_cast(bf16x8, hf[k]), o2[0], 0, 0, 0);
      if constexpr (k + 2 < 43) ldd(IC<k + 2>{});
      GAP();
      o2[1] = __builtin_amdgcn_mfma_f32_32x32x16_bf16(df[k % 3][1], __builtin_bit_cast(bf16x8, hf[k]), o2[1], 0, 0, 0);
      if constexpr (k + PD < 43) hload(IC<k + PD>{});
      GAP();
    });
    STAMP(7);
    if (!all_ge(rfl(fl), 11u)) wait_min4<0>(FREE, 11);
    sfor<2>([&](auto n) AI { sfor<4>([&](auto g) AI {
      f32x4 v = {o2[n][4 * g], o2[n][4 * g + 1], o2[n][4 * g + 2], o2[n][4 * g + 3]};
      ldsw<f32x4>(PART + wave * 8192 + n * 4096 + g * 1024 + lane * 16, v);
    }); });
    waitl();
    lds_set(FREE + wave, 12);
    STAMP(8);
    DONE();
  } else {
    __builtin_amdgcn_s_setprio(PRL);
    const rsrc_t rg = mk_rsrc(mkp(ka[2], ka[3]), D * F * 2), ru = mk_rsrc(mkp(ka[4], ka[5]), D * F * 2);
    const int crot = ROT * ((rb + cg) & 1);
    auto chx = [&](int c, u32 sb) AI { dma4(sb + 4 * j * 1024, xv[0], xv[1], xv[2], xv[3], rq, (rb * 128 + 32 * j) * 1024 + (c ^ crot) * 128); };
    auto chw = [&](int c, u32 sb) AI {
      dma3(sb + G_OFF + 3 * j * 1024, wv[0], wv[1], wv[2], rg, (c ^ crot) * 64 * F * 2);
      dma3(sb + U_OFF + 3 * j * 1024, wv[0], wv[1], wv[2], ru, (c ^ crot) * 64 * F * 2);
    };
    if (has) {
      chx(0, 0); chw(0, 0); chx(1, SLOT); chw(1, SLOT);
      int ic = 2, lc = 0;
      u32 sb = 2 * SLOT;
#pragma unroll 1
      for (u32 it = 0; it < SPIN_CAP * 16; ++it) {
        if (lc < ic && vm_pending() <= 10u * (u32)(ic - lc - 1)) {
          asm volatile("" ::: "memory");
          ++lc;
          lds_set(FULL + j, lc == 8 ? 9 : lc);
          if (lc == 1) STAMP(1);
          if (lc == 8) break;
        } else if (ic < 8 && all_ge(rfl(lds_ld1(FREE)), ic - 2)) {
          asm volatile("" ::: "memory");
          chx(ic, sb);
          if (lc < ic && vm_pending() <= 10u * (u32)(ic - lc - 1) + 4u) {
            lds_set(FULL + j, ++lc);
          }
          chw(ic, sb);
          ++ic;
          sb = sb == 2 * SLOT ? 0 : sb + SLOT;
        } else {
          __builtin_amdgcn_s_sleep(LSLEEP);
        }
      }
      waitv<0>();
    } else if (csw) {
      sfor<8>([&](auto c) AI {
        if constexpr (c >= 5) { waitv<24>(); lds_set(FULL + j, (6u * c - vm_pending()) * 43u >> 8); wait_min4<1>(FREE, c - 4); }
        chw(c, (c % 5) * CSLOT - G_OFF);
        if constexpr (c >= 2) lds_set(FULL + j, (6u * (c + 1) - vm_pending()) * 43u >> 8);
      });
      waitv<12>(); lds_set(FULL + j, 6);
      waitv<6>(); lds_set(FULL + j, 7);
      waitv<0>(); lds_set(FULL + j, 8);
    }
    STAMP(2);
    const rsrc_t rd = mk_rsrc(mkp(kb[0], kb[1]), F * D * 2);
    u32 dv[4];
    sfor<4>([&](auto t) AI { dv[t] = (7 * t + l8) * 1024 + ((c8 ^ (((l8 >> 1) & 1) << 2)) << 4) + ct * 128; });
    auto wso = [&](int g) AI { return (u32)(((g >> 1) & 1) * 688 + (g >> 2) * 64 + (g & 1) * 32) * 1024u; };
    auto wd_groups = [&](int lo, int hi) AI {
#pragma unroll 1
      for (int g = lo + j; g < hi; g += 4) { dma4(g * 4096, dv[0], dv[1], dv[2], dv[3], rd, wso(g)); waitv<WDFL>(); }
    };
    u32x4 wr[5][4];
    auto wst = [&](auto t, u32 a) AI { sfor<4>([&](auto i) AI { pin(wr[t][i]); ldsw<u32x4>(a + j * 4096 + i * 1024 + lane * 16, wr[t][i]); }); };
    sfor<5>([&](auto t) AI { const u32 so = wso(24 + 4 * t + j); sfor<4>([&](auto i) AI { bld4o<i * 1024>(wr[t][i], dv[i], rd, so); }); });
    wait_min4<1>(FREE, WD1);
    wd_groups(21, 24);
    wait_min4<1>(FREE, WD2);
    wd_groups(0, 10);
    wait_min4<1>(FREE, WD3);
    wd_groups(10, 21);
    waitv<0>();
    lds_set(WDRDY + j, 1);
    STAMP(3);
    wait_min4<4>(CVP, 5);
    wst(IC<0>{}, XCH); wst(IC<1>{}, CONV); wst(IC<2>{}, CONV + CH_SZ);
    waitl();
    lds_set(WDRDY + j, 2);
    STAMP(4);
    wait_min4<2>(FREE, 10);
    wst(IC<3>{}, 0); wst(IC<4>{}, 16384);
    waitl();
    lds_set(WDRDY + j, 3);
    STAMP(5);
    DONE();
  }
}

extern "C" void kernel_launch(void* const* d_in, const int* in_sizes, int n_in, void* d_out, int out_size, void* d_ws, size_t ws_size,
                              hipStream_t stream) {
  Args a;
  a.q = (const u16*)d_in[0]; a.k = (const u16*)d_in[1]; a.v = (const u16*)d_in[2]; a.sk = (const u16*)d_in[3];
  a.h = (const u16*)d_in[4]; a.dv = (const u16*)d_in[5];
  a.wg = (const u16*)d_in[7]; a.wu = (const u16*)d_in[8]; a.wd = (const u16*)d_in[9];
  a.out = (u16*)d_out; a.ws = (char*)d_ws; a.pad = 0;
  (void)hipFuncSetAttribute((const void*)fused, hipFuncAttributeMaxDynamicSharedMemorySize, LDS_BYTES);
  fused<<<256, 768, LDS_BYTES, stream>>>(a);
}
```

```cpp
#define AMPID 9
#define AMPWAVE 0
#include <hip/hip_runtime.h>
#include <stdint.h>
#include <utility>

typedef unsigned short u16;
typedef unsigned int u32;
typedef unsigned long long u64;
typedef __bf16 bf16x8 __attribute__((ext_vector_type(8)));
typedef __bf16 bf16x2 __attribute__((ext_vector_type(2)));
typedef short s16x4 __attribute__((ext_vector_type(4)));
typedef short s16x8 __attribute__((ext_vector_type(8)));
typedef float f32x16 __attribute__((ext_vector_type(16)));
typedef float f32x4 __attribute__((ext_vector_type(4)));
typedef float f32x2 __attribute__((ext_vector_type(2)));
typedef u32 u32x4 __attribute__((ext_vector_type(4)));
typedef u32 u32x2 __attribute__((ext_vector_type(2)));
typedef __amdgpu_buffer_rsrc_t rsrc_t;
#define DI __device__ __forceinline__
#define AI __attribute__((always_inline))
#define LAS __attribute__((address_space(3)))
#define GAS __attribute__((address_space(1)))
#define RLX_AGENT __ATOMIC_RELAXED, __HIP_MEMORY_SCOPE_AGENT
#define RLX_WG __ATOMIC_RELAXED, __HIP_MEMORY_SCOPE_WORKGROUP

constexpr int S = 2048, D = 512, F = 1376;
#define UPAD 32
#define SLOTB (40960 + UPAD)
constexpr u32 SLOT = SLOTB, G_OFF = 16384, U_OFF = 16384 + 12288 + UPAD;
constexpr u32 CSLOT = 2 * (12288 + UPAD) + 64 - 2 * UPAD % 64;
constexpr u32 CONV = 3 * 41216;
constexpr u32 CH_SZ = 17520, HA = 64, HB = 4292, KA = 8400, KB = 12976, XB = 4304;
constexpr u32 MISC = CONV + 2 * CH_SZ;
constexpr u32 CS = MISC, ST = MISC + 1536, CNT = MISC + 2560;
constexpr u32 CKV = CNT + 8, CSP = CNT + 12, EPO = CNT + 16, CVP = CNT + 24, FULL = CNT + 32, FREE = CNT + 36, STRDY = CNT + 40, WDRDY = CNT + 44;
constexpr u32 LDS_BYTES = MISC + 2816;
static_assert(LDS_BYTES <= 163840, "LDS");
constexpr u32 XCH = 98304, ZST = 114688, PART = 32768;
constexpr u32 W_E = 0, W_PF = 4096, W_HF = W_PF + 16384, W_ZF = W_HF + 16384, W_SG = W_ZF + 16384, W_CG = W_SG + 16384;
constexpr u32 W_KV = 131072, W_ZT = W_KV + 2097152 + 4096, W_H = W_ZT + 4194304, W_END = W_H + 64 * 86 * 1024;
constexpr u32 W_DBG = 16u << 20;
constexpr u32 CODE_BYTES = 26112;
#define PRL 1
#define PRS 0
#define CONVAFTER 9
#define LSLEEP 1
#define PRA 0
#define ROT 0
#define S2AFTER 8
#define WDFL 2
#define KVSTAG 0
#define SSTOP 9
#define POLLS 4
#define PD 10
#define WD1 6
#define WD2 7
#define PFLO 8
#define PFPRIO 0
#define PFWAIT 0
#define KVMODE 0
#define WD3 8
#define PRE 2
#define PRW 0
#define WSL 1
#define PRC 3
#define ZSLEEP 4
#define ZAFTER 11
#define KVAFTER 1
constexpr u32 SPIN_CAP = 1u << 18;

constexpr int cpair(int g) {
  int i = 0;
  for (int n = 0; n < 8; ++n) for (int m = 0; m <= n; ++m) if (m + n <= 7) {
    if (i++ == g) return n * 8 + m;
    if (m < n && i++ == g) return m * 8 + n;
  }
  return 0;
}
template <class Fn, int... I> DI void sfor_impl(Fn&& f, std::integer_sequence<int, I...>) { (f(std::integral_constant<int, I>{}), ...); }
template <int N, class Fn> DI void sfor(Fn&& f) { sfor_impl(f, std::make_integer_sequence<int, N>{}); }

DI rsrc_t mk_rsrc(const void* p, u32 bytes) { return __builtin_amdgcn_make_buffer_rsrc((void*)p, (short)0, (int)bytes, 0x00020000); }
DI void dma(u32 lds, u32 voff, rsrc_t r, u32 soff) {
  asm volatile("s_mov_b32 m0, %0\n\ts_nop 0\n\tbuffer_load_dwordx4 %1, %2, %3 offen lds" ::"s"(lds), "v"(voff), "s"(r), "s"(soff) : "memory");
}

DI void dma4(u32 lds, u32 v0, u32 v1, u32 v2, u32 v3, rsrc_t r, u32 soff) {
  asm volatile("s_mov_b32 m0, %0\n\ts_nop 0\n\tbuffer_load_dwordx4 %1, %5, %6 offen lds\n\tbuffer_load_dwordx4 %2, %5, %6 offen offset:1024 lds\n\t"
               "buffer_load_dwordx4 %3, %5, %6 offen offset:2048 lds\n\tbuffer_load_dwordx4 %4, %5, %6 offen offset:3072 lds"
               ::"s"(lds), "v"(v0), "v"(v1), "v"(v2), "v"(v3), "s"(r), "s"(soff) : "memory");
}
DI void dma3(u32 lds, u32 v0, u32 v1, u32 v2, rsrc_t r, u32 soff) {
  asm volatile("s_mov_b32 m0, %0\n\ts_nop 0\n\tbuffer_load_dwordx4 %1, %4, %5 offen lds\n\tbuffer_load_dwordx4 %2, %4, %5 offen offset:1024 lds\n\t"
               "buffer_load_dwordx4 %3, %4, %5 offen offset:2048 lds"
               ::"s"(lds), "v"(v0), "v"(v1), "v"(v2), "s"(r), "s"(soff) : "memory");
}
template <int OFF> DI void bld4_sc1(u32x4& d, u32 voff, rsrc_t r, u32 soff) {
  asm volatile("buffer_load_dwordx4 %0, %1, %2, %3 offen offset:%4 sc1" : "=v"(d) : "v"(voff), "s"(r), "s"(soff), "i"(OFF));
}
template <int OFF> DI void bld4o(u32x4& d, u32 voff, rsrc_t r, u32 soff) { asm volatile("buffer_load_dwordx4 %0, %1, %2, %3 offen offset:%4" : "=v"(d) : "v"(voff), "s"(r), "s"(soff), "i"(OFF)); }
DI void bld4(u32x4& d, u32 voff, rsrc_t r, u32 soff) { asm volatile("buffer_load_dwordx4 %0, %1, %2, %3 offen" : "=v"(d) : "v"(voff), "s"(r), "s"(soff)); }
template <int OFF> DI void bld2(u32x2& d, u32 voff, rsrc_t r) { asm volatile("buffer_load_dwordx2 %0, %1, %2, 0 offen offset:%3" : "=v"(d) : "v"(voff), "s"(r), "i"(OFF)); }
template <int OFF> DI void gld4_sc1(u32x4& d, u32 voff, const char* base) {
  asm volatile("global_load_dwordx4 %0, %1, %2 offset:%3 sc1" : "=v"(d) : "v"(voff), "s"(base), "i"(OFF));
}
template <int N> DI void waitv_pin(u32x4& d) { asm volatile("s_waitcnt vmcnt(%1)" : "+v"(d) : "i"(N)); }
template <int N> DI void waitv() { asm volatile("s_waitcnt vmcnt(%0)" ::"i"(N) : "memory"); }
DI u32 vm_pending() { u32 r; asm volatile("s_getreg_b32 %0, hwreg(HW_REG_IB_STS)" : "=s"(r)::"memory"); return (r & 15u) | ((r >> 18) & 48u); }
DI void bar() { asm volatile("s_waitcnt lgkmcnt(0)\n\ts_barrier" ::: "memory"); }
DI void waitl() { asm volatile("s_waitcnt lgkmcnt(0)" ::: "memory"); }
DI float bflo(u32 x) { return __uint_as_float(x << 16); }
DI float bfhi(u32 x) { return __uint_as_float(x & 0xffff0000u); }
DI u32 pk2(float lo, float hi) { f32x2 v = {lo, hi}; return __builtin_bit_cast(u32, __builtin_convertvector(v, bf16x2)); }
DI u32 pk2s(float lo, float hi) { asm("" : "+v"(lo)); asm("" : "+v"(hi)); return pk2(lo, hi); }
DI float rbf(float x) { return bflo(pk2(x, x)); }
template <class T> DI T ldsr(u32 a) { return *(const LAS T*)a; }
template <class T> DI void ldsw(u32 a, T v) { *(LAS T*)a = v; }
DI u32x2 ldtr(u32 a) { return __builtin_bit_cast(u32x2, __builtin_amdgcn_ds_read_tr16_b64_v4i16((LAS s16x4*)a)); }
DI bf16x8 cat(u32x2 a, u32x2 b) { u32x4 v = {a.x, a.y, b.x, b.y}; return __builtin_bit_cast(bf16x8, v); }
template <int CTRL, int RM> DI float dpp_add(float v) {
  return v + __builtin_bit_cast(float, __builtin_amdgcn_update_dpp(0, __builtin_bit_cast(int, v), CTRL, RM, 0xf, false));
}
DI float wave_sum(float v) {
  v = dpp_add<0x111, 0xf>(v); v = dpp_add<0x112, 0xf>(v); v = dpp_add<0x114, 0xf>(v); v = dpp_add<0x118, 0xf>(v);
  v = dpp_add<0x142, 0xa>(v); v = dpp_add<0x143, 0xc>(v);
  return __builtin_bit_cast(float, __builtin_amdgcn_readlane(__builtin_bit_cast(int, v), 63));
}
DI void wave_sum2(float& a, float& c) {
  a = dpp_add<0x111, 0xf>(a); c = dpp_add<0x111, 0xf>(c); a = dpp_add<0x112, 0xf>(a); c = dpp_add<0x112, 0xf>(c);
  a = dpp_add<0x114, 0xf>(a); c = dpp_add<0x114, 0xf>(c); a = dpp_add<0x118, 0xf>(a); c = dpp_add<0x118, 0xf>(c);
  a = dpp_add<0x142, 0xa>(a); c = dpp_add<0x142, 0xa>(c); a = dpp_add<0x143, 0xc>(a); c = dpp_add<0x143, 0xc>(c);
  a = __builtin_bit_cast(float, __builtin_amdgcn_readlane(__builtin_bit_cast(int, a), 63));
  c = __builtin_bit_cast(float, __builtin_amdgcn_readlane(__builtin_bit_cast(int, c), 63));
}
DI void gld8_sc1(u64& d, const char* p) { asm volatile("global_load_dwordx2 %0, %1, off sc1" : "=v"(d) : "v"(p)); }
DI void gld_u16(u32& d, u32 voff, const char* base) { asm volatile("global_load_ushort %0, %1, %2" : "=v"(d) : "v"(voff), "s"(base)); }
template <int SLEEP> DI void poll_flags(const char* ws, u32 off, int n, u64 epoch, int lane) {
  const GAS u64* p = (const GAS u64*)(ws + off + (u32)(lane < n ? lane : 0) * 64u);
#pragma unroll 1
  for (u32 it = 0; it < SPIN_CAP; ++it) {
    u64 v = __hip_atomic_load(p, RLX_AGENT);
    if (__all(v == epoch)) break;
    __builtin_amdgcn_s_sleep(SLEEP);
  }
  asm volatile("" ::: "memory");
}
DI u32 poll_granule(const char* ws, u32 row, u32 tag) {
  const GAS u64* p = (const GAS u64*)(ws + W_SG + row * 8u);
  u64 v = 0;
#pragma unroll 1
  for (u32 it = 0; it < SPIN_CAP; ++it) {
    v = __hip_atomic_load(p, RLX_AGENT);
    if (__all((u32)(v >> 32) == tag)) break;
    __builtin_amdgcn_s_sleep(1);
  }
  return (u32)v;
}
DI u32 lds_arrive(u32 a, int lane) {
  u32 r = 0;
  waitl();
  if (lane == 0) r = __hip_atomic_fetch_add((LAS u32*)a, 1u, RLX_WG);
  return (u32)__builtin_amdgcn_readfirstlane(r) + 1u;
}
DI void lds_wait(u32 a, u32 want) {
#pragma unroll 1
  for (u32 it = 0; it < SPIN_CAP * 16; ++it) {
    u32 v = __hip_atomic_load((LAS u32*)a, RLX_WG);
    if ((u32)__builtin_amdgcn_readfirstlane(v) >= want) break;
  }
  asm volatile("" ::: "memory");
}

DI int lane_id() { int l; asm volatile("v_mbcnt_lo_u32_b32 %0, -1, 0\n\tv_mbcnt_hi_u32_b32 %0, -1, %0" : "=v"(l)); return l; }
DI u32 rfl(u32 x) { return (u32)__builtin_amdgcn_readfirstlane(x); }
DI u32x2 lds_ld2(u32 a) { return __builtin_bit_cast(u32x2, __hip_atomic_load((LAS u64*)a, RLX_WG)); }
template <int SLEEP> DI void wait_min2(u32 a, u32 want) {
#pragma unroll 1
  for (u32 it = 0; it < SPIN_CAP * 16; ++it) {
    const u32x2 v = lds_ld2(a);
    if (rfl(min(v.x, v.y)) >= want) break;
    if (SLEEP) __builtin_amdgcn_s_sleep(SLEEP);
  }
  asm volatile("" ::: "memory");
}
DI u32 lds_ld1(u32 a) { return __hip_atomic_load((LAS u32*)a, RLX_WG); }
DI bool all_ge(u32 w, u32 want) { return ((w - want * 0x01010101u) & 0x80808080u) == 0u; }
template <int SLEEP> DI void wait_min4(u32 a, u32 want) {
#pragma unroll 1
  for (u32 it = 0; it < SPIN_CAP * 16; ++it) {
    if (all_ge(rfl(lds_ld1(a)), want)) break;
    if (SLEEP) __builtin_amdgcn_s_sleep(SLEEP);
  }
  asm volatile("" ::: "memory");
}
template <int SLEEP> DI void wait_byte(u32 a, u32 want) {
#pragma unroll 1
  for (u32 it = 0; it < SPIN_CAP * 16; ++it) {
    if (rfl(__hip_atomic_load((LAS unsigned char*)a, RLX_WG)) >= want) break;
    if (SLEEP) __builtin_amdgcn_s_sleep(SLEEP);
  }
  asm volatile("" ::: "memory");
}
DI u64 wait_epoch() {
  u32x2 e;
#pragma unroll 1
  for (u32 it = 0; it < SPIN_CAP * 16; ++it) {
    e = lds_ld2(EPO);
    if (rfl(e.x) != 0u) break;
    __builtin_amdgcn_s_sleep(2);
  }
  return ((u64)rfl(e.y) << 32) | rfl(e.x);
}
DI u64 gld_agent(const char* p) { return __hip_atomic_load((const GAS u64*)p, RLX_AGENT); }
DI void lds_set(u32 a, u32 v) { __hip_atomic_store((LAS unsigned char*)a, (unsigned char)v, RLX_WG); }
DI void gather(u32& junk, u32 voff, rsrc_t r, u32 soff) { asm volatile("buffer_load_dword %0, %1, %2, %3 offen" : "=v"(junk) : "v"(voff), "s"(r), "s"(soff)); }
template <int N> using IC = std::integral_constant<int, N>;

#define DONE() do { if (wave >= 8) { const u64 d_ = rfl(ldsr<u32>(CNT + 60)); while (__builtin_amdgcn_s_memrealtime() - tstart_ < 40 * d_) __builtin_amdgcn_s_sleep(1); } } while (0)
#define STAMP(id) do { if ((id) == AMPID && wave == AMPWAVE) ldsw<u32>(CNT + 60, (u32)(__builtin_amdgcn_s_memrealtime() - tstart_)); } while (0)

struct Args { const u16 *q, *wg, *wu, *k, *v, *sk, *h, *dv, *wd; u16* out; char* ws; u64 pad; };
typedef u32 u32x16 __attribute__((ext_vector_type(16)));
typedef u32 u32x8 __attribute__((ext_vector_type(8)));
DI char* mkp(u32 lo, u32 hi) { return (char*)(((u64)hi << 32) | lo); }
#define GAP() __builtin_amdgcn_sched_barrier(0)
DI void barx() { asm volatile("s_barrier" ::: "memory"); }
template <class T> DI void spin(T& x) { asm volatile("" : "+s"(x)); }
template <class T> DI void pin(T& x) { asm volatile("" : "+v"(x)); }

__global__ void __launch_bounds__(768, 3) fused(Args) {
  u64 pc0; asm volatile("s_getpc_b64 %0" : "=s"(pc0));
  const u64 tstart_ = __builtin_amdgcn_s_memrealtime();
  asm volatile("s_waitcnt lgkmcnt(0)");
  u32x16 ka; u32x8 kb;
  asm volatile("s_load_dwordx16 %0, %2, 0x0\n\ts_load_dwordx8 %1, %2, 0x40" : "=&s"(ka), "=&s"(kb) : "s"(__builtin_amdgcn_kernarg_segment_ptr()));
  GAP();
  const int tid = threadIdx.x, lane = tid & 63;
  const int wave = __builtin_amdgcn_readfirstlane(tid >> 6);
  const int b = blockIdx.x, xcd = b & 7, li = b >> 3;
  if (tid < 16) ldsw<u32>(CNT + tid * 4, 0u);
  const bool has = li < 30;
  const int id = 30 * xcd + (has ? li : 0), rr_ = id % 120, rb = 8 * (id / 120) + (rr_ & 7);
  const int cg = has ? rr_ >> 3 : 4 * (xcd & 3) + 2 * (xcd >> 2) + (li - 30);
  const bool csw = !has && cg < 15;
  const int rt = 4 * xcd + (li & 3), ct = li >> 2;
  const int j = wave & 3, l8 = lane >> 3, c8 = lane & 7;
  u32 xv[4], wv[3];
  sfor<4>([&](auto t) AI { xv[t] = (7 * t + l8) * 1024 + ((c8 ^ ((4 * t + (l8 >> 1)) & 7)) << 4); pin(xv[t]); });
  sfor<3>([&](auto t) AI {
    const u32 byte = t * 1024 + lane * 16, row = byte / 192u;
    const u32 seg = byte - row * 192u;
    wv[t] = cg * 192 + seg >= F * 2 ? 0x40000000u : (16 * j + row) * (F * 2) + cg * 192 + seg - t * 1024;
    pin(wv[t]);
  });
  asm volatile("s_waitcnt lgkmcnt(0)\n\ts_barrier" : "+s"(ka), "+s"(kb)::"memory");
  STAMP(9);
  char* const ws = mkp(kb[4], kb[5]);
  const rsrc_t rq = mk_rsrc(mkp(ka[0], ka[1]), S * D * 2);
  if (wave >= 8) {
    __builtin_amdgcn_s_setprio(3);
    u64 epoch;
    asm volatile("s_load_dwordx2 %0, %1, 0x0" : "=s"(epoch) : "s"(ws));
    const int lane = lane_id(), l8 = lane >> 3, c8 = lane & 7;
    const int ch = j & 1, cp = j >> 1, c16 = lane & 15, q = lane >> 4;
    const rsrc_t rws = mk_rsrc(ws, W_END);
    const u32 cb = CONV + ch * CH_SZ, hown = cb + (cp ? XB : HA), kown = cb + (cp ? KB : KA);
    const int srow = 256 * xcd + 8 * li + 2 * j;
    if (SSTOP == 0) wait_min4<8>(FREE, 8);
    u32x4 sx[2];
    sfor<2>([&](auto r) AI { bld4(sx[r], lane * 16, rq, (srow + r) * 1024); });
    STAMP(23);
    const int pa = b & 31, pg = b >> 5, s0 = 64 * pa + 16 * j;
    u32x2 skw[2];
    u32 dvv;
    auto kvload = [&]() AI {
      const rsrc_t rk = mk_rsrc(mkp(ka[6], ka[7]), S * D * 2), rv = mk_rsrc(mkp(ka[8], ka[9]), S * D * 2), rs_ = mk_rsrc(mkp(ka[10], ka[11]), D * 8);
      const u32 kvv = l8 * 1024 + c8 * 16 + pg * 128;
      sfor<3>([&](auto t) AI { const int r = s0 - 8 + 8 * t; dma(hown + t * 1024, kvv, rk, (r < 0 ? 0 : r) * 1024); });
      sfor<2>([&](auto t) AI { dma(kown + t * 1024, kvv, rv, (s0 + 8 * t) * 1024); });
      sfor<2>([&](auto t) AI { bld2<256 * t>(skw[t], (64 * pg + 16 * (q >> 1) + c16) * 8, rs_); });
      gld_u16(dvv, (2 * b + ch) * 2, mkp(ka[14], ka[15]));
    };
    asm volatile("s_waitcnt lgkmcnt(0)" : "+s"(epoch));
    epoch += 0x9E3779B97F4A7C15ull;
    epoch = (epoch ^ (epoch >> 30)) * 0xBF58476D1CE4E5B9ull; epoch = (epoch ^ (epoch >> 27)) * 0x94D049BB133111EBull;
    epoch = (epoch ^ (epoch >> 31)) | 1ull;
    ldsw<u64>(EPO, epoch);
    STAMP(18);
    const u32 tag = (u32)(epoch >> 17);
    if (has) wait_min4<2>(FULL, KVAFTER + KVSTAG * (li & 3));
    STAMP(19);
    kvload();
    STAMP(20);
    asm volatile("s_waitcnt vmcnt(8)" : "+v"(sx[0]), "+v"(sx[1]));
    STAMP(21);
    __builtin_amdgcn_s_setprio(0);
    {
      float x[2][8], mean[2], var[2];
      sfor<2>([&](auto r) AI {
        sfor<4>([&](auto i) AI { x[r][2 * i] = bflo(sx[r][(int)i]); x[r][2 * i + 1] = bfhi(sx[r][(int)i]); });
        mean[r] = x[r][0];
        sfor<7>([&](auto i) AI { mean[r] += x[r][i + 1]; });
      });
      wave_sum2(mean[0], mean[1]);
      sfor<2>([&](auto r) AI {
        mean[r] *= 1.0f / D; var[r] = 0.f;
        sfor<8>([&](auto i) AI { float d = x[r][i] - mean[r]; asm("" : "+v"(d)); var[r] += d * d; });
      });
      wave_sum2(var[0], var[1]);
      STAMP(22);
      const float mn = lane ? mean[1] : mean[0], vr = rbf((lane ? var[1] : var[0]) * (1.0f / D));
      const u32 g = pk2s(mn, __builtin_sqrtf(rbf(vr + 1e-5f)));
      if (lane < 2) __hip_atomic_store((GAS u64*)(ws + W_SG + (srow + lane) * 8), ((u64)tag << 32) | g, RLX_AGENT);
    }
    STAMP(1);
    asm volatile("s_waitcnt vmcnt(1)" : "+v"(skw[0]), "+v"(skw[1]), "+v"(dvv)::"memory");
    STAMP(2);
    {
      const bool first = s0 == 0 && (q & 1) == 0;
      u32x2 kr[2][5];
      sfor<2>([&](auto gi) AI {
        const u32 a = (8 * (q & 1) + (c16 >> 2)) * 128 + 64 * gi + 32 * (q >> 1) + 8 * (lane & 3);
        kr[gi][0] = ldtr(hown + a + 512); kr[gi][1] = ldtr(hown + a + 1024); kr[gi][2] = ldtr(hown + a + 1536); kr[gi][3] = ldtr(kown + a); kr[gi][4] = ldtr(kown + a + 512);
      });
      GAP();
      if (SSTOP == 1) wait_min4<8>(FREE, 8);
      sfor<2>([&](auto gi) AI {
        const float w0 = bflo(skw[gi].x), w1 = bfhi(skw[gi].x), w2 = bflo(skw[gi].y), w3 = bfhi(skw[gi].y);
        const u32x2 k0 = kr[gi][0], k1 = kr[gi][1], k2 = kr[gi][2], v0 = kr[gi][3], v1 = kr[gi][4];
        const float kk[11] = {first ? 0.f : bfhi(k0.x), first ? 0.f : bflo(k0.y), first ? 0.f : bfhi(k0.y), bflo(k1.x), bfhi(k1.x), bflo(k1.y), bfhi(k1.y), bflo(k2.x), bfhi(k2.x), bflo(k2.y), bfhi(k2.y)};
        const float vv[8] = {bflo(v0.x), bfhi(v0.x), bflo(v0.y), bfhi(v0.y), bflo(v1.x), bfhi(v1.x), bflo(v1.y), bfhi(v1.y)};
        u32 a4[4], m4[4];
        sfor<4>([&](auto t) AI { a4[t] = pk2s(w3 * kk[2 * t], w3 * kk[2 * t + 1]); m4[t] = pk2s(w2 * kk[2 * t + 1], w2 * kk[2 * t + 2]); });
        GAP();
        sfor<4>([&](auto t) AI { a4[t] = pk2s(bflo(a4[t]) + bflo(m4[t]), bfhi(a4[t]) + bfhi(m4[t])); m4[t] = pk2s(w1 * kk[2 * t + 2], w1 * kk[2 * t + 3]); });
        GAP();
        sfor<4>([&](auto t) AI { a4[t] = pk2s(bflo(a4[t]) + bflo(m4[t]), bfhi(a4[t]) + bfhi(m4[t])); m4[t] = pk2s(w0 * kk[2 * t + 3], w0 * kk[2 * t + 4]); });
        GAP();
        sfor<4>([&](auto t) AI { a4[t] = pk2s(bflo(a4[t]) + bflo(m4[t]), bfhi(a4[t]) + bfhi(m4[t])); });
        GAP();
        u32x4 ov;
        sfor<4>([&](auto t) AI { ov[3 - t] = pk2s(bfhi(a4[t]) * vv[2 * t + 1], bflo(a4[t]) * vv[2 * t]); });
        const int dg = 2 * gi + (q >> 1);
        ldsw<u32x4>(CONV + (dg & 1) * CH_SZ + (dg >> 1 ? KB : KA) + 2048 + c16 * 128 + (((7 - 2 * j - (q & 1)) ^ (c16 & 7)) << 4), ov);
      });
    }
    waitl();
    lds_set(CVP + j, 1);
    __builtin_amdgcn_s_setprio(3);
    STAMP(17);
    wait_min4<0>(CVP, 1);
    {
      u32x4 o[2];
      sfor<2>([&](auto t) AI { o[t] = ldsr<u32x4>(kown + 2048 + (8 * t + l8) * 128 + ((c8 ^ l8) << 4)); });
      sfor<2>([&](auto t) AI { __builtin_amdgcn_raw_buffer_store_b128(o[t], rws, W_KV + (64 * pg + 16 * j + 8 * t + l8) * 4096 + (1984 - 64 * pa) * 2 + c8 * 16, 0, 16); });
    }
    const int mh = j & 1, db = j >> 1, s31 = lane & 31, hh = lane >> 5;
    const int orow = 64 * rt + 32 * mh + s31, ocol = 64 * ct + 32 * db;
    u32x2 xr[4];
    u32 sg;
    {
      const char* p1 = ws + W_SG + (hh ? orow : rb * 128 + 32 * j + s31) * 8;
      const char* p2 = ws + W_CG + ((has ? cg : 0) * 192 + 48 * j + (lane < 48 ? lane : 0)) * 8;
      u64 g1, g2;
      gld8_sc1(g1, p1); gld8_sc1(g2, p2);
      asm volatile("s_waitcnt vmcnt(0)" : "+v"(g1), "+v"(g2)::"memory");
      if (lds_arrive(CKV, lane) == 4 && lane == 0) __hip_atomic_store((GAS u64*)(ws + W_PF + b * 64), epoch, RLX_AGENT);
      STAMP(3);
#pragma unroll 1
      for (u32 it = 0; it < SPIN_CAP; ++it) {
        if (__all((u32)(g1 >> 32) == tag && (u32)(g2 >> 32) == tag)) break;
        __builtin_amdgcn_s_sleep(4);
        g1 = gld_agent(p1); g2 = gld_agent(p2);
      }
      sg = (u32)g1;
      if (has) {
        if (lane < 32) ldsw<f32x2>(ST + (32 * j + lane) * 8, f32x2{bflo(sg), __builtin_amdgcn_rcpf(bfhi(sg))});
        if (lane < 48) ldsw<u32>(CS + (48 * j + lane) * 4, (u32)g2);
      }
      lds_set(STRDY + j, 1);
      STAMP(4);
    }
    if (has) wait_min4<4>(FULL, S2AFTER);
    STAMP(24);
    {
      u64 f;
      gld8_sc1(f, ws + W_PF + ((b >> 5) * 32 + (j == 0 ? s31 : 0)) * 64);
      dma4(cb + (cp ? HB : HA), lane * 16, lane * 16, lane * 16, lane * 16, mk_rsrc(mkp(ka[12], ka[13]), D * S * 2), (2 * b + ch) * 4096);
      if (lane < (cp ? 33 : 16)) ldsw<u32>(cb + (cp ? 4160 : 0) + lane * 4, 0u);
      if (lane < (cp ? 28 : 30)) ldsw<u32x4>(kown + 4096 + lane * 16, u32x4{0u, 0u, 0u, 0u});
      asm volatile("s_waitcnt vmcnt(4)" : "+v"(f));
      sg = (u32)__builtin_amdgcn_permlane32_swap(sg, sg, false, false)[1];
      if (j == 0) { if (!__all(f == epoch)) poll_flags<2>(ws, W_PF + (b >> 5) * 32 * 64, 32, epoch, lane); lds_set(CVP + 4, 1); }
      wait_byte<1>(CVP + 4, 1);
      u32x4 kvr[4];
      const char* src = ws + W_KV + (2 * b + ch) * 4096 + 2 * cp;
      sfor<4>([&](auto t) AI { gld4_sc1<t * 1024>(kvr[t], lane * 16, src); });
      wait_min4<1>(FREE, 6);
      sfor<2>([&](auto t) AI {
        const int r = 16 * t + (lane >> 2);
        dma(ZST + j * 2048 + t * 1024, r * 1024 + (((lane & 3) ^ ((r >> 1) & 3)) << 4), rq, (64 * rt + 32 * mh) * 1024 + ocol * 2);
      });
      asm volatile("s_waitcnt vmcnt(0)" : "+v"(kvr[0]), "+v"(kvr[1]), "+v"(kvr[2]), "+v"(kvr[3])::"memory");
      if (cp && lane == 63) kvr[3].w &= 0xffffu;
      sfor<4>([&](auto t) AI { ldsw<u32x4>(kown + t * 1024 + lane * 16, kvr[t]); });
      sfor<4>([&](auto g) AI { xr[g] = ldsr<u32x2>(ZST + j * 2048 + s31 * 64 + ((g ^ ((s31 >> 1) & 3)) << 4) + 8 * hh); });
      waitl();
      pin(xr[0]); pin(xr[1]); pin(xr[2]); pin(xr[3]);
      lds_set(CVP + j, 2);
      STAMP(5);
    }
    __builtin_amdgcn_s_setprio(0);
    wait_min4<1>(FREE, CONVAFTER);
    wait_byte<0>(CVP + (j ^ 2), 2);
    __builtin_amdgcn_s_setprio(PRC);
    STAMP(6);
    f32x4 dacc[8];
    sfor<8>([&](auto i) AI { dacc[i] = f32x4{0.f, 0.f, 0.f, 0.f}; });
    u32 kv2[8];
    {
      u32 hb = cb + ((c16 & 1) ? HA : HB) + 4 * (c16 - 15) + 16 * q + 256 * cp;
      u32 kb_ = cb + ((c16 & 1) ? KA : KB) + 448 - 64 * (c16 >> 1) + 16 * q + 256 * cp;
      u32 hb2 = hb + 8;
      pin(hb); pin(hb2); pin(kb_);
      bf16x8 ta[12], kf[12];
      auto ld1 = [&](auto odd, auto n, auto w, auto off) AI {
        constexpr int r = (n < 4 && odd) ? 8 + n : n;
        if constexpr (w == 0) kf[r] = ldsr<bf16x8>(kb_ + 512 * (7 - n) + off);
        if constexpr (w == 1) { const u32x2 v = ldsr<u32x2>(hb + 512 * n + off); u32x4 t = __builtin_bit_cast(u32x4, ta[r]); t.x = v.x; t.y = v.y; ta[r] = __builtin_bit_cast(bf16x8, t); }
        if constexpr (w == 2) { const u32x2 v = ldsr<u32x2>(hb2 + 512 * n + off); u32x4 t = __builtin_bit_cast(u32x4, ta[r]); t.z = v.x; t.w = v.y; ta[r] = __builtin_bit_cast(bf16x8, t); }
      };
      auto cstep = [&](auto odd, auto off) AI {
        sfor<36>([&](auto g) AI {
          constexpr int c = cpair(35 - g) >> 3, dl = cpair(35 - g) & 7;
          constexpr int rc = (c < 4 && odd) ? 8 + c : c, rd_ = (dl < 4 && odd) ? 8 + dl : dl;
          dacc[c + dl] = __builtin_amdgcn_mfma_f32_16x16x32_bf16(kf[rc], ta[rd_], dacc[c + dl], 0, 0, 0);
          if constexpr (g < 12) ld1(IC<1 - odd>{}, IC<g / 3>{}, IC<g % 3>{}, off);
          else if constexpr (g < 24) ld1(IC<1 - odd>{}, IC<7 - (g - 12) / 3>{}, IC<g % 3>{}, off);
          GAP();
        });
      };
      sfor<24>([&](auto i) AI { ld1(IC<0>{}, IC<(i < 3 ? 0 : i < 6 ? 7 : i < 9 ? 1 : i < 12 ? 6 : i < 15 ? 2 : i < 18 ? 5 : i < 21 ? 3 : 4)>{}, IC<i % 3>{}, IC<0>{}); GAP(); });
#pragma unroll 1
      for (int i = 0; i < 2; ++i) {
        cstep(IC<0>{}, IC<64>{});
        cstep(IC<1>{}, IC<128>{});
        hb += 128; hb2 += 128; kb_ += 128;
      }
    }
    __builtin_amdgcn_s_setprio(3);
    STAMP(25);
    sfor<4>([&](auto i) AI { ldsw<f32x4>(XCH + j * 4096 + i * 1024 + lane * 16, cp ? dacc[i] : dacc[4 + i]); });
    lds_set(CVP + j, 4);
    {
      const int l = lane_id();
      u32 a = cb + KA + 2 * (2046 - 2 * (l & 15) - 64 * (l >> 4) - 1024 * cp) - 1600;
      pin(a);
      sfor<8>([&](auto i) AI { kv2[i] = ldsr<u32>(a + 64 * (1 - (i & 1)) + 512 * (3 - (i >> 1))); });
    }
    STAMP(7);
    {
      const float dv = bflo(dvv);
      sfor<8>([&](auto i) AI { kv2[i] = pk2s(bfhi(kv2[i]) * dv, bflo(kv2[i]) * dv); pin(kv2[i]); });
      wait_byte<0>(CVP + (j ^ 2), 4);
      f32x4 o[4];
      sfor<4>([&](auto i) AI { o[i] = ldsr<f32x4>(XCH + (j ^ 2) * 4096 + i * 1024 + lane * 16); });
      asm volatile("" ::: "memory");
      lds_set(CVP + j, 5);
      sfor<4>([&](auto i) AI {
        const f32x4 m = cp ? dacc[4 + i] : dacc[i];
        sfor<2>([&](auto rr) AI {
          const u32 yb = pk2s(m[2 * rr] + o[i][2 * rr], m[2 * rr + 1] + o[i][2 * rr + 1]), kd = kv2[2 * i + rr];
          ldsw<u32>(ZST + j * 2048 + 4 * c16 + 128 * q + 64 * rr + 512 * i, pk2s(bflo(yb) + bflo(kd), bfhi(yb) + bfhi(kd)));
        });
      });
      waitl();
      u32x2 zo[4];
      sfor<4>([&](auto t) AI { zo[t] = ldsr<u32x2>(ZST + j * 2048 + t * 512 + lane * 8); });
      sfor<4>([&](auto t) AI {
        __builtin_amdgcn_raw_buffer_store_b128(u32x4{zo[t].x, tag, zo[t].y, tag}, rws, lane * 16 + W_ZT + (2 * b + ch) * 8192 + cp * 4096 + t * 1024, 0, 16);
      });
    }
    STAMP(8);
    const float mean = bflo(sg), rs = __builtin_amdgcn_rcpf(bfhi(sg));
    u32 xn[8];
    sfor<8>([&](auto i) AI {
      const u32 x2 = xr[i >> 1][i & 1], n2 = pk2s(bflo(x2) - mean, bfhi(x2) - mean);
      xn[i] = pk2s(bflo(n2) * rs, bfhi(n2) * rs);
      pin(xn[i]);
    });
    if (b == 0 && j == 3) {
      sfor<4>([&](auto t) AI { poll_flags<1>(ws, W_PF + t * 4096, 64, epoch, lane); });
      if (lane == 0) __hip_atomic_store((GAS u64*)(ws + W_E), epoch, RLX_AGENT);
    }
    wait_min4<2>(FREE, ZAFTER);
    u32x4 zr[4];
#pragma unroll 1
    for (u32 it = 0; it < SPIN_CAP; ++it) {
      sfor<4>([&](auto t) AI { bld4_sc1<0>(zr[t], l8 * 8192 + c8 * 16, rws, W_ZT + (ocol + 8 * t) * 8192 + (64 * rt + 32 * mh) * 4); });
      asm volatile("s_waitcnt vmcnt(0)" : "+v"(zr[0]), "+v"(zr[1]), "+v"(zr[2]), "+v"(zr[3])::"memory");
      u32 bad = 0;
      sfor<4>([&](auto t) AI { bad |= (zr[t].y ^ tag) | (zr[t].w ^ tag); });
      if (__all(bad == 0u)) break;
      __builtin_amdgcn_s_sleep(ZSLEEP);
    }
    STAMP(10);
    sfor<4>([&](auto t) AI { ldsw<u32x2>(ZST + j * 2048 + t * 512 + lane * 8, u32x2{zr[t].x, zr[t].z}); });
    waitl();
    float tt[16];
    sfor<4>([&](auto g) AI {
      const u32x2 z4 = ldtr(ZST + j * 2048 + (8 * g + 4 * hh + ((lane & 15) >> 2)) * 64 + 32 * ((lane >> 4) & 1) + 8 * (lane & 3));
      sfor<2>([&](auto e) AI {
        const u32 x2 = xr[g][(int)e], z2 = z4[(int)e];
        u32 n2 = pk2s(bflo(xn[2 * g + e]) * bflo(z2), bfhi(xn[2 * g + e]) * bfhi(z2));
        n2 = pk2s(bflo(x2) + bflo(n2), bfhi(x2) + bfhi(n2));
        tt[4 * g + 2 * e] = bflo(n2); tt[4 * g + 2 * e + 1] = bfhi(n2);
      });
    });
    sfor<16>([&](auto i) AI { pin(tt[i]); });
    STAMP(11);
    wait_min4<0>(FREE, 12);
    STAMP(12);
    u32x2 ob[4];
    sfor<4>([&](auto g) AI {
      const f32x4 v = ldsr<f32x4>(PART + mh * 8192 + db * 4096 + g * 1024 + lane * 16) + ldsr<f32x4>(PART + (2 + mh) * 8192 + db * 4096 + g * 1024 + lane * 16);
      ob[g].x = pk2(tt[4 * g] + rs * v.x, tt[4 * g + 1] + rs * v.y);
      ob[g].y = pk2(tt[4 * g + 2] + rs * v.z, tt[4 * g + 3] + rs * v.w);
    });
    sfor<2>([&](auto gp) AI {
      auto s0 = __builtin_amdgcn_permlane32_swap(ob[2 * gp].x, ob[2 * gp + 1].x, false, false);
      auto s1 = __builtin_amdgcn_permlane32_swap(ob[2 * gp].y, ob[2 * gp + 1].y, false, false);
      const u32x4 v = {(u32)s0[0], (u32)s1[0], (u32)s0[1], (u32)s1[1]};
      *(GAS u32x4*)((GAS char*)mkp(kb[2], kb[3]) + orow * 1024 + ocol * 2 + 32 * gp + 16 * hh) = v;
    });
    STAMP(13);
    DONE();
  } else if (wave < 4) {
    u32 junk, junk2;
    asm volatile("s_load_dword %0, %2, %3\n\ts_load_dword %1, %2, %4" : "=&s"(junk), "=&s"(junk2)
                 : "s"(pc0 & ~127ull), "s"((u32)(li + 32 * j) * 128u), "s"(min((u32)(li + 32 * (4 + j)) * 128u, CODE_BYTES)));
    const rsrc_t rws = mk_rsrc(ws, W_END);
    const int wm = wave >> 1, wn = wave & 1;
    const int l0 = lane_id();
    u32 wa = ((l0 >> 4) & 1) * (12288 + UPAD) + (8 * (l0 >> 5) + ((l0 & 15) >> 2)) * 192 + 96 * wn + 8 * (l0 & 3);
    u64 epoch;
    f32x16 acc[2][3];
    sfor<2>([&](auto m) AI { sfor<3>([&](auto n) AI { sfor<16>([&](auto r) AI { acc[m][n][(int)r] = 0.f; }); pin(acc[m][n]); }); });
    u32 xa[4];
    sfor<4>([&](auto ks) AI { xa[ks] = (64 * wm + (l0 & 31)) * 128 + (((2 * ks + (l0 >> 5)) ^ ((l0 >> 1) & 7)) << 4); pin(xa[ks]); });
    asm volatile("s_waitcnt lgkmcnt(0)" : "+s"(junk), "+s"(junk2));
    STAMP(10);
    if (has) {
      bf16x8 xf[2][2], wf[2][3];
      auto ldw = [&](auto st, auto n) AI { const u32 a = wa + G_OFF + (st & 3) * 3072 + n * 32; wf[st & 1][n] = cat(ldtr(a), ldtr(a + 768)); };
      auto ldx = [&](auto st, auto m) AI { xf[st & 1][m] = ldsr<bf16x8>(xa[st & 3] + m * 4096); };
      wait_min4<1>(FULL, 1);
      u32 accw = 0, cntw = 0;
      STAMP(1);
      ldw(IC<0>{}, IC<0>{}); ldx(IC<0>{}, IC<0>{}); ldx(IC<0>{}, IC<1>{}); ldw(IC<0>{}, IC<1>{}); ldw(IC<0>{}, IC<2>{});
      ldw(IC<1>{}, IC<0>{}); ldw(IC<1>{}, IC<1>{}); ldx(IC<1>{}, IC<0>{});
      GAP();
      u32 needw = 0x01010101u, off = 0;
#pragma unroll 1
      for (int c = 0; c < 8; ++c) {
        u32 fl, adv, have;
        sfor<24>([&](auto gg) AI {
          constexpr int st = gg / 6, g = gg % 6, n = g >> 1, m = g & 1;
          acc[m][n] = __builtin_amdgcn_mfma_f32_32x32x16_bf16(wf[st & 1][n], xf[st & 1][m], acc[m][n], 0, 0, 0);
          if constexpr (g == 0) { ldw(IC<st + 1>{}, IC<2>{}); ldx(IC<st + 1>{}, IC<1>{}); }
          if constexpr (g == 2) ldw(IC<st + 2>{}, IC<0>{});
          if constexpr (g == 4) ldw(IC<st + 2>{}, IC<1>{});
          if constexpr (g == 5) ldx(IC<st + 2>{}, IC<0>{});
          if constexpr (st == 0 && g == 3) { const u32 o2 = off == 2 * SLOT ? 0u : off + SLOT; adv = o2 - off; off = o2; needw += 0x01010101u; spin(adv); spin(off); spin(needw); }
          if constexpr (st == 1 && g == 1) fl = lds_ld1(FULL);
          if constexpr (st == 1 && g == 3) { xa[0] += adv; xa[1] += adv; xa[2] += adv; pin(xa[0]); pin(xa[1]); pin(xa[2]); }
          if constexpr (st == 2 && g == 0) { have = (rfl(fl) - needw) & 0x80808080u; spin(have); }
          if constexpr (st == 2 && g == 1) {
            xa[3] += adv; wa += adv; pin(xa[3]); pin(wa);
            if (__builtin_expect(have != 0u, 0)) wait_min4<0>(FULL, c + 2);
          }
          if constexpr (st == 3 && g == 1) lds_set(FREE + wave, c + 1);
          GAP();
        });
      }
      STAMP(2);
      __builtin_amdgcn_s_setprio(PRE);
      const int lane = lane_id(), s31 = lane & 31, hh = lane >> 5;
      f32x2 st[2];
      f32x4 ca[4];
      auto ldc = [&](auto n) AI { sfor<4>([&](auto g) AI { const u32 a = CS + (wn * 96 + n * 32 + 8 * g + 4 * hh) * 4; ca[g] = ldsr<f32x4>(a); }); };
#pragma unroll 1
      for (u32 it = 0; it < SPIN_CAP * 16; ++it) {
        const u32 r1 = lds_ld1(STRDY);
        asm volatile("" ::: "memory");
        const u32x2 e = lds_ld2(EPO);
        sfor<2>([&](auto m) AI { st[m] = ldsr<f32x2>(ST + (64 * wm + 32 * m + s31) * 8); });
        ldc(IC<0>{});
        asm volatile("" ::: "memory");
        epoch = ((u64)rfl(e.y) << 32) | rfl(e.x);
        if (__builtin_expect(all_ge(rfl(r1), 1), 1)) break;
      }
      STAMP(11);
      sfor<3>([&](auto n) AI {
        const int ks = 6 * cg + 3 * wn + n;
        if (ks < 86) {
          f32x4 c4[4];
          sfor<4>([&](auto g) AI { c4[g] = ca[g]; });
          f32x2 g[8], u[8], e[8];
          sfor<8>([&](auto i) AI {
            constexpr int m = i >> 2, r = 2 * (i & 3);
            const f32x2 cg2 = {c4[r >> 2][r & 3], c4[r >> 2][(r & 3) + 1]};
            g[i] = f32x2{acc[m][n][r], acc[m][n][r + 1]} - st[m].x * cg2;
          });
          GAP();
          sfor<8>([&](auto i) AI { g[i] = g[i] * st[i >> 2].y; });
          GAP();
          sfor<8>([&](auto i) AI { e[i] = g[i] * g[i]; });
          GAP();
          sfor<8>([&](auto i) AI { e[i] = e[i] * -0.1029432397f - 2.3022081985f; });
          GAP();
          sfor<8>([&](auto i) AI { e[i] = e[i] * g[i]; });
          GAP();
          sfor<8>([&](auto i) AI {
            constexpr int m = i >> 2, r = 2 * (i & 3);
            const f32x2 cu2 = {c4[2 + (r >> 2)][r & 3], c4[2 + (r >> 2)][(r & 3) + 1]};
            e[i] = f32x2{__builtin_amdgcn_exp2f(e[i].x), __builtin_amdgcn_exp2f(e[i].y)};
            u[i] = f32x2{acc[m][n][r + 8], acc[m][n][r + 9]} - st[m].x * cu2;
            GAP();
          });
          if constexpr (n < 2) ldc(IC<n + 1>{});
          sfor<8>([&](auto i) AI { e[i] = e[i] + 1.0f; });
          GAP();
          sfor<8>([&](auto i) AI {
            e[i] = f32x2{__builtin_amdgcn_rcpf(e[i].x), __builtin_amdgcn_rcpf(e[i].y)};
            g[i] = g[i] * u[i];
            GAP();
          });
          sfor<8>([&](auto i) AI { g[i] = g[i] * e[i]; });
          GAP();
          sfor<2>([&](auto m) AI {
            const u32x4 o = {pk2(g[4 * m].x, g[4 * m].y), pk2(g[4 * m + 1].x, g[4 * m + 1].y), pk2(g[4 * m + 2].x, g[4 * m + 2].y), pk2(g[4 * m + 3].x, g[4 * m + 3].y)};
            __builtin_amdgcn_raw_buffer_store_b128(o, rws, lane * 16 + W_H + ((4 * rb + 2 * wm + m) * 86 + ks) * 1024, 0, 16);
          });
          GAP();
          if constexpr (n < 2) STAMP(20 + n);
        }
      });
      lds_set(FREE + wave, 9);
      STAMP(3);
      waitv<0>();
      __builtin_amdgcn_s_setprio(PRW);
      if (lds_arrive(CNT + 0, lane) == 4 && lane == 0) __hip_atomic_store((GAS u64*)(ws + W_HF + (rb * 16 + cg) * 64), epoch, RLX_AGENT);
      STAMP(4);
    } else {
      if (csw) {
        f32x4 cs[3];
        sfor<3>([&](auto n) AI { cs[n] = f32x4{0.f, 0.f, 0.f, 0.f}; });
        const u32 o2_ = (l0 & 15) == ((l0 >> 4) & 1) ? 0x3f803f80u : 0u;
        const bf16x8 sel = __builtin_bit_cast(bf16x8, u32x4{o2_, o2_, o2_, o2_});
        wa += wm * 6144;
#pragma unroll 1
        for (int c = 0; c < 8; ++c) {
          wait_min4<1>(FULL, c + 1);
          bf16x8 w[6];
          sfor<6>([&](auto i) AI { const u32 a = wa + (i / 3) * 3072 + (i % 3) * 32; w[i] = cat(ldtr(a), ldtr(a + 768)); });
          asm volatile("" ::: "memory");
          lds_set(FREE + wave, c < 3 ? c + 1 : 3);
          sfor<6>([&](auto i) AI { cs[i % 3] = __builtin_amdgcn_mfma_f32_16x16x32_bf16(w[i], sel, cs[i % 3], 0, 0, 0); });
          wa += c == 4 ? (u32)(-4 * (int)CSLOT) : CSLOT;
        }
        if ((l0 & 15) < 2) sfor<3>([&](auto n) AI { ldsw<f32x4>(CS + (wm * 192 + wn * 96 + n * 32 + (l0 & 15) * 16 + 4 * (l0 >> 4)) * 4, cs[n]); });
      }
      lds_set(FREE + wave, 9);
      epoch = wait_epoch();
      if (csw && wave < 3) {
        wait_min4<1>(FREE, 9);
        const u32 i = 64 * wave + l0;
        const float v = ldsr<float>(CS + i * 4) + ldsr<float>(CS + 768 + i * 4);
        __hip_atomic_store((GAS u64*)(ws + W_CG + (cg * 192 + i) * 8), ((u64)(u32)(epoch >> 17) << 32) | __float_as_uint(v), RLX_AGENT);
      }
      STAMP(4);
    }
    const int lane = lane_id(), hh = lane >> 5, g16 = lane >> 4, q4 = (lane & 15) >> 2, p4 = lane & 3;
    const int kh = wave >> 1, mh = wave & 1;
    u32x4 hf[43];
    const u32 hso = W_H + ((2 * rt + mh) * 86 + 43 * kh) * 1024, hvo = lane * 16;
    f32x16 o2[2];
    sfor<2>([&](auto n) AI { sfor<16>([&](auto r) AI { o2[n][(int)r] = 0.f; }); pin(o2[n]); });
    u32 db_[2][3];
    sfor<2>([&](auto n) AI {
      const u32 da = kh * 8192 + (4 * hh + q4) * 128 + ((4 * ((int)n ^ (q4 >> 1)) + 2 * (g16 & 1) + (p4 >> 1)) << 4) + 8 * (p4 & 1);
      sfor<3>([&](auto w) AI { db_[n][w] = da + w * 61440; pin(db_[n][w]); });
    });
    bf16x8 df[3][2];
    u32 fl;
    auto ldd = [&](auto k) AI {
      constexpr int c = k >> 2, ks = k & 3;
      constexpr u32 wb = (c < 7 ? 16384u * c : c == 7 ? CONV : c == 8 ? CONV + CH_SZ : 16384u * (c - 9)) + ks * 2048;
      constexpr int w = wb / 61440 > 2 ? 2 : wb / 61440;
      sfor<2>([&](auto n) AI { const u32 a = db_[n][w] + (wb - w * 61440); df[k % 3][n] = cat(ldtr(a), ldtr(a + 1024)); });
    };
    if (wave == 0) { poll_flags<1>(ws, W_HF + (rt >> 1) * 16 * 64, 15, epoch, lane); lds_set(CVP + 6, 1); }
    else wait_byte<WSL>(CVP + 6, 1);
    __builtin_amdgcn_s_setprio(2);
    STAMP(5);
    auto hload = [&](auto k) AI { bld4_sc1<(k & 3) * 1024>(hf[k], hvo, rws, hso + (k >> 2) * 4096); };
    sfor<PD>([&](auto k) AI { hload(k); });
    wait_min4<0>(WDRDY, 1);
    STAMP(6);
    ldd(IC<0>{}); ldd(IC<1>{});
    GAP();
    sfor<43>([&](auto k) AI {
      if constexpr (k == 10) lds_set(FREE + wave, 10);
      if constexpr (k == 18) lds_set(FREE + wave, 11);
      if constexpr (k == 20 || k == 32) fl = lds_ld1(WDRDY);
      if constexpr (k == 40) fl = lds_ld1(FREE);
      if constexpr (k == 22 || k == 34) { STAMP(k == 22 ? 12 : 14); if (__builtin_expect(!all_ge(rfl(fl), k == 22 ? 2u : 3u), 0)) wait_min4<0>(WDRDY, k == 22 ? 2 : 3); STAMP(k == 22 ? 13 : 15); GAP(); }
      constexpr int younger = (k + PD <= 43 ? PD : 43 - k) - 1;
      waitv_pin<younger>(hf[k]);
      if constexpr (k == 0) STAMP(24);
      if constexpr (k == 8) STAMP(25);
      if constexpr (k == 16) STAMP(26);
      o2[0] = __builtin_amdgcn_mfma_f32_32x32x16_bf16(df[k % 3][0], __builtin_bit_cast(bf16x8, hf[k]), o2[0], 0, 0, 0);
      if constexpr (k + 2 < 43) ldd(IC<k + 2>{});
      GAP();
      o2[1] = __builtin_amdgcn_mfma_f32_32x32x16_bf16(df[k % 3][1], __builtin_bit_cast(bf16x8, hf[k]), o2[1], 0, 0, 0);
      if constexpr (k + PD < 43) hload(IC<k + PD>{});
      GAP();
    });
    STAMP(7);
    if (!all_ge(rfl(fl), 11u)) wait_min4<0>(FREE, 11);
    sfor<2>([&](auto n) AI { sfor<4>([&](auto g) AI {
      f32x4 v = {o2[n][4 * g], o2[n][4 * g + 1], o2[n][4 * g + 2], o2[n][4 * g + 3]};
      ldsw<f32x4>(PART + wave * 8192 + n * 4096 + g * 1024 + lane * 16, v);
    }); });
    waitl();
    lds_set(FREE + wave, 12);
    STAMP(8);
    DONE();
  } else {
    __builtin_amdgcn_s_setprio(PRL);
    const rsrc_t rg = mk_rsrc(mkp(ka[2], ka[3]), D * F * 2), ru = mk_rsrc(mkp(ka[4], ka[5]), D * F * 2);
    const int crot = ROT * ((rb + cg) & 1);
    auto chx = [&](int c, u32 sb) AI { dma4(sb + 4 * j * 1024, xv[0], xv[1], xv[2], xv[3], rq, (rb * 128 + 32 * j) * 1024 + (c ^ crot) * 128); };
    auto chw = [&](int c, u32 sb) AI {
      dma3(sb + G_OFF + 3 * j * 1024, wv[0], wv[1], wv[2], rg, (c ^ crot) * 64 * F * 2);
      dma3(sb + U_OFF + 3 * j * 1024, wv[0], wv[1], wv[2], ru, (c ^ crot) * 64 * F * 2);
    };
    if (has) {
      chx(0, 0); chw(0, 0); chx(1, SLOT); chw(1, SLOT);
      int ic = 2, lc = 0;
      u32 sb = 2 * SLOT;
#pragma unroll 1
      for (u32 it = 0; it < SPIN_CAP * 16; ++it) {
        if (lc < ic && vm_pending() <= 10u * (u32)(ic - lc - 1)) {
          asm volatile("" ::: "memory");
          ++lc;
          lds_set(FULL + j, lc == 8 ? 9 : lc);
          if (lc == 1) STAMP(1);
          if (lc == 8) break;
        } else if (ic < 8 && all_ge(rfl(lds_ld1(FREE)), ic - 2)) {
          asm volatile("" ::: "memory");
          chx(ic, sb);
          if (lc < ic && vm_pending() <= 10u * (u32)(ic - lc - 1) + 4u) {
            lds_set(FULL + j, ++lc);
          }
          chw(ic, sb);
          ++ic;
          sb = sb == 2 * SLOT ? 0 : sb + SLOT;
        } else {
          __builtin_amdgcn_s_sleep(LSLEEP);
        }
      }
      waitv<0>();
    } else if (csw) {
      sfor<8>([&](auto c) AI {
        if constexpr (c >= 5) { waitv<24>(); lds_set(FULL + j, (6u * c - vm_pending()) * 43u >> 8); wait_min4<1>(FREE, c - 4); }
        chw(c, (c % 5) * CSLOT - G_OFF);
        if constexpr (c >= 2) lds_set(FULL + j, (6u * (c + 1) - vm_pending()) * 43u >> 8);
      });
      waitv<12>(); lds_set(FULL + j, 6);
      waitv<6>(); lds_set(FULL + j, 7);
      waitv<0>(); lds_set(FULL + j, 8);
    }
    STAMP(2);
    const rsrc_t rd = mk_rsrc(mkp(kb[0], kb[1]), F * D * 2);
    u32 dv[4];
    sfor<4>([&](auto t) AI { dv[t] = (7 * t + l8) * 1024 + ((c8 ^ (((l8 >> 1) & 1) << 2)) << 4) + ct * 128; });
    auto wso = [&](int g) AI { return (u32)(((g >> 1) & 1) * 688 + (g >> 2) * 64 + (g & 1) * 32) * 1024u; };
    auto wd_groups = [&](int lo, int hi) AI {
#pragma unroll 1
      for (int g = lo + j; g < hi; g += 4) { dma4(g * 4096, dv[0], dv[1], dv[2], dv[3], rd, wso(g)); waitv<WDFL>(); }
    };
    u32x4 wr[5][4];
    auto wst = [&](auto t, u32 a) AI { sfor<4>([&](auto i) AI { pin(wr[t][i]); ldsw<u32x4>(a + j * 4096 + i * 1024 + lane * 16, wr[t][i]); }); };
    sfor<5>([&](auto t) AI { const u32 so = wso(24 + 4 * t + j); sfor<4>([&](auto i) AI { bld4o<i * 1024>(wr[t][i], dv[i], rd, so); }); });
    wait_min4<1>(FREE, WD1);
    wd_groups(21, 24);
    wait_min4<1>(FREE, WD2);
    wd_groups(0, 10);
    wait_min4<1>(FREE, WD3);
    wd_groups(10, 21);
    waitv<0>();
    lds_set(WDRDY + j, 1);
    STAMP(3);
    wait_min4<4>(CVP, 5);
    wst(IC<0>{}, XCH); wst(IC<1>{}, CONV); wst(IC<2>{}, CONV + CH_SZ);
    waitl();
    lds_set(WDRDY + j, 2);
    STAMP(4);
    wait_min4<2>(FREE, 10);
    wst(IC<3>{}, 0); wst(IC<4>{}, 16384);
    waitl();
    lds_set(WDRDY + j, 3);
    STAMP(5);
    DONE();
  }
}

extern "C" void kernel_launch(void* const* d_in, const int* in_sizes, int n_in, void* d_out, int out_size, void* d_ws, size_t ws_size,
                              hipStream_t stream) {
  Args a;
  a.q = (const u16*)d_in[0]; a.k = (const u16*)d_in[1]; a.v = (const u16*)d_in[2]; a.sk = (const u16*)d_in[3];
  a.h = (const u16*)d_in[4]; a.dv = (const u16*)d_in[5];
  a.wg = (const u16*)d_in[7]; a.wu = (const u16*)d_in[8]; a.wd = (const u16*)d_in[9];
  a.out = (u16*)d_out; a.ws = (char*)d_ws; a.pad = 0;
  (void)hipFuncSetAttribute((const void*)fused, hipFuncAttributeMaxDynamicSharedMemorySize, LDS_BYTES);
  fused<<<256, 768, LDS_BYTES, stream>>>(a);
}
```

```cpp
#define CODE2
#include <hip/hip_runtime.h>
#include <stdint.h>
#include <utility>

typedef unsigned short u16;
typedef unsigned int u32;
typedef unsigned long long u64;
typedef __bf16 bf16x8 __attribute__((ext_vector_type(8)));
typedef __bf16 bf16x2 __attribute__((ext_vector_type(2)));
typedef short s16x4 __attribute__((ext_vector_type(4)));
typedef short s16x8 __attribute__((ext_vector_type(8)));
typedef float f32x16 __attribute__((ext_vector_type(16)));
typedef float f32x4 __attribute__((ext_vector_type(4)));
typedef float f32x2 __attribute__((ext_vector_type(2)));
typedef u32 u32x4 __attribute__((ext_vector_type(4)));
typedef u32 u32x2 __attribute__((ext_vector_type(2)));
typedef __amdgpu_buffer_rsrc_t rsrc_t;
#define DI __device__ __forceinline__
#define AI __attribute__((always_inline))
#define LAS __attribute__((address_space(3)))
#define GAS __attribute__((address_space(1)))
#define RLX_AGENT __ATOMIC_RELAXED, __HIP_MEMORY_SCOPE_AGENT
#define RLX_WG __ATOMIC_RELAXED, __HIP_MEMORY_SCOPE_WORKGROUP

constexpr int S = 2048, D = 512, F = 1376;
#define UPAD 32
#define SLOTB (40960 + UPAD)
constexpr u32 SLOT = SLOTB, G_OFF = 16384, U_OFF = 16384 + 12288 + UPAD;
constexpr u32 CSLOT = 2 * (12288 + UPAD) + 64 - 2 * UPAD % 64;
constexpr u32 CONV = 3 * 41216;
constexpr u32 CH_SZ = 17520, HA = 64, HB = 4292, KA = 8400, KB = 12976, XB = 4304;
constexpr u32 MISC = CONV + 2 * CH_SZ;
constexpr u32 CS = MISC, ST = MISC + 1536, CNT = MISC + 2560;
constexpr u32 CKV = CNT + 8, CSP = CNT + 12, EPO = CNT + 16, CVP = CNT + 24, FULL = CNT + 32, FREE = CNT + 36, STRDY = CNT + 40, WDRDY = CNT + 44;
constexpr u32 LDS_BYTES = MISC + 2816;
static_assert(LDS_BYTES <= 163840, "LDS");
constexpr u32 XCH = 98304, ZST = 114688, PART = 32768;
constexpr u32 W_E = 0, W_PF = 4096, W_HF = W_PF + 16384, W_ZF = W_HF + 16384, W_SG = W_ZF + 16384, W_CG = W_SG + 16384;
constexpr u32 W_KV = 131072, W_ZT = W_KV + 2097152 + 4096, W_H = W_ZT + 4194304, W_END = W_H + 64 * 86 * 1024;
constexpr u32 W_DBG = 16u << 20;
constexpr u32 CODE_BYTES = 26112;
#define PRL 1
#define PRS 0
#define CONVAFTER 9
#define LSLEEP 1
#define PRA 0
#define ROT 0
#define S2AFTER 8
#define WDFL 2
#define KVSTAG 0
#define SSTOP 9
#define POLLS 4
#define PD 10
#define WD1 6
#define WD2 7
#define PFLO 8
#define PFPRIO 0
#define PFWAIT 0
#define KVMODE 0
#define WD3 8
#define PRE 2
#define PRW 0
#define WSL 1
#define PRC 3
#define ZSLEEP 4
#define ZAFTER 11
#define KVAFTER 1
constexpr u32 SPIN_CAP = 1u << 18;

constexpr int cpair(int g) {
  int i = 0;
  for (int n = 0; n < 8; ++n) for (int m = 0; m <= n; ++m) if (m + n <= 7) {
    if (i++ == g) return n * 8 + m;
    if (m < n && i++ == g) return m * 8 + n;
  }
  return 0;
}
template <class Fn, int... I> DI void sfor_impl(Fn&& f, std::integer_sequence<int, I...>) { (f(std::integral_constant<int, I>{}), ...); }
template <int N, class Fn> DI void sfor(Fn&& f) { sfor_impl(f, std::make_integer_sequence<int, N>{}); }

DI rsrc_t mk_rsrc(const void* p, u32 bytes) { return __builtin_amdgcn_make_buffer_rsrc((void*)p, (short)0, (int)bytes, 0x00020000); }
DI void dma(u32 lds, u32 voff, rsrc_t r, u32 soff) {
  asm volatile("s_mov_b32 m0, %0\n\ts_nop 0\n\tbuffer_load_dwordx4 %1, %2, %3 offen lds" ::"s"(lds), "v"(voff), "s"(r), "s"(soff) : "memory");
}

DI void dma4(u32 lds, u32 v0, u32 v1, u32 v2, u32 v3, rsrc_t r, u32 soff) {
  asm volatile("s_mov_b32 m0, %0\n\ts_nop 0\n\tbuffer_load_dwordx4 %1, %5, %6 offen lds\n\tbuffer_load_dwordx4 %2, %5, %6 offen offset:1024 lds\n\t"
               "buffer_load_dwordx4 %3, %5, %6 offen offset:2048 lds\n\tbuffer_load_dwordx4 %4, %5, %6 offen offset:3072 lds"
               ::"s"(lds), "v"(v0), "v"(v1), "v"(v2), "v"(v3), "s"(r), "s"(soff) : "memory");
}
DI void dma3(u32 lds, u32 v0, u32 v1, u32 v2, rsrc_t r, u32 soff) {
  asm volatile("s_mov_b32 m0, %0\n\ts_nop 0\n\tbuffer_load_dwordx4 %1, %4, %5 offen lds\n\tbuffer_load_dwordx4 %2, %4, %5 offen offset:1024 lds\n\t"
               "buffer_load_dwordx4 %3, %4, %5 offen offset:2048 lds"
               ::"s"(lds), "v"(v0), "v"(v1), "v"(v2), "s"(r), "s"(soff) : "memory");
}
template <int OFF> DI void bld4_sc1(u32x4& d, u32 voff, rsrc_t r, u32 soff) {
  asm volatile("buffer_load_dwordx4 %0, %1, %2, %3 offen offset:%4 sc1" : "=v"(d) : "v"(voff), "s"(r), "s"(soff), "i"(OFF));
}
template <int OFF> DI void bld4o(u32x4& d, u32 voff, rsrc_t r, u32 soff) { asm volatile("buffer_load_dwordx4 %0, %1, %2, %3 offen offset:%4" : "=v"(d) : "v"(voff), "s"(r), "s"(soff), "i"(OFF)); }
DI void bld4(u32x4& d, u32 voff, rsrc_t r, u32 soff) { asm volatile("buffer_load_dwordx4 %0, %1, %2, %3 offen" : "=v"(d) : "v"(voff), "s"(r), "s"(soff)); }
template <int OFF> DI void bld2(u32x2& d, u32 voff, rsrc_t r) { asm volatile("buffer_load_dwordx2 %0, %1, %2, 0 offen offset:%3" : "=v"(d) : "v"(voff), "s"(r), "i"(OFF)); }
template <int OFF> DI void gld4_sc1(u32x4& d, u32 voff, const char* base) {
  asm volatile("global_load_dwordx4 %0, %1, %2 offset:%3 sc1" : "=v"(d) : "v"(voff), "s"(base), "i"(OFF));
}
template <int N> DI void waitv_pin(u32x4& d) { asm volatile("s_waitcnt vmcnt(%1)" : "+v"(d) : "i"(N)); }
template <int N> DI void waitv() { asm volatile("s_waitcnt vmcnt(%0)" ::"i"(N) : "memory"); }
DI u32 vm_pending() { u32 r; asm volatile("s_getreg_b32 %0, hwreg(HW_REG_IB_STS)" : "=s"(r)::"memory"); return (r & 15u) | ((r >> 18) & 48u); }
DI void bar() { asm volatile("s_waitcnt lgkmcnt(0)\n\ts_barrier" ::: "memory"); }
DI void waitl() { asm volatile("s_waitcnt lgkmcnt(0)" ::: "memory"); }
DI float bflo(u32 x) { return __uint_as_float(x << 16); }
DI float bfhi(u32 x) { return __uint_as_float(x & 0xffff0000u); }
DI u32 pk2(float lo, float hi) { f32x2 v = {lo, hi}; return __builtin_bit_cast(u32, __builtin_convertvector(v, bf16x2)); }
DI u32 pk2s(float lo, float hi) { asm("" : "+v"(lo)); asm("" : "+v"(hi)); return pk2(lo, hi); }
DI float rbf(float x) { return bflo(pk2(x, x)); }
template <class T> DI T ldsr(u32 a) { return *(const LAS T*)a; }
template <class T> DI void ldsw(u32 a, T v) { *(LAS T*)a = v; }
DI u32x2 ldtr(u32 a) { return __builtin_bit_cast(u32x2, __builtin_amdgcn_ds_read_tr16_b64_v4i16((LAS s16x4*)a)); }
DI bf16x8 cat(u32x2 a, u32x2 b) { u32x4 v = {a.x, a.y, b.x, b.y}; return __builtin_bit_cast(bf16x8, v); }
template <int CTRL, int RM> DI float dpp_add(float v) {
  return v + __builtin_bit_cast(float, __builtin_amdgcn_update_dpp(0, __builtin_bit_cast(int, v), CTRL, RM, 0xf, false));
}
DI float wave_sum(float v) {
  v = dpp_add<0x111, 0xf>(v); v = dpp_add<0x112, 0xf>(v); v = dpp_add<0x114, 0xf>(v); v = dpp_add<0x118, 0xf>(v);
  v = dpp_add<0x142, 0xa>(v); v = dpp_add<0x143, 0xc>(v);
  return __builtin_bit_cast(float, __builtin_amdgcn_readlane(__builtin_bit_cast(int, v), 63));
}
DI void wave_sum2(float& a, float& c) {
  a = dpp_add<0x111, 0xf>(a); c = dpp_add<0x111, 0xf>(c); a = dpp_add<0x112, 0xf>(a); c = dpp_add<0x112, 0xf>(c);
  a = dpp_add<0x114, 0xf>(a); c = dpp_add<0x114, 0xf>(c); a = dpp_add<0x118, 0xf>(a); c = dpp_add<0x118, 0xf>(c);
  a = dpp_add<0x142, 0xa>(a); c = dpp_add<0x142, 0xa>(c); a = dpp_add<0x143, 0xc>(a); c = dpp_add<0x143, 0xc>(c);
  a = __builtin_bit_cast(float, __builtin_amdgcn_readlane(__builtin_bit_cast(int, a), 63));
  c = __builtin_bit_cast(float, __builtin_amdgcn_readlane(__builtin_bit_cast(int, c), 63));
}
DI void gld8_sc1(u64& d, const char* p) { asm volatile("global_load_dwordx2 %0, %1, off sc1" : "=v"(d) : "v"(p)); }
DI void gld_u16(u32& d, u32 voff, const char* base) { asm volatile("global_load_ushort %0, %1, %2" : "=v"(d) : "v"(voff), "s"(base)); }
template <int SLEEP> DI void poll_flags(const char* ws, u32 off, int n, u64 epoch, int lane) {
  const GAS u64* p = (const GAS u64*)(ws + off + (u32)(lane < n ? lane : 0) * 64u);
#pragma unroll 1
  for (u32 it = 0; it < SPIN_CAP; ++it) {
    u64 v = __hip_atomic_load(p, RLX_AGENT);
    if (__all(v == epoch)) break;
    __builtin_amdgcn_s_sleep(SLEEP);
  }
  asm volatile("" ::: "memory");
}
DI u32 poll_granule(const char* ws, u32 row, u32 tag) {
  const GAS u64* p = (const GAS u64*)(ws + W_SG + row * 8u);
  u64 v = 0;
#pragma unroll 1
  for (u32 it = 0; it < SPIN_CAP; ++it) {
    v = __hip_atomic_load(p, RLX_AGENT);
    if (__all((u32)(v >> 32) == tag)) break;
    __builtin_amdgcn_s_sleep(1);
  }
  return (u32)v;
}
DI u32 lds_arrive(u32 a, int lane) {
  u32 r = 0;
  waitl();
  if (lane == 0) r = __hip_atomic_fetch_add((LAS u32*)a, 1u, RLX_WG);
  return (u32)__builtin_amdgcn_readfirstlane(r) + 1u;
}
DI void lds_wait(u32 a, u32 want) {
#pragma unroll 1
  for (u32 it = 0; it < SPIN_CAP * 16; ++it) {
    u32 v = __hip_atomic_load((LAS u32*)a, RLX_WG);
    if ((u32)__builtin_amdgcn_readfirstlane(v) >= want) break;
  }
  asm volatile("" ::: "memory");
}

DI int lane_id() { int l; asm volatile("v_mbcnt_lo_u32_b32 %0, -1, 0\n\tv_mbcnt_hi_u32_b32 %0, -1, %0" : "=v"(l)); return l; }
DI u32 rfl(u32 x) { return (u32)__builtin_amdgcn_readfirstlane(x); }
DI u32x2 lds_ld2(u32 a) { return __builtin_bit_cast(u32x2, __hip_atomic_load((LAS u64*)a, RLX_WG)); }
template <int SLEEP> DI void wait_min2(u32 a, u32 want) {
#pragma unroll 1
  for (u32 it = 0; it < SPIN_CAP * 16; ++it) {
    const u32x2 v = lds_ld2(a);
    if (rfl(min(v.x, v.y)) >= want) break;
    if (SLEEP) __builtin_amdgcn_s_sleep(SLEEP);
  }
  asm volatile("" ::: "memory");
}
DI u32 lds_ld1(u32 a) { return __hip_atomic_load((LAS u32*)a, RLX_WG); }
DI bool all_ge(u32 w, u32 want) { return ((w - want * 0x01010101u) & 0x80808080u) == 0u; }
template <int SLEEP> DI void wait_min4(u32 a, u32 want) {
#pragma unroll 1
  for (u32 it = 0; it < SPIN_CAP * 16; ++it) {
    if (all_ge(rfl(lds_ld1(a)), want)) break;
    if (SLEEP) __builtin_amdgcn_s_sleep(SLEEP);
  }
  asm volatile("" ::: "memory");
}
template <int SLEEP> DI void wait_byte(u32 a, u32 want) {
#pragma unroll 1
  for (u32 it = 0; it < SPIN_CAP * 16; ++it) {
    if (rfl(__hip_atomic_load((LAS unsigned char*)a, RLX_WG)) >= want) break;
    if (SLEEP) __builtin_amdgcn_s_sleep(SLEEP);
  }
  asm volatile("" ::: "memory");
}
DI u64 wait_epoch() {
  u32x2 e;
#pragma unroll 1
  for (u32 it = 0; it < SPIN_CAP * 16; ++it) {
    e = lds_ld2(EPO);
    if (rfl(e.x) != 0u) break;
    __builtin_amdgcn_s_sleep(2);
  }
  return ((u64)rfl(e.y) << 32) | rfl(e.x);
}
DI u64 gld_agent(const char* p) { return __hip_atomic_load((const GAS u64*)p, RLX_AGENT); }
DI void lds_set(u32 a, u32 v) { __hip_atomic_store((LAS unsigned char*)a, (unsigned char)v, RLX_WG); }
DI void gather(u32& junk, u32 voff, rsrc_t r, u32 soff) { asm volatile("buffer_load_dword %0, %1, %2, %3 offen" : "=v"(junk) : "v"(voff), "s"(r), "s"(soff)); }
template <int N> using IC = std::integral_constant<int, N>;

#define DONE() do {} while (0)
#define STAMP(id) do {} while (0)

struct Args { const u16 *q, *wg, *wu, *k, *v, *sk, *h, *dv, *wd; u16* out; char* ws; u64 pad; };
typedef u32 u32x16 __attribute__((ext_vector_type(16)));
typedef u32 u32x8 __attribute__((ext_vector_type(8)));
DI char* mkp(u32 lo, u32 hi) { return (char*)(((u64)hi << 32) | lo); }
#define GAP() __builtin_amdgcn_sched_barrier(0)
DI void barx() { asm volatile("s_barrier" ::: "memory"); }
template <class T> DI void spin(T& x) { asm volatile("" : "+s"(x)); }
template <class T> DI void pin(T& x) { asm volatile("" : "+v"(x)); }

__global__ void __launch_bounds__(768, 3) fused(Args) {
  u64 pc0; asm volatile("s_getpc_b64 %0" : "=s"(pc0));
  u32x16 ka; u32x8 kb;
  asm volatile("s_load_dwordx16 %0, %2, 0x0\n\ts_load_dwordx8 %1, %2, 0x40" : "=&s"(ka), "=&s"(kb) : "s"(__builtin_amdgcn_kernarg_segment_ptr()));
  GAP();
  const int tid = threadIdx.x, lane = tid & 63;
  const int wave = __builtin_amdgcn_readfirstlane(tid >> 6);
  const int b = blockIdx.x, xcd = b & 7, li = b >> 3;
  if (tid < 16) ldsw<u32>(CNT + tid * 4, 0u);
  const bool has = li < 30;
  const int id = 30 * xcd + (has ? li : 0), rr_ = id % 120, rb = 8 * (id / 120) + (rr_ & 7);
  const int cg = has ? rr_ >> 3 : 4 * (xcd & 3) + 2 * (xcd >> 2) + (li - 30);
  const bool csw = !has && cg < 15;
  const int rt = 4 * xcd + (li & 3), ct = li >> 2;
  const int j = wave & 3, l8 = lane >> 3, c8 = lane & 7;
  u32 xv[4], wv[3];
  sfor<4>([&](auto t) AI { xv[t] = (7 * t + l8) * 1024 + ((c8 ^ ((4 * t + (l8 >> 1)) & 7)) << 4); pin(xv[t]); });
  sfor<3>([&](auto t) AI {
    const u32 byte = t * 1024 + lane * 16, row = byte / 192u;
    const u32 seg = byte - row * 192u;
    wv[t] = cg * 192 + seg >= F * 2 ? 0x40000000u : (16 * j + row) * (F * 2) + cg * 192 + seg - t * 1024;
    pin(wv[t]);
  });
  asm volatile("s_waitcnt lgkmcnt(0)\n\ts_barrier" : "+s"(ka), "+s"(kb)::"memory");
  STAMP(9);
  char* const ws = mkp(kb[4], kb[5]);
  const rsrc_t rq = mk_rsrc(mkp(ka[0], ka[1]), S * D * 2);
  if (wave >= 8) {
    __builtin_amdgcn_s_setprio(3);
    u64 epoch;
    asm volatile("s_load_dwordx2 %0, %1, 0x0" : "=s"(epoch) : "s"(ws));
    const int lane = lane_id(), l8 = lane >> 3, c8 = lane & 7;
    const int ch = j & 1, cp = j >> 1, c16 = lane & 15, q = lane >> 4;
    const rsrc_t rws = mk_rsrc(ws, W_END);
    const u32 cb = CONV + ch * CH_SZ, hown = cb + (cp ? XB : HA), kown = cb + (cp ? KB : KA);
    const int srow = 256 * xcd + 8 * li + 2 * j;
    if (SSTOP == 0) wait_min4<8>(FREE, 8);
    u32x4 sx[2];
    sfor<2>([&](auto r) AI { bld4(sx[r], lane * 16, rq, (srow + r) * 1024); });
    STAMP(23);
    const int pa = b & 31, pg = b >> 5, s0 = 64 * pa + 16 * j;
    u32x2 skw[2];
    u32 dvv;
    auto kvload = [&]() AI {
      const rsrc_t rk = mk_rsrc(mkp(ka[6], ka[7]), S * D * 2), rv = mk_rsrc(mkp(ka[8], ka[9]), S * D * 2), rs_ = mk_rsrc(mkp(ka[10], ka[11]), D * 8);
      const u32 kvv = l8 * 1024 + c8 * 16 + pg * 128;
      sfor<3>([&](auto t) AI { const int r = s0 - 8 + 8 * t; dma(hown + t * 1024, kvv, rk, (r < 0 ? 0 : r) * 1024); });
      sfor<2>([&](auto t) AI { dma(kown + t * 1024, kvv, rv, (s0 + 8 * t) * 1024); });
      sfor<2>([&](auto t) AI { bld2<256 * t>(skw[t], (64 * pg + 16 * (q >> 1) + c16) * 8, rs_); });
      gld_u16(dvv, (2 * b + ch) * 2, mkp(ka[14], ka[15]));
    };
    asm volatile("s_waitcnt lgkmcnt(0)" : "+s"(epoch));
    epoch += 0x9E3779B97F4A7C15ull;
    epoch = (epoch ^ (epoch >> 30)) * 0xBF58476D1CE4E5B9ull; epoch = (epoch ^ (epoch >> 27)) * 0x94D049BB133111EBull;
    epoch = (epoch ^ (epoch >> 31)) | 1ull;
    ldsw<u64>(EPO, epoch);
    STAMP(18);
    const u32 tag = (u32)(epoch >> 17);
    if (has) wait_min4<2>(FULL, KVAFTER + KVSTAG * (li & 3));
    STAMP(19);
    kvload();
    STAMP(20);
    asm volatile("s_waitcnt vmcnt(8)" : "+v"(sx[0]), "+v"(sx[1]));
    STAMP(21);
    __builtin_amdgcn_s_setprio(0);
    {
      float x[2][8], mean[2], var[2];
      sfor<2>([&](auto r) AI {
        sfor<4>([&](auto i) AI { x[r][2 * i] = bflo(sx[r][(int)i]); x[r][2 * i + 1] = bfhi(sx[r][(int)i]); });
        mean[r] = x[r][0];
        sfor<7>([&](auto i) AI { mean[r] += x[r][i + 1]; });
      });
      wave_sum2(mean[0], mean[1]);
      sfor<2>([&](auto r) AI {
        mean[r] *= 1.0f / D; var[r] = 0.f;
        sfor<8>([&](auto i) AI { float d = x[r][i] - mean[r]; asm("" : "+v"(d)); var[r] += d * d; });
      });
      wave_sum2(var[0], var[1]);
      STAMP(22);
      const float mn = lane ? mean[1] : mean[0], vr = rbf((lane ? var[1] : var[0]) * (1.0f / D));
      const u32 g = pk2s(mn, __builtin_sqrtf(rbf(vr + 1e-5f)));
      if (lane < 2) __hip_atomic_store((GAS u64*)(ws + W_SG + (srow + lane) * 8), ((u64)tag << 32) | g, RLX_AGENT);
    }
    STAMP(1);
    asm volatile("s_waitcnt vmcnt(1)" : "+v"(skw[0]), "+v"(skw[1]), "+v"(dvv)::"memory");
    STAMP(2);
    {
      const bool first = s0 == 0 && (q & 1) == 0;
      u32x2 kr[2][5];
      sfor<2>([&](auto gi) AI {
        const u32 a = (8 * (q & 1) + (c16 >> 2)) * 128 + 64 * gi + 32 * (q >> 1) + 8 * (lane & 3);
        kr[gi][0] = ldtr(hown + a + 512); kr[gi][1] = ldtr(hown + a + 1024); kr[gi][2] = ldtr(hown + a + 1536); kr[gi][3] = ldtr(kown + a); kr[gi][4] = ldtr(kown + a + 512);
      });
      GAP();
      if (SSTOP == 1) wait_min4<8>(FREE, 8);
      sfor<2>([&](auto gi) AI {
        const float w0 = bflo(skw[gi].x), w1 = bfhi(skw[gi].x), w2 = bflo(skw[gi].y), w3 = bfhi(skw[gi].y);
        const u32x2 k0 = kr[gi][0], k1 = kr[gi][1], k2 = kr[gi][2], v0 = kr[gi][3], v1 = kr[gi][4];
        const float kk[11] = {first ? 0.f : bfhi(k0.x), first ? 0.f : bflo(k0.y), first ? 0.f : bfhi(k0.y), bflo(k1.x), bfhi(k1.x), bflo(k1.y), bfhi(k1.y), bflo(k2.x), bfhi(k2.x), bflo(k2.y), bfhi(k2.y)};
        const float vv[8] = {bflo(v0.x), bfhi(v0.x), bflo(v0.y), bfhi(v0.y), bflo(v1.x), bfhi(v1.x), bflo(v1.y), bfhi(v1.y)};
        u32 a4[4], m4[4];
        sfor<4>([&](auto t) AI { a4[t] = pk2s(w3 * kk[2 * t], w3 * kk[2 * t + 1]); m4[t] = pk2s(w2 * kk[2 * t + 1], w2 * kk[2 * t + 2]); });
        GAP();
        sfor<4>([&](auto t) AI { a4[t] = pk2s(bflo(a4[t]) + bflo(m4[t]), bfhi(a4[t]) + bfhi(m4[t])); m4[t] = pk2s(w1 * kk[2 * t + 2], w1 * kk[2 * t + 3]); });
        GAP();
        sfor<4>([&](auto t) AI { a4[t] = pk2s(bflo(a4[t]) + bflo(m4[t]), bfhi(a4[t]) + bfhi(m4[t])); m4[t] = pk2s(w0 * kk[2 * t + 3], w0 * kk[2 * t + 4]); });
        GAP();
        sfor<4>([&](auto t) AI { a4[t] = pk2s(bflo(a4[t]) + bflo(m4[t]), bfhi(a4[t]) + bfhi(m4[t])); });
        GAP();
        u32x4 ov;
        sfor<4>([&](auto t) AI { ov[3 - t] = pk2s(bfhi(a4[t]) * vv[2 * t + 1], bflo(a4[t]) * vv[2 * t]); });
        const int dg = 2 * gi + (q >> 1);
        ldsw<u32x4>(CONV + (dg & 1) * CH_SZ + (dg >> 1 ? KB : KA) + 2048 + c16 * 128 + (((7 - 2 * j - (q & 1)) ^ (c16 & 7)) << 4), ov);
      });
    }
    waitl();
    lds_set(CVP + j, 1);
    __builtin_amdgcn_s_setprio(3);
    STAMP(17);
    wait_min4<0>(CVP, 1);
    {
      u32x4 o[2];
      sfor<2>([&](auto t) AI { o[t] = ldsr<u32x4>(kown + 2048 + (8 * t + l8) * 128 + ((c8 ^ l8) << 4)); });
      sfor<2>([&](auto t) AI { __builtin_amdgcn_raw_buffer_store_b128(o[t], rws, W_KV + (64 * pg + 16 * j + 8 * t + l8) * 4096 + (1984 - 64 * pa) * 2 + c8 * 16, 0, 16); });
    }
    const int mh = j & 1, db = j >> 1, s31 = lane & 31, hh = lane >> 5;
    const int orow = 64 * rt + 32 * mh + s31, ocol = 64 * ct + 32 * db;
    u32x2 xr[4];
    u32 sg;
    {
      const char* p1 = ws + W_SG + (hh ? orow : rb * 128 + 32 * j + s31) * 8;
      const char* p2 = ws + W_CG + ((has ? cg : 0) * 192 + 48 * j + (lane < 48 ? lane : 0)) * 8;
      u64 g1, g2;
      gld8_sc1(g1, p1); gld8_sc1(g2, p2);
      asm volatile("s_waitcnt vmcnt(0)" : "+v"(g1), "+v"(g2)::"memory");
      if (lds_arrive(CKV, lane) == 4 && lane == 0) __hip_atomic_store((GAS u64*)(ws + W_PF + b * 64), epoch, RLX_AGENT);
      STAMP(3);
#pragma unroll 1
      for (u32 it = 0; it < SPIN_CAP; ++it) {
        if (__all((u32)(g1 >> 32) == tag && (u32)(g2 >> 32) == tag)) break;
        __builtin_amdgcn_s_sleep(4);
        g1 = gld_agent(p1); g2 = gld_agent(p2);
      }
      sg = (u32)g1;
      if (has) {
        if (lane < 32) ldsw<f32x2>(ST + (32 * j + lane) * 8, f32x2{bflo(sg), __builtin_amdgcn_rcpf(bfhi(sg))});
        if (lane < 48) ldsw<u32>(CS + (48 * j + lane) * 4, (u32)g2);
      }
      lds_set(STRDY + j, 1);
      STAMP(4);
    }
    if (has) wait_min4<4>(FULL, S2AFTER);
    STAMP(24);
    { u32 t0, t1; asm volatile("s_load_dword %0, %2, %3\n\ts_load_dword %1, %2, %4\n\ts_waitcnt lgkmcnt(0)" : "=&s"(t0), "=&s"(t1)
                 : "s"(pc0 & ~127ull), "s"((u32)(li + 32 * j) * 128u), "s"(min((u32)(li + 32 * (4 + j)) * 128u, CODE_BYTES))); }
    {
      u64 f;
      gld8_sc1(f, ws + W_PF + ((b >> 5) * 32 + (j == 0 ? s31 : 0)) * 64);
      dma4(cb + (cp ? HB : HA), lane * 16, lane * 16, lane * 16, lane * 16, mk_rsrc(mkp(ka[12], ka[13]), D * S * 2), (2 * b + ch) * 4096);
      if (lane < (cp ? 33 : 16)) ldsw<u32>(cb + (cp ? 4160 : 0) + lane * 4, 0u);
      if (lane < (cp ? 28 : 30)) ldsw<u32x4>(kown + 4096 + lane * 16, u32x4{0u, 0u, 0u, 0u});
      asm volatile("s_waitcnt vmcnt(4)" : "+v"(f));
      sg = (u32)__builtin_amdgcn_permlane32_swap(sg, sg, false, false)[1];
      if (j == 0) { if (!__all(f == epoch)) poll_flags<2>(ws, W_PF + (b >> 5) * 32 * 64, 32, epoch, lane); lds_set(CVP + 4, 1); }
      wait_byte<1>(CVP + 4, 1);
      u32x4 kvr[4];
      const char* src = ws + W_KV + (2 * b + ch) * 4096 + 2 * cp;
      sfor<4>([&](auto t) AI { gld4_sc1<t * 1024>(kvr[t], lane * 16, src); });
      wait_min4<1>(FREE, 6);
      sfor<2>([&](auto t) AI {
        const int r = 16 * t + (lane >> 2);
        dma(ZST + j * 2048 + t * 1024, r * 1024 + (((lane & 3) ^ ((r >> 1) & 3)) << 4), rq, (64 * rt + 32 * mh) * 1024 + ocol * 2);
      });
      asm volatile("s_waitcnt vmcnt(0)" : "+v"(kvr[0]), "+v"(kvr[1]), "+v"(kvr[2]), "+v"(kvr[3])::"memory");
      if (cp && lane == 63) kvr[3].w &= 0xffffu;
      sfor<4>([&](auto t) AI { ldsw<u32x4>(kown + t * 1024 + lane * 16, kvr[t]); });
      sfor<4>([&](auto g) AI { xr[g] = ldsr<u32x2>(ZST + j * 2048 + s31 * 64 + ((g ^ ((s31 >> 1) & 3)) << 4) + 8 * hh); });
      waitl();
      pin(xr[0]); pin(xr[1]); pin(xr[2]); pin(xr[3]);
      lds_set(CVP + j, 2);
      STAMP(5);
    }
    __builtin_amdgcn_s_setprio(0);
    wait_min4<1>(FREE, CONVAFTER);
    wait_byte<0>(CVP + (j ^ 2), 2);
    __builtin_amdgcn_s_setprio(PRC);
    STAMP(6);
    f32x4 dacc[8];
    sfor<8>([&](auto i) AI { dacc[i] = f32x4{0.f, 0.f, 0.f, 0.f}; });
    u32 kv2[8];
    {
      u32 hb = cb + ((c16 & 1) ? HA : HB) + 4 * (c16 - 15) + 16 * q + 256 * cp;
      u32 kb_ = cb + ((c16 & 1) ? KA : KB) + 448 - 64 * (c16 >> 1) + 16 * q + 256 * cp;
      u32 hb2 = hb + 8;
      pin(hb); pin(hb2); pin(kb_);
      bf16x8 ta[12], kf[12];
      auto ld1 = [&](auto odd, auto n, auto w, auto off) AI {
        constexpr int r = (n < 4 && odd) ? 8 + n : n;
        if constexpr (w == 0) kf[r] = ldsr<bf16x8>(kb_ + 512 * (7 - n) + off);
        if constexpr (w == 1) { const u32x2 v = ldsr<u32x2>(hb + 512 * n + off); u32x4 t = __builtin_bit_cast(u32x4, ta[r]); t.x = v.x; t.y = v.y; ta[r] = __builtin_bit_cast(bf16x8, t); }
        if constexpr (w == 2) { const u32x2 v = ldsr<u32x2>(hb2 + 512 * n + off); u32x4 t = __builtin_bit_cast(u32x4, ta[r]); t.z = v.x; t.w = v.y; ta[r] = __builtin_bit_cast(bf16x8, t); }
      };
      auto cstep = [&](auto odd, auto off) AI {
        sfor<36>([&](auto g) AI {
          constexpr int c = cpair(35 - g) >> 3, dl = cpair(35 - g) & 7;
          constexpr int rc = (c < 4 && odd) ? 8 + c : c, rd_ = (dl < 4 && odd) ? 8 + dl : dl;
          dacc[c + dl] = __builtin_amdgcn_mfma_f32_16x16x32_bf16(kf[rc], ta[rd_], dacc[c + dl], 0, 0, 0);
          if constexpr (g < 12) ld1(IC<1 - odd>{}, IC<g / 3>{}, IC<g % 3>{}, off);
          else if constexpr (g < 24) ld1(IC<1 - odd>{}, IC<7 - (g - 12) / 3>{}, IC<g % 3>{}, off);
          GAP();
        });
      };
      sfor<24>([&](auto i) AI { ld1(IC<0>{}, IC<(i < 3 ? 0 : i < 6 ? 7 : i < 9 ? 1 : i < 12 ? 6 : i < 15 ? 2 : i < 18 ? 5 : i < 21 ? 3 : 4)>{}, IC<i % 3>{}, IC<0>{}); GAP(); });
#pragma unroll 1
      for (int i = 0; i < 2; ++i) {
        cstep(IC<0>{}, IC<64>{});
        cstep(IC<1>{}, IC<128>{});
        hb += 128; hb2 += 128; kb_ += 128;
      }
    }
    __builtin_amdgcn_s_setprio(3);
    STAMP(25);
    sfor<4>([&](auto i) AI { ldsw<f32x4>(XCH + j * 4096 + i * 1024 + lane * 16, cp ? dacc[i] : dacc[4 + i]); });
    lds_set(CVP + j, 4);
    {
      const int l = lane_id();
      u32 a = cb + KA + 2 * (2046 - 2 * (l & 15) - 64 * (l >> 4) - 1024 * cp) - 1600;
      pin(a);
      sfor<8>([&](auto i) AI { kv2[i] = ldsr<u32>(a + 64 * (1 - (i & 1)) + 512 * (3 - (i >> 1))); });
    }
    STAMP(7);
    {
      const float dv = bflo(dvv);
      sfor<8>([&](auto i) AI { kv2[i] = pk2s(bfhi(kv2[i]) * dv, bflo(kv2[i]) * dv); pin(kv2[i]); });
      wait_byte<0>(CVP + (j ^ 2), 4);
      f32x4 o[4];
      sfor<4>([&](auto i) AI { o[i] = ldsr<f32x4>(XCH + (j ^ 2) * 4096 + i * 1024 + lane * 16); });
      asm volatile("" ::: "memory");
      lds_set(CVP + j, 5);
      sfor<4>([&](auto i) AI {
        const f32x4 m = cp ? dacc[4 + i] : dacc[i];
        sfor<2>([&](auto rr) AI {
          const u32 yb = pk2s(m[2 * rr] + o[i][2 * rr], m[2 * rr + 1] + o[i][2 * rr + 1]), kd = kv2[2 * i + rr];
          ldsw<u32>(ZST + j * 2048 + 4 * c16 + 128 * q + 64 * rr + 512 * i, pk2s(bflo(yb) + bflo(kd), bfhi(yb) + bfhi(kd)));
        });
      });
      waitl();
      u32x2 zo[4];
      sfor<4>([&](auto t) AI { zo[t] = ldsr<u32x2>(ZST + j * 2048 + t * 512 + lane * 8); });
      sfor<4>([&](auto t) AI {
        __builtin_amdgcn_raw_buffer_store_b128(u32x4{zo[t].x, tag, zo[t].y, tag}, rws, lane * 16 + W_ZT + (2 * b + ch) * 8192 + cp * 4096 + t * 1024, 0, 16);
      });
    }
    STAMP(8);
    const float mean = bflo(sg), rs = __builtin_amdgcn_rcpf(bfhi(sg));
    u32 xn[8];
    sfor<8>([&](auto i) AI {
      const u32 x2 = xr[i >> 1][i & 1], n2 = pk2s(bflo(x2) - mean, bfhi(x2) - mean);
      xn[i] = pk2s(bflo(n2) * rs, bfhi(n2) * rs);
      pin(xn[i]);
    });
    if (b == 0 && j == 3) {
      sfor<4>([&](auto t) AI { poll_flags<1>(ws, W_PF + t * 4096, 64, epoch, lane); });
      if (lane == 0) __hip_atomic_store((GAS u64*)(ws + W_E), epoch, RLX_AGENT);
    }
    wait_min4<2>(FREE, ZAFTER);
    u32x4 zr[4];
#pragma unroll 1
    for (u32 it = 0; it < SPIN_CAP; ++it) {
      sfor<4>([&](auto t) AI { bld4_sc1<0>(zr[t], l8 * 8192 + c8 * 16, rws, W_ZT + (ocol + 8 * t) * 8192 + (64 * rt + 32 * mh) * 4); });
      asm volatile("s_waitcnt vmcnt(0)" : "+v"(zr[0]), "+v"(zr[1]), "+v"(zr[2]), "+v"(zr[3])::"memory");
      u32 bad = 0;
      sfor<4>([&](auto t) AI { bad |= (zr[t].y ^ tag) | (zr[t].w ^ tag); });
      if (__all(bad == 0u)) break;
      __builtin_amdgcn_s_sleep(ZSLEEP);
    }
    STAMP(10);
    sfor<4>([&](auto t) AI { ldsw<u32x2>(ZST + j * 2048 + t * 512 + lane * 8, u32x2{zr[t].x, zr[t].z}); });
    waitl();
    float tt[16];
    sfor<4>([&](auto g) AI {
      const u32x2 z4 = ldtr(ZST + j * 2048 + (8 * g + 4 * hh + ((lane & 15) >> 2)) * 64 + 32 * ((lane >> 4) & 1) + 8 * (lane & 3));
      sfor<2>([&](auto e) AI {
        const u32 x2 = xr[g][(int)e], z2 = z4[(int)e];
        u32 n2 = pk2s(bflo(xn[2 * g + e]) * bflo(z2), bfhi(xn[2 * g + e]) * bfhi(z2));
        n2 = pk2s(bflo(x2) + bflo(n2), bfhi(x2) + bfhi(n2));
        tt[4 * g + 2 * e] = bflo(n2); tt[4 * g + 2 * e + 1] = bfhi(n2);
      });
    });
    sfor<16>([&](auto i) AI { pin(tt[i]); });
    STAMP(11);
    wait_min4<0>(FREE, 12);
    STAMP(12);
    u32x2 ob[4];
    sfor<4>([&](auto g) AI {
      const f32x4 v = ldsr<f32x4>(PART + mh * 8192 + db * 4096 + g * 1024 + lane * 16) + ldsr<f32x4>(PART + (2 + mh) * 8192 + db * 4096 + g * 1024 + lane * 16);
      ob[g].x = pk2(tt[4 * g] + rs * v.x, tt[4 * g + 1] + rs * v.y);
      ob[g].y = pk2(tt[4 * g + 2] + rs * v.z, tt[4 * g + 3] + rs * v.w);
    });
    sfor<2>([&](auto gp) AI {
      auto s0 = __builtin_amdgcn_permlane32_swap(ob[2 * gp].x, ob[2 * gp + 1].x, false, false);
      auto s1 = __builtin_amdgcn_permlane32_swap(ob[2 * gp].y, ob[2 * gp + 1].y, false, false);
      const u32x4 v = {(u32)s0[0], (u32)s1[0], (u32)s0[1], (u32)s1[1]};
      *(GAS u32x4*)((GAS char*)mkp(kb[2], kb[3]) + orow * 1024 + ocol * 2 + 32 * gp + 16 * hh) = v;
    });
    STAMP(13);
    DONE();
  } else if (wave < 4) {
    u32 junk, junk2;
    asm volatile("s_load_dword %0, %2, %3\n\ts_load_dword %1, %2, %4" : "=&s"(junk), "=&s"(junk2)
                 : "s"(pc0 & ~127ull), "s"((u32)(li + 32 * j) * 128u), "s"(min((u32)(li + 32 * (4 + j)) * 128u, CODE_BYTES)));
    const rsrc_t rws = mk_rsrc(ws, W_END);
    const int wm = wave >> 1, wn = wave & 1;
    const int l0 = lane_id();
    u32 wa = ((l0 >> 4) & 1) * (12288 + UPAD) + (8 * (l0 >> 5) + ((l0 & 15) >> 2)) * 192 + 96 * wn + 8 * (l0 & 3);
    u64 epoch;
    f32x16 acc[2][3];
    sfor<2>([&](auto m) AI { sfor<3>([&](auto n) AI { sfor<16>([&](auto r) AI { acc[m][n][(int)r] = 0.f; }); pin(acc[m][n]); }); });
    u32 xa[4];
    sfor<4>([&](auto ks) AI { xa[ks] = (64 * wm + (l0 & 31)) * 128 + (((2 * ks + (l0 >> 5)) ^ ((l0 >> 1) & 7)) << 4); pin(xa[ks]); });
    asm volatile("s_waitcnt lgkmcnt(0)" : "+s"(junk), "+s"(junk2));
    STAMP(10);
    if (has) {
      bf16x8 xf[2][2], wf[2][3];
      auto ldw = [&](auto st, auto n) AI { const u32 a = wa + G_OFF + (st & 3) * 3072 + n * 32; wf[st & 1][n] = cat(ldtr(a), ldtr(a + 768)); };
      auto ldx = [&](auto st, auto m) AI { xf[st & 1][m] = ldsr<bf16x8>(xa[st & 3] + m * 4096); };
      wait_min4<1>(FULL, 1);
      u32 accw = 0, cntw = 0;
      STAMP(1);
      ldw(IC<0>{}, IC<0>{}); ldx(IC<0>{}, IC<0>{}); ldx(IC<0>{}, IC<1>{}); ldw(IC<0>{}, IC<1>{}); ldw(IC<0>{}, IC<2>{});
      ldw(IC<1>{}, IC<0>{}); ldw(IC<1>{}, IC<1>{}); ldx(IC<1>{}, IC<0>{});
      GAP();
      u32 needw = 0x01010101u, off = 0;
#pragma unroll 1
      for (int c = 0; c < 8; ++c) {
        u32 fl, adv, have;
        sfor<24>([&](auto gg) AI {
          constexpr int st = gg / 6, g = gg % 6, n = g >> 1, m = g & 1;
          acc[m][n] = __builtin_amdgcn_mfma_f32_32x32x16_bf16(wf[st & 1][n], xf[st & 1][m], acc[m][n], 0, 0, 0);
          if constexpr (g == 0) { ldw(IC<st + 1>{}, IC<2>{}); ldx(IC<st + 1>{}, IC<1>{}); }
          if constexpr (g == 2) ldw(IC<st + 2>{}, IC<0>{});
          if constexpr (g == 4) ldw(IC<st + 2>{}, IC<1>{});
          if constexpr (g == 5) ldx(IC<st + 2>{}, IC<0>{});
          if constexpr (st == 0 && g == 3) { const u32 o2 = off == 2 * SLOT ? 0u : off + SLOT; adv = o2 - off; off = o2; needw += 0x01010101u; spin(adv); spin(off); spin(needw); }
          if constexpr (st == 1 && g == 1) fl = lds_ld1(FULL);
          if constexpr (st == 1 && g == 3) { xa[0] += adv; xa[1] += adv; xa[2] += adv; pin(xa[0]); pin(xa[1]); pin(xa[2]); }
          if constexpr (st == 2 && g == 0) { have = (rfl(fl) - needw) & 0x80808080u; spin(have); }
          if constexpr (st == 2 && g == 1) {
            xa[3] += adv; wa += adv; pin(xa[3]); pin(wa);
            if (__builtin_expect(have != 0u, 0)) wait_min4<0>(FULL, c + 2);
          }
          if constexpr (st == 3 && g == 1) lds_set(FREE + wave, c + 1);
          GAP();
        });
      }
      STAMP(2);
      __builtin_amdgcn_s_setprio(PRE);
      const int lane = lane_id(), s31 = lane & 31, hh = lane >> 5;
      f32x2 st[2];
      f32x4 ca[4];
      auto ldc = [&](auto n) AI { sfor<4>([&](auto g) AI { const u32 a = CS + (wn * 96 + n * 32 + 8 * g + 4 * hh) * 4; ca[g] = ldsr<f32x4>(a); }); };
#pragma unroll 1
      for (u32 it = 0; it < SPIN_CAP * 16; ++it) {
        const u32 r1 = lds_ld1(STRDY);
        asm volatile("" ::: "memory");
        const u32x2 e = lds_ld2(EPO);
        sfor<2>([&](auto m) AI { st[m] = ldsr<f32x2>(ST + (64 * wm + 32 * m + s31) * 8); });
        ldc(IC<0>{});
        asm volatile("" ::: "memory");
        epoch = ((u64)rfl(e.y) << 32) | rfl(e.x);
        if (__builtin_expect(all_ge(rfl(r1), 1), 1)) break;
      }
      STAMP(11);
      sfor<3>([&](auto n) AI {
        const int ks = 6 * cg + 3 * wn + n;
        if (ks < 86) {
          f32x4 c4[4];
          sfor<4>([&](auto g) AI { c4[g] = ca[g]; });
          f32x2 g[8], u[8], e[8];
          sfor<8>([&](auto i) AI {
            constexpr int m = i >> 2, r = 2 * (i & 3);
            const f32x2 cg2 = {c4[r >> 2][r & 3], c4[r >> 2][(r & 3) + 1]};
            g[i] = f32x2{acc[m][n][r], acc[m][n][r + 1]} - st[m].x * cg2;
          });
          GAP();
          sfor<8>([&](auto i) AI { g[i] = g[i] * st[i >> 2].y; });
          GAP();
          sfor<8>([&](auto i) AI { e[i] = g[i] * g[i]; });
          GAP();
          sfor<8>([&](auto i) AI { e[i] = e[i] * -0.1029432397f - 2.3022081985f; });
          GAP();
          sfor<8>([&](auto i) AI { e[i] = e[i] * g[i]; });
          GAP();
          sfor<8>([&](auto i) AI {
            constexpr int m = i >> 2, r = 2 * (i & 3);
            const f32x2 cu2 = {c4[2 + (r >> 2)][r & 3], c4[2 + (r >> 2)][(r & 3) + 1]};
            e[i] = f32x2{__builtin_amdgcn_exp2f(e[i].x), __builtin_amdgcn_exp2f(e[i].y)};
            u[i] = f32x2{acc[m][n][r + 8], acc[m][n][r + 9]} - st[m].x * cu2;
            GAP();
          });
          if constexpr (n < 2) ldc(IC<n + 1>{});
          sfor<8>([&](auto i) AI { e[i] = e[i] + 1.0f; });
          GAP();
          sfor<8>([&](auto i) AI {
            e[i] = f32x2{__builtin_amdgcn_rcpf(e[i].x), __builtin_amdgcn_rcpf(e[i].y)};
            g[i] = g[i] * u[i];
            GAP();
          });
          sfor<8>([&](auto i) AI { g[i] = g[i] * e[i]; });
          GAP();
          sfor<2>([&](auto m) AI {
            const u32x4 o = {pk2(g[4 * m].x, g[4 * m].y), pk2(g[4 * m + 1].x, g[4 * m + 1].y), pk2(g[4 * m + 2].x, g[4 * m + 2].y), pk2(g[4 * m + 3].x, g[4 * m + 3].y)};
            __builtin_amdgcn_raw_buffer_store_b128(o, rws, lane * 16 + W_H + ((4 * rb + 2 * wm + m) * 86 + ks) * 1024, 0, 16);
          });
          GAP();
          if constexpr (n < 2) STAMP(20 + n);
        }
      });
      lds_set(FREE + wave, 9);
      STAMP(3);
      waitv<0>();
      __builtin_amdgcn_s_setprio(PRW);
      if (lds_arrive(CNT + 0, lane) == 4 && lane == 0) __hip_atomic_store((GAS u64*)(ws + W_HF + (rb * 16 + cg) * 64), epoch, RLX_AGENT);
      STAMP(4);
    } else {
      if (csw) {
        f32x4 cs[3];
        sfor<3>([&](auto n) AI { cs[n] = f32x4{0.f, 0.f, 0.f, 0.f}; });
        const u32 o2_ = (l0 & 15) == ((l0 >> 4) & 1) ? 0x3f803f80u : 0u;
        const bf16x8 sel = __builtin_bit_cast(bf16x8, u32x4{o2_, o2_, o2_, o2_});
        wa += wm * 6144;
#pragma unroll 1
        for (int c = 0; c < 8; ++c) {
          wait_min4<1>(FULL, c + 1);
          bf16x8 w[6];
          sfor<6>([&](auto i) AI { const u32 a = wa + (i / 3) * 3072 + (i % 3) * 32; w[i] = cat(ldtr(a), ldtr(a + 768)); });
          asm volatile("" ::: "memory");
          lds_set(FREE + wave, c < 3 ? c + 1 : 3);
          sfor<6>([&](auto i) AI { cs[i % 3] = __builtin_amdgcn_mfma_f32_16x16x32_bf16(w[i], sel, cs[i % 3], 0, 0, 0); });
          wa += c == 4 ? (u32)(-4 * (int)CSLOT) : CSLOT;
        }
        if ((l0 & 15) < 2) sfor<3>([&](auto n) AI { ldsw<f32x4>(CS + (wm * 192 + wn * 96 + n * 32 + (l0 & 15) * 16 + 4 * (l0 >> 4)) * 4, cs[n]); });
      }
      lds_set(FREE + wave, 9);
      epoch = wait_epoch();
      if (csw && wave < 3) {
        wait_min4<1>(FREE, 9);
        const u32 i = 64 * wave + l0;
        const float v = ldsr<float>(CS + i * 4) + ldsr<float>(CS + 768 + i * 4);
        __hip_atomic_store((GAS u64*)(ws + W_CG + (cg * 192 + i) * 8), ((u64)(u32)(epoch >> 17) << 32) | __float_as_uint(v), RLX_AGENT);
      }
      STAMP(4);
    }
    const int lane = lane_id(), hh = lane >> 5, g16 = lane >> 4, q4 = (lane & 15) >> 2, p4 = lane & 3;
    const int kh = wave >> 1, mh = wave & 1;
    u32x4 hf[43];
    const u32 hso = W_H + ((2 * rt + mh) * 86 + 43 * kh) * 1024, hvo = lane * 16;
    f32x16 o2[2];
    sfor<2>([&](auto n) AI { sfor<16>([&](auto r) AI { o2[n][(int)r] = 0.f; }); pin(o2[n]); });
    u32 db_[2][3];
    sfor<2>([&](auto n) AI {
      const u32 da = kh * 8192 + (4 * hh + q4) * 128 + ((4 * ((int)n ^ (q4 >> 1)) + 2 * (g16 & 1) + (p4 >> 1)) << 4) + 8 * (p4 & 1);
      sfor<3>([&](auto w) AI { db_[n][w] = da + w * 61440; pin(db_[n][w]); });
    });
    bf16x8 df[3][2];
    u32 fl;
    auto ldd = [&](auto k) AI {
      constexpr int c = k >> 2, ks = k & 3;
      constexpr u32 wb = (c < 7 ? 16384u * c : c == 7 ? CONV : c == 8 ? CONV + CH_SZ : 16384u * (c - 9)) + ks * 2048;
      constexpr int w = wb / 61440 > 2 ? 2 : wb / 61440;
      sfor<2>([&](auto n) AI { const u32 a = db_[n][w] + (wb - w * 61440); df[k % 3][n] = cat(ldtr(a), ldtr(a + 1024)); });
    };
    if (wave == 0) { poll_flags<1>(ws, W_HF + (rt >> 1) * 16 * 64, 15, epoch, lane); lds_set(CVP + 6, 1); }
    else wait_byte<WSL>(CVP + 6, 1);
    __builtin_amdgcn_s_setprio(2);
    STAMP(5);
    auto hload = [&](auto k) AI { bld4_sc1<(k & 3) * 1024>(hf[k], hvo, rws, hso + (k >> 2) * 4096); };
    sfor<PD>([&](auto k) AI { hload(k); });
    wait_min4<0>(WDRDY, 1);
    STAMP(6);
    ldd(IC<0>{}); ldd(IC<1>{});
    GAP();
    sfor<43>([&](auto k) AI {
      if constexpr (k == 10) lds_set(FREE + wave, 10);
      if constexpr (k == 18) lds_set(FREE + wave, 11);
      if constexpr (k == 20 || k == 32) fl = lds_ld1(WDRDY);
      if constexpr (k == 40) fl = lds_ld1(FREE);
      if constexpr (k == 22 || k == 34) { STAMP(k == 22 ? 12 : 14); if (__builtin_expect(!all_ge(rfl(fl), k == 22 ? 2u : 3u), 0)) wait_min4<0>(WDRDY, k == 22 ? 2 : 3); STAMP(k == 22 ? 13 : 15); GAP(); }
      constexpr int younger = (k + PD <= 43 ? PD : 43 - k) - 1;
      waitv_pin<younger>(hf[k]);
      if constexpr (k == 0) STAMP(24);
      if constexpr (k == 8) STAMP(25);
      if constexpr (k == 16) STAMP(26);
      o2[0] = __builtin_amdgcn_mfma_f32_32x32x16_bf16(df[k % 3][0], __builtin_bit_cast(bf16x8, hf[k]), o2[0], 0, 0, 0);
      if constexpr (k + 2 < 43) ldd(IC<k + 2>{});
      GAP();
      o2[1] = __builtin_amdgcn_mfma_f32_32x32x16_bf16(df[k % 3][1], __builtin_bit_cast(bf16x8, hf[k]), o2[1], 0, 0, 0);
      if constexpr (k + PD < 43) hload(IC<k + PD>{});
      GAP();
    });
    STAMP(7);
    if (!all_ge(rfl(fl), 11u)) wait_min4<0>(FREE, 11);
    sfor<2>([&](auto n) AI { sfor<4>([&](auto g) AI {
      f32x4 v = {o2[n][4 * g], o2[n][4 * g + 1], o2[n][4 * g + 2], o2[n][4 * g + 3]};
      ldsw<f32x4>(PART + wave * 8192 + n * 4096 + g * 1024 + lane * 16, v);
    }); });
    waitl();
    lds_set(FREE + wave, 12);
    STAMP(8);
    DONE();
  } else {
    __builtin_amdgcn_s_setprio(PRL);
    const rsrc_t rg = mk_rsrc(mkp(ka[2], ka[3]), D * F * 2), ru = mk_rsrc(mkp(ka[4], ka[5]), D * F * 2);
    const int crot = ROT * ((rb + cg) & 1);
    auto chx = [&](int c, u32 sb) AI { dma4(sb + 4 * j * 1024, xv[0], xv[1], xv[2], xv[3], rq, (rb * 128 + 32 * j) * 1024 + (c ^ crot) * 128); };
    auto chw = [&](int c, u32 sb) AI {
      dma3(sb + G_OFF + 3 * j * 1024, wv[0], wv[1], wv[2], rg, (c ^ crot) * 64 * F * 2);
      dma3(sb + U_OFF + 3 * j * 1024, wv[0], wv[1], wv[2], ru, (c ^ crot) * 64 * F * 2);
    };
    if (has) {
      chx(0, 0); chw(0, 0); chx(1, SLOT); chw(1, SLOT);
      int ic = 2, lc = 0;
      u32 sb = 2 * SLOT;
#pragma unroll 1
      for (u32 it = 0; it < SPIN_CAP * 16; ++it) {
        if (lc < ic && vm_pending() <= 10u * (u32)(ic - lc - 1)) {
          asm volatile("" ::: "memory");
          ++lc;
          lds_set(FULL + j, lc == 8 ? 9 : lc);
          if (lc == 1) STAMP(1);
          if (lc == 8) break;
        } else if (ic < 8 && all_ge(rfl(lds_ld1(FREE)), ic - 2)) {
          asm volatile("" ::: "memory");
          chx(ic, sb);
          if (lc < ic && vm_pending() <= 10u * (u32)(ic - lc - 1) + 4u) {
            lds_set(FULL + j, ++lc);
          }
          chw(ic, sb);
          ++ic;
          sb = sb == 2 * SLOT ? 0 : sb + SLOT;
        } else {
          __builtin_amdgcn_s_sleep(LSLEEP);
        }
      }
      waitv<0>();
    } else if (csw) {
      sfor<8>([&](auto c) AI {
        if constexpr (c >= 5) { waitv<24>(); lds_set(FULL + j, (6u * c - vm_pending()) * 43u >> 8); wait_min4<1>(FREE, c - 4); }
        chw(c, (c % 5) * CSLOT - G_OFF);
        if constexpr (c >= 2) lds_set(FULL + j, (6u * (c + 1) - vm_pending()) * 43u >> 8);
      });
      waitv<12>(); lds_set(FULL + j, 6);
      waitv<6>(); lds_set(FULL + j, 7);
      waitv<0>(); lds_set(FULL + j, 8);
    }
    STAMP(2);
    const rsrc_t rd = mk_rsrc(mkp(kb[0], kb[1]), F * D * 2);
    u32 dv[4];
    sfor<4>([&](auto t) AI { dv[t] = (7 * t + l8) * 1024 + ((c8 ^ (((l8 >> 1) & 1) << 2)) << 4) + ct * 128; });
    auto wso = [&](int g) AI { return (u32)(((g >> 1) & 1) * 688 + (g >> 2) * 64 + (g & 1) * 32) * 1024u; };
    auto wd_groups = [&](int lo, int hi) AI {
#pragma unroll 1
      for (int g = lo + j; g < hi; g += 4) { dma4(g * 4096, dv[0], dv[1], dv[2], dv[3], rd, wso(g)); waitv<WDFL>(); }
    };
    u32x4 wr[5][4];
    auto wst = [&](auto t, u32 a) AI { sfor<4>([&](auto i) AI { pin(wr[t][i]); ldsw<u32x4>(a + j * 4096 + i * 1024 + lane * 16, wr[t][i]); }); };
    sfor<5>([&](auto t) AI { const u32 so = wso(24 + 4 * t + j); sfor<4>([&](auto i) AI { bld4o<i * 1024>(wr[t][i], dv[i], rd, so); }); });
    wait_min4<1>(FREE, WD1);
    wd_groups(21, 24);
    wait_min4<1>(FREE, WD2);
    wd_groups(0, 10);
    wait_min4<1>(FREE, WD3);
    wd_groups(10, 21);
    waitv<0>();
    lds_set(WDRDY + j, 1);
    STAMP(3);
    wait_min4<4>(CVP, 5);
    wst(IC<0>{}, XCH); wst(IC<1>{}, CONV); wst(IC<2>{}, CONV + CH_SZ);
    waitl();
    lds_set(WDRDY + j, 2);
    STAMP(4);
    wait_min4<2>(FREE, 10);
    wst(IC<3>{}, 0); wst(IC<4>{}, 16384);
    waitl();
    lds_set(WDRDY + j, 3);
    STAMP(5);
    DONE();
  }
}

extern "C" void kernel_launch(void* const* d_in, const int* in_sizes, int n_in, void* d_out, int out_size, void* d_ws, size_t ws_size,
                              hipStream_t stream) {
  Args a;
  a.q = (const u16*)d_in[0]; a.k = (const u16*)d_in[1]; a.v = (const u16*)d_in[2]; a.sk = (const u16*)d_in[3];
  a.h = (const u16*)d_in[4]; a.dv = (const u16*)d_in[5];
  a.wg = (const u16*)d_in[7]; a.wu = (const u16*)d_in[8]; a.wd = (const u16*)d_in[9];
  a.out = (u16*)d_out; a.ws = (char*)d_ws; a.pad = 0;
  (void)hipFuncSetAttribute((const void*)fused, hipFuncAttributeMaxDynamicSharedMemorySize, LDS_BYTES);
  fused<<<256, 768, LDS_BYTES, stream>>>(a);
}
```

```cpp
#define AMPID 99
#define AMPWAVE 0
#define ICT 4
#include <hip/hip_runtime.h>
#include <stdint.h>
#include <utility>

typedef unsigned short u16;
typedef unsigned int u32;
typedef unsigned long long u64;
typedef __bf16 bf16x8 __attribute__((ext_vector_type(8)));
typedef __bf16 bf16x2 __attribute__((ext_vector_type(2)));
typedef short s16x4 __attribute__((ext_vector_type(4)));
typedef short s16x8 __attribute__((ext_vector_type(8)));
typedef float f32x16 __attribute__((ext_vector_type(16)));
typedef float f32x4 __attribute__((ext_vector_type(4)));
typedef float f32x2 __attribute__((ext_vector_type(2)));
typedef u32 u32x4 __attribute__((ext_vector_type(4)));
typedef u32 u32x2 __attribute__((ext_vector_type(2)));
typedef __amdgpu_buffer_rsrc_t rsrc_t;
#define DI __device__ __forceinline__
#define AI __attribute__((always_inline))
#define LAS __attribute__((address_space(3)))
#define GAS __attribute__((address_space(1)))
#define RLX_AGENT __ATOMIC_RELAXED, __HIP_MEMORY_SCOPE_AGENT
#define RLX_WG __ATOMIC_RELAXED, __HIP_MEMORY_SCOPE_WORKGROUP

constexpr int S = 2048, D = 512, F = 1376;
#define UPAD 32
#define SLOTB (40960 + UPAD)
constexpr u32 SLOT = SLOTB, G_OFF = 16384, U_OFF = 16384 + 12288 + UPAD;
constexpr u32 CSLOT = 2 * (12288 + UPAD) + 64 - 2 * UPAD % 64;
constexpr u32 CONV = 3 * 41216;
constexpr u32 CH_SZ = 17520, HA = 64, HB = 4292, KA = 8400, KB = 12976, XB = 4304;
constexpr u32 MISC = CONV + 2 * CH_SZ;
constexpr u32 CS = MISC, ST = MISC + 1536, CNT = MISC + 2560;
constexpr u32 CKV = CNT + 8, CSP = CNT + 12, EPO = CNT + 16, CVP = CNT + 24, FULL = CNT + 32, FREE = CNT + 36, STRDY = CNT + 40, WDRDY = CNT + 44;
constexpr u32 LDS_BYTES = MISC + 2816;
static_assert(LDS_BYTES <= 163840, "LDS");
constexpr u32 XCH = 98304, ZST = 114688, PART = 32768;
constexpr u32 W_E = 0, W_PF = 4096, W_HF = W_PF + 16384, W_ZF = W_HF + 16384, W_SG = W_ZF + 16384, W_CG = W_SG + 16384;
constexpr u32 W_KV = 131072, W_ZT = W_KV + 2097152 + 4096, W_H = W_ZT + 4194304, W_END = W_H + 64 * 86 * 1024;
constexpr u32 W_DBG = 16u << 20;
constexpr u32 CODE_BYTES = 30208;
#define PRL 1
#define PRS 0
#define CONVAFTER 9
#define LSLEEP 1
#define PRA 0
#define ROT 0
#define S2AFTER 8
#define WDFL 2
#define KVSTAG 0
#define SSTOP 9
#define POLLS 4
#define PD 10
#define WD1 6
#define WD2 7
#define PFLO 8
#define PFPRIO 0
#define PFWAIT 0
#define KVMODE 0
#define WD3 8
#define PRE 2
#define PRW 0
#define WSL 1
#define PRC 3
#define ZSLEEP 4
#define ZAFTER 11
#define KVAFTER 1
constexpr u32 SPIN_CAP = 1u << 18;

constexpr int cpair(int g) {
  int i = 0;
  for (int n = 0; n < 8; ++n) for (int m = 0; m <= n; ++m) if (m + n <= 7) {
    if (i++ == g) return n * 8 + m;
    if (m < n && i++ == g) return m * 8 + n;
  }
  return 0;
}
template <class Fn, int... I> DI void sfor_impl(Fn&& f, std::integer_sequence<int, I...>) { (f(std::integral_constant<int, I>{}), ...); }
template <int N, class Fn> DI void sfor(Fn&& f) { sfor_impl(f, std::make_integer_sequence<int, N>{}); }

DI rsrc_t mk_rsrc(const void* p, u32 bytes) { return __builtin_amdgcn_make_buffer_rsrc((void*)p, (short)0, (int)bytes, 0x00020000); }
DI void dma(u32 lds, u32 voff, rsrc_t r, u32 soff) {
  asm volatile("s_mov_b32 m0, %0\n\ts_nop 0\n\tbuffer_load_dwordx4 %1, %2, %3 offen lds" ::"s"(lds), "v"(voff), "s"(r), "s"(soff) : "memory");
}

DI void dma4(u32 lds, u32 v0, u32 v1, u32 v2, u32 v3, rsrc_t r, u32 soff) {
  asm volatile("s_mov_b32 m0, %0\n\ts_nop 0\n\tbuffer_load_dwordx4 %1, %5, %6 offen lds\n\tbuffer_load_dwordx4 %2, %5, %6 offen offset:1024 lds\n\t"
               "buffer_load_dwordx4 %3, %5, %6 offen offset:2048 lds\n\tbuffer_load_dwordx4 %4, %5, %6 offen offset:3072 lds"
               ::"s"(lds), "v"(v0), "v"(v1), "v"(v2), "v"(v3), "s"(r), "s"(soff) : "memory");
}
DI void dma3(u32 lds, u32 v0, u32 v1, u32 v2, rsrc_t r, u32 soff) {
  asm volatile("s_mov_b32 m0, %0\n\ts_nop 0\n\tbuffer_load_dwordx4 %1, %4, %5 offen lds\n\tbuffer_load_dwordx4 %2, %4, %5 offen offset:1024 lds\n\t"
               "buffer_load_dwordx4 %3, %4, %5 offen offset:2048 lds"
               ::"s"(lds), "v"(v0), "v"(v1), "v"(v2), "s"(r), "s"(soff) : "memory");
}
template <int OFF> DI void bld4_sc1(u32x4& d, u32 voff, rsrc_t r, u32 soff) {
  asm volatile("buffer_load_dwordx4 %0, %1, %2, %3 offen offset:%4 sc1" : "=v"(d) : "v"(voff), "s"(r), "s"(soff), "i"(OFF));
}
template <int OFF> DI void bld4o(u32x4& d, u32 voff, rsrc_t r, u32 soff) { asm volatile("buffer_load_dwordx4 %0, %1, %2, %3 offen offset:%4" : "=v"(d) : "v"(voff), "s"(r), "s"(soff), "i"(OFF)); }
DI void bld4(u32x4& d, u32 voff, rsrc_t r, u32 soff) { asm volatile("buffer_load_dwordx4 %0, %1, %2, %3 offen" : "=v"(d) : "v"(voff), "s"(r), "s"(soff)); }
template <int OFF> DI void bld2(u32x2& d, u32 voff, rsrc_t r) { asm volatile("buffer_load_dwordx2 %0, %1, %2, 0 offen offset:%3" : "=v"(d) : "v"(voff), "s"(r), "i"(OFF)); }
template <int OFF> DI void gld4_sc1(u32x4& d, u32 voff, const char* base) {
  asm volatile("global_load_dwordx4 %0, %1, %2 offset:%3 sc1" : "=v"(d) : "v"(voff), "s"(base), "i"(OFF));
}
template <int N> DI void waitv_pin(u32x4& d) { asm volatile("s_waitcnt vmcnt(%1)" : "+v"(d) : "i"(N)); }
template <int N> DI void waitv() { asm volatile("s_waitcnt vmcnt(%0)" ::"i"(N) : "memory"); }
DI void ict_(u32& x) { asm volatile("v_add3_u32 %0, %0, %0, %0\n\t" "v_add3_u32 %0, %0, %0, %0\n\t" "v_add3_u32 %0, %0, %0, %0\n\t" "v_add3_u32 %0, %0, %0, %0\n\t" "v_add3_u32 %0, %0, %0, %0\n\t" "v_add3_u32 %0, %0, %0, %0\n\t" "v_add3_u32 %0, %0, %0, %0\n\t" "v_add3_u32 %0, %0, %0, %0\n\t" "v_add3_u32 %0, %0, %0, %0\n\t" "v_add3_u32 %0, %0, %0, %0\n\t" "v_add3_u32 %0, %0, %0, %0\n\t" "v_add3_u32 %0, %0, %0, %0\n\t" "v_add3_u32 %0, %0, %0, %0\n\t" "v_add3_u32 %0, %0, %0, %0\n\t" "v_add3_u32 %0, %0, %0, %0\n\t" "v_add3_u32 %0, %0, %0, %0\n\t" : "+v"(x)); }
DI u32 vm_pending() { u32 r; asm volatile("s_getreg_b32 %0, hwreg(HW_REG_IB_STS)" : "=s"(r)::"memory"); return (r & 15u) | ((r >> 18) & 48u); }
DI void bar() { asm volatile("s_waitcnt lgkmcnt(0)\n\ts_barrier" ::: "memory"); }
DI void waitl() { asm volatile("s_waitcnt lgkmcnt(0)" ::: "memory"); }
DI float bflo(u32 x) { return __uint_as_float(x << 16); }
DI float bfhi(u32 x) { return __uint_as_float(x & 0xffff0000u); }
DI u32 pk2(float lo, float hi) { f32x2 v = {lo, hi}; return __builtin_bit_cast(u32, __builtin_convertvector(v, bf16x2)); }
DI u32 pk2s(float lo, float hi) { asm("" : "+v"(lo)); asm("" : "+v"(hi)); return pk2(lo, hi); }
DI float rbf(float x) { return bflo(pk2(x, x)); }
template <class T> DI T ldsr(u32 a) { return *(const LAS T*)a; }
template <class T> DI void ldsw(u32 a, T v) { *(LAS T*)a = v; }
DI u32x2 ldtr(u32 a) { return __builtin_bit_cast(u32x2, __builtin_amdgcn_ds_read_tr16_b64_v4i16((LAS s16x4*)a)); }
DI bf16x8 cat(u32x2 a, u32x2 b) { u32x4 v = {a.x, a.y, b.x, b.y}; return __builtin_bit_cast(bf16x8, v); }
template <int CTRL, int RM> DI float dpp_add(float v) {
  return v + __builtin_bit_cast(float, __builtin_amdgcn_update_dpp(0, __builtin_bit_cast(int, v), CTRL, RM, 0xf, false));
}
DI float wave_sum(float v) {
  v = dpp_add<0x111, 0xf>(v); v = dpp_add<0x112, 0xf>(v); v = dpp_add<0x114, 0xf>(v); v = dpp_add<0x118, 0xf>(v);
  v = dpp_add<0x142, 0xa>(v); v = dpp_add<0x143, 0xc>(v);
  return __builtin_bit_cast(float, __builtin_amdgcn_readlane(__builtin_bit_cast(int, v), 63));
}
DI void wave_sum2(float& a, float& c) {
  a = dpp_add<0x111, 0xf>(a); c = dpp_add<0x111, 0xf>(c); a = dpp_add<0x112, 0xf>(a); c = dpp_add<0x112, 0xf>(c);
  a = dpp_add<0x114, 0xf>(a); c = dpp_add<0x114, 0xf>(c); a = dpp_add<0x118, 0xf>(a); c = dpp_add<0x118, 0xf>(c);
  a = dpp_add<0x142, 0xa>(a); c = dpp_add<0x142, 0xa>(c); a = dpp_add<0x143, 0xc>(a); c = dpp_add<0x143, 0xc>(c);
  a = __builtin_bit_cast(float, __builtin_amdgcn_readlane(__builtin_bit_cast(int, a), 63));
  c = __builtin_bit_cast(float, __builtin_amdgcn_readlane(__builtin_bit_cast(int, c), 63));
}
DI void gld8_sc1(u64& d, const char* p) { asm volatile("global_load_dwordx2 %0, %1, off sc1" : "=v"(d) : "v"(p)); }
DI void gld_u16(u32& d, u32 voff, const char* base) { asm volatile("global_load_ushort %0, %1, %2" : "=v"(d) : "v"(voff), "s"(base)); }
template <int SLEEP> DI void poll_flags(const char* ws, u32 off, int n, u64 epoch, int lane) {
  const GAS u64* p = (const GAS u64*)(ws + off + (u32)(lane < n ? lane : 0) * 64u);
#pragma unroll 1
  for (u32 it = 0; it < SPIN_CAP; ++it) {
    u64 v = __hip_atomic_load(p, RLX_AGENT);
    if (__all(v == epoch)) break;
    __builtin_amdgcn_s_sleep(SLEEP);
  }
  asm volatile("" ::: "memory");
}
DI u32 poll_granule(const char* ws, u32 row, u32 tag) {
  const GAS u64* p = (const GAS u64*)(ws + W_SG + row * 8u);
  u64 v = 0;
#pragma unroll 1
  for (u32 it = 0; it < SPIN_CAP; ++it) {
    v = __hip_atomic_load(p, RLX_AGENT);
    if (__all((u32)(v >> 32) == tag)) break;
    __builtin_amdgcn_s_sleep(1);
  }
  return (u32)v;
}
DI u32 lds_arrive(u32 a, int lane) {
  u32 r = 0;
  waitl();
  if (lane == 0) r = __hip_atomic_fetch_add((LAS u32*)a, 1u, RLX_WG);
  return (u32)__builtin_amdgcn_readfirstlane(r) + 1u;
}
DI void lds_wait(u32 a, u32 want) {
#pragma unroll 1
  for (u32 it = 0; it < SPIN_CAP * 16; ++it) {
    u32 v = __hip_atomic_load((LAS u32*)a, RLX_WG);
    if ((u32)__builtin_amdgcn_readfirstlane(v) >= want) break;
  }
  asm volatile("" ::: "memory");
}

DI int lane_id() { int l; asm volatile("v_mbcnt_lo_u32_b32 %0, -1, 0\n\tv_mbcnt_hi_u32_b32 %0, -1, %0" : "=v"(l)); return l; }
DI u32 rfl(u32 x) { return (u32)__builtin_amdgcn_readfirstlane(x); }
DI u32x2 lds_ld2(u32 a) { return __builtin_bit_cast(u32x2, __hip_atomic_load((LAS u64*)a, RLX_WG)); }
template <int SLEEP> DI void wait_min2(u32 a, u32 want) {
#pragma unroll 1
  for (u32 it = 0; it < SPIN_CAP * 16; ++it) {
    const u32x2 v = lds_ld2(a);
    if (rfl(min(v.x, v.y)) >= want) break;
    if (SLEEP) __builtin_amdgcn_s_sleep(SLEEP);
  }
  asm volatile("" ::: "memory");
}
DI u32 lds_ld1(u32 a) { return __hip_atomic_load((LAS u32*)a, RLX_WG); }
DI bool all_ge(u32 w, u32 want) { return ((w - want * 0x01010101u) & 0x80808080u) == 0u; }
template <int SLEEP> DI void wait_min4(u32 a, u32 want) {
#pragma unroll 1
  for (u32 it = 0; it < SPIN_CAP * 16; ++it) {
    if (all_ge(rfl(lds_ld1(a)), want)) break;
    if (SLEEP) __builtin_amdgcn_s_sleep(SLEEP);
  }
  asm volatile("" ::: "memory");
}
template <int SLEEP> DI void wait_byte(u32 a, u32 want) {
#pragma unroll 1
  for (u32 it = 0; it < SPIN_CAP * 16; ++it) {
    if (rfl(__hip_atomic_load((LAS unsigned char*)a, RLX_WG)) >= want) break;
    if (SLEEP) __builtin_amdgcn_s_sleep(SLEEP);
  }
  asm volatile("" ::: "memory");
}
DI u64 wait_epoch() {
  u32x2 e;
#pragma unroll 1
  for (u32 it = 0; it < SPIN_CAP * 16; ++it) {
    e = lds_ld2(EPO);
    if (rfl(e.x) != 0u) break;
    __builtin_amdgcn_s_sleep(2);
  }
  return ((u64)rfl(e.y) << 32) | rfl(e.x);
}
DI u64 gld_agent(const char* p) { return __hip_atomic_load((const GAS u64*)p, RLX_AGENT); }
DI void lds_set(u32 a, u32 v) { __hip_atomic_store((LAS unsigned char*)a, (unsigned char)v, RLX_WG); }
DI void gather(u32& junk, u32 voff, rsrc_t r, u32 soff) { asm volatile("buffer_load_dword %0, %1, %2, %3 offen" : "=v"(junk) : "v"(voff), "s"(r), "s"(soff)); }
template <int N> using IC = std::integral_constant<int, N>;

#define DONE() do { if (wave >= 8) { const u64 d_ = rfl(ldsr<u32>(CNT + 60)); while (__builtin_amdgcn_s_memrealtime() - tstart_ < 200 * d_) __builtin_amdgcn_s_sleep(1); } } while (0)
#define STAMP(id) do { if ((id) == AMPID && wave == AMPWAVE) ldsw<u32>(CNT + 60, (u32)(__builtin_amdgcn_s_memrealtime() - tstart_)); } while (0)

struct Args { const u16 *q, *wg, *wu, *k, *v, *sk, *h, *dv, *wd; u16* out; char* ws; u64 pad; };
typedef u32 u32x16 __attribute__((ext_vector_type(16)));
typedef u32 u32x8 __attribute__((ext_vector_type(8)));
DI char* mkp(u32 lo, u32 hi) { return (char*)(((u64)hi << 32) | lo); }
#define GAP() __builtin_amdgcn_sched_barrier(0)
DI void barx() { asm volatile("s_barrier" ::: "memory"); }
template <class T> DI void spin(T& x) { asm volatile("" : "+s"(x)); }
template <class T> DI void pin(T& x) { asm volatile("" : "+v"(x)); }

__global__ void __launch_bounds__(768, 3) fused(Args) {
  u64 pc0; asm volatile("s_getpc_b64 %0" : "=s"(pc0));
  const u64 tstart_ = __builtin_amdgcn_s_memrealtime();
  asm volatile("s_waitcnt lgkmcnt(0)");
  u32x16 ka; u32x8 kb;
  asm volatile("s_load_dwordx16 %0, %2, 0x0\n\ts_load_dwordx8 %1, %2, 0x40" : "=&s"(ka), "=&s"(kb) : "s"(__builtin_amdgcn_kernarg_segment_ptr()));
  GAP();
  const int tid = threadIdx.x, lane = tid & 63;
  const int wave = __builtin_amdgcn_readfirstlane(tid >> 6);
  const int b = blockIdx.x, xcd = b & 7, li = b >> 3;
  if (tid < 16) ldsw<u32>(CNT + tid * 4, 0u);
  const bool has = li < 30;
  const int id = 30 * xcd + (has ? li : 0), rr_ = id % 120, rb = 8 * (id / 120) + (rr_ & 7);
  const int cg = has ? rr_ >> 3 : 4 * (xcd & 3) + 2 * (xcd >> 2) + (li - 30);
  const bool csw = !has && cg < 15;
  const int rt = 4 * xcd + (li & 3), ct = li >> 2;
  const int j = wave & 3, l8 = lane >> 3, c8 = lane & 7;
  u32 xv[4], wv[3];
  sfor<4>([&](auto t) AI { xv[t] = (7 * t + l8) * 1024 + ((c8 ^ ((4 * t + (l8 >> 1)) & 7)) << 4); pin(xv[t]); });
  sfor<3>([&](auto t) AI {
    const u32 byte = t * 1024 + lane * 16, row = byte / 192u;
    const u32 seg = byte - row * 192u;
    wv[t] = cg * 192 + seg >= F * 2 ? 0x40000000u : (16 * j + row) * (F * 2) + cg * 192 + seg - t * 1024;
    pin(wv[t]);
  });
  asm volatile("s_waitcnt lgkmcnt(0)\n\ts_barrier" : "+s"(ka), "+s"(kb)::"memory");
  STAMP(9);
  char* const ws = mkp(kb[4], kb[5]);
  const rsrc_t rq = mk_rsrc(mkp(ka[0], ka[1]), S * D * 2);
  if (wave >= 8) {
    __builtin_amdgcn_s_setprio(3);
    u64 epoch;
    asm volatile("s_load_dwordx2 %0, %1, 0x0" : "=s"(epoch) : "s"(ws));
    const int lane = lane_id(), l8 = lane >> 3, c8 = lane & 7;
    const int ch = j & 1, cp = j >> 1, c16 = lane & 15, q = lane >> 4;
    const rsrc_t rws = mk_rsrc(ws, W_END);
    const u32 cb = CONV + ch * CH_SZ, hown = cb + (cp ? XB : HA), kown = cb + (cp ? KB : KA);
    const int srow = 256 * xcd + 8 * li + 2 * j;
    if (SSTOP == 0) wait_min4<8>(FREE, 8);
    u32x4 sx[2];
    sfor<2>([&](auto r) AI { bld4(sx[r], lane * 16, rq, (srow + r) * 1024); });
    STAMP(23);
    const int pa = b & 31, pg = b >> 5, s0 = 64 * pa + 16 * j;
    u32x2 skw[2];
    u32 dvv;
    auto kvload = [&]() AI {
      const rsrc_t rk = mk_rsrc(mkp(ka[6], ka[7]), S * D * 2), rv = mk_rsrc(mkp(ka[8], ka[9]), S * D * 2), rs_ = mk_rsrc(mkp(ka[10], ka[11]), D * 8);
      const u32 kvv = l8 * 1024 + c8 * 16 + pg * 128;
      sfor<3>([&](auto t) AI { const int r = s0 - 8 + 8 * t; dma(hown + t * 1024, kvv, rk, (r < 0 ? 0 : r) * 1024); });
      sfor<2>([&](auto t) AI { dma(kown + t * 1024, kvv, rv, (s0 + 8 * t) * 1024); });
      sfor<2>([&](auto t) AI { bld2<256 * t>(skw[t], (64 * pg + 16 * (q >> 1) + c16) * 8, rs_); });
      gld_u16(dvv, (2 * b + ch) * 2, mkp(ka[14], ka[15]));
    };
    asm volatile("s_waitcnt lgkmcnt(0)" : "+s"(epoch));
    epoch += 0x9E3779B97F4A7C15ull;
    epoch = (epoch ^ (epoch >> 30)) * 0xBF58476D1CE4E5B9ull; epoch = (epoch ^ (epoch >> 27)) * 0x94D049BB133111EBull;
    epoch = (epoch ^ (epoch >> 31)) | 1ull;
    ldsw<u64>(EPO, epoch);
    STAMP(18);
    const u32 tag = (u32)(epoch >> 17);
    if (has) wait_min4<2>(FULL, KVAFTER + KVSTAG * (li & 3));
    STAMP(19);
    kvload();
    STAMP(20);
    asm volatile("s_waitcnt vmcnt(8)" : "+v"(sx[0]), "+v"(sx[1]));
    STAMP(21);
    __builtin_amdgcn_s_setprio(0);
    {
      float x[2][8], mean[2], var[2];
      sfor<2>([&](auto r) AI {
        sfor<4>([&](auto i) AI { x[r][2 * i] = bflo(sx[r][(int)i]); x[r][2 * i + 1] = bfhi(sx[r][(int)i]); });
        mean[r] = x[r][0];
        sfor<7>([&](auto i) AI { mean[r] += x[r][i + 1]; });
      });
      wave_sum2(mean[0], mean[1]);
      sfor<2>([&](auto r) AI {
        mean[r] *= 1.0f / D; var[r] = 0.f;
        sfor<8>([&](auto i) AI { float d = x[r][i] - mean[r]; asm("" : "+v"(d)); var[r] += d * d; });
      });
      wave_sum2(var[0], var[1]);
      STAMP(22);
      const float mn = lane ? mean[1] : mean[0], vr = rbf((lane ? var[1] : var[0]) * (1.0f / D));
      const u32 g = pk2s(mn, __builtin_sqrtf(rbf(vr + 1e-5f)));
      if (lane < 2) __hip_atomic_store((GAS u64*)(ws + W_SG + (srow + lane) * 8), ((u64)tag << 32) | g, RLX_AGENT);
    }
    STAMP(1);
    asm volatile("s_waitcnt vmcnt(1)" : "+v"(skw[0]), "+v"(skw[1]), "+v"(dvv)::"memory");
    STAMP(2);
    {
      const bool first = s0 == 0 && (q & 1) == 0;
      u32x2 kr[2][5];
      sfor<2>([&](auto gi) AI {
        const u32 a = (8 * (q & 1) + (c16 >> 2)) * 128 + 64 * gi + 32 * (q >> 1) + 8 * (lane & 3);
        kr[gi][0] = ldtr(hown + a + 512); kr[gi][1] = ldtr(hown + a + 1024); kr[gi][2] = ldtr(hown + a + 1536); kr[gi][3] = ldtr(kown + a); kr[gi][4] = ldtr(kown + a + 512);
      });
      GAP();
      if (SSTOP == 1) wait_min4<8>(FREE, 8);
      sfor<2>([&](auto gi) AI {
        const float w0 = bflo(skw[gi].x), w1 = bfhi(skw[gi].x), w2 = bflo(skw[gi].y), w3 = bfhi(skw[gi].y);
        const u32x2 k0 = kr[gi][0], k1 = kr[gi][1], k2 = kr[gi][2], v0 = kr[gi][3], v1 = kr[gi][4];
        const float kk[11] = {first ? 0.f : bfhi(k0.x), first ? 0.f : bflo(k0.y), first ? 0.f : bfhi(k0.y), bflo(k1.x), bfhi(k1.x), bflo(k1.y), bfhi(k1.y), bflo(k2.x), bfhi(k2.x), bflo(k2.y), bfhi(k2.y)};
        const float vv[8] = {bflo(v0.x), bfhi(v0.x), bflo(v0.y), bfhi(v0.y), bflo(v1.x), bfhi(v1.x), bflo(v1.y), bfhi(v1.y)};
        u32 a4[4], m4[4];
        sfor<4>([&](auto t) AI { a4[t] = pk2s(w3 * kk[2 * t], w3 * kk[2 * t + 1]); m4[t] = pk2s(w2 * kk[2 * t + 1], w2 * kk[2 * t + 2]); });
        GAP();
        sfor<4>([&](auto t) AI { a4[t] = pk2s(bflo(a4[t]) + bflo(m4[t]), bfhi(a4[t]) + bfhi(m4[t])); m4[t] = pk2s(w1 * kk[2 * t + 2], w1 * kk[2 * t + 3]); });
        GAP();
        sfor<4>([&](auto t) AI { a4[t] = pk2s(bflo(a4[t]) + bflo(m4[t]), bfhi(a4[t]) + bfhi(m4[t])); m4[t] = pk2s(w0 * kk[2 * t + 3], w0 * kk[2 * t + 4]); });
        GAP();
        sfor<4>([&](auto t) AI { a4[t] = pk2s(bflo(a4[t]) + bflo(m4[t]), bfhi(a4[t]) + bfhi(m4[t])); });
        GAP();
        u32x4 ov;
        sfor<4>([&](auto t) AI { ov[3 - t] = pk2s(bfhi(a4[t]) * vv[2 * t + 1], bflo(a4[t]) * vv[2 * t]); });
        const int dg = 2 * gi + (q >> 1);
        ldsw<u32x4>(CONV + (dg & 1) * CH_SZ + (dg >> 1 ? KB : KA) + 2048 + c16 * 128 + (((7 - 2 * j - (q & 1)) ^ (c16 & 7)) << 4), ov);
      });
    }
    waitl();
    lds_set(CVP + j, 1);
    __builtin_amdgcn_s_setprio(3);
    STAMP(17);
    wait_min4<0>(CVP, 1);
    {
      u32x4 o[2];
      sfor<2>([&](auto t) AI { o[t] = ldsr<u32x4>(kown + 2048 + (8 * t + l8) * 128 + ((c8 ^ l8) << 4)); });
      sfor<2>([&](auto t) AI { __builtin_amdgcn_raw_buffer_store_b128(o[t], rws, W_KV + (64 * pg + 16 * j + 8 * t + l8) * 4096 + (1984 - 64 * pa) * 2 + c8 * 16, 0, 16); });
    }
    const int mh = j & 1, db = j >> 1, s31 = lane & 31, hh = lane >> 5;
    const int orow = 64 * rt + 32 * mh + s31, ocol = 64 * ct + 32 * db;
    u32x2 xr[4];
    u32 sg;
    {
      const char* p1 = ws + W_SG + (hh ? orow : rb * 128 + 32 * j + s31) * 8;
      const char* p2 = ws + W_CG + ((has ? cg : 0) * 192 + 48 * j + (lane < 48 ? lane : 0)) * 8;
      u64 g1, g2;
      gld8_sc1(g1, p1); gld8_sc1(g2, p2);
      asm volatile("s_waitcnt vmcnt(0)" : "+v"(g1), "+v"(g2)::"memory");
      if (lds_arrive(CKV, lane) == 4 && lane == 0) __hip_atomic_store((GAS u64*)(ws + W_PF + b * 64), epoch, RLX_AGENT);
      STAMP(3);
#pragma unroll 1
      for (u32 it = 0; it < SPIN_CAP; ++it) {
        if (__all((u32)(g1 >> 32) == tag && (u32)(g2 >> 32) == tag)) break;
        __builtin_amdgcn_s_sleep(4);
        g1 = gld_agent(p1); g2 = gld_agent(p2);
      }
      sg = (u32)g1;
      if (has) {
        if (lane < 32) ldsw<f32x2>(ST + (32 * j + lane) * 8, f32x2{bflo(sg), __builtin_amdgcn_rcpf(bfhi(sg))});
        if (lane < 48) ldsw<u32>(CS + (48 * j + lane) * 4, (u32)g2);
      }
      lds_set(STRDY + j, 1);
      STAMP(4);
    }
    if (has) wait_min4<4>(FULL, S2AFTER);
    STAMP(24);
    {
      u64 f;
      gld8_sc1(f, ws + W_PF + ((b >> 5) * 32 + (j == 0 ? s31 : 0)) * 64);
      dma4(cb + (cp ? HB : HA), lane * 16, lane * 16, lane * 16, lane * 16, mk_rsrc(mkp(ka[12], ka[13]), D * S * 2), (2 * b + ch) * 4096);
      if (lane < (cp ? 33 : 16)) ldsw<u32>(cb + (cp ? 4160 : 0) + lane * 4, 0u);
      if (lane < (cp ? 28 : 30)) ldsw<u32x4>(kown + 4096 + lane * 16, u32x4{0u, 0u, 0u, 0u});
      asm volatile("s_waitcnt vmcnt(4)" : "+v"(f));
      sg = (u32)__builtin_amdgcn_permlane32_swap(sg, sg, false, false)[1];
      if (j == 0) { if (!__all(f == epoch)) poll_flags<2>(ws, W_PF + (b >> 5) * 32 * 64, 32, epoch, lane); lds_set(CVP + 4, 1); }
      wait_byte<1>(CVP + 4, 1);
      u32x4 kvr[4];
      const char* src = ws + W_KV + (2 * b + ch) * 4096 + 2 * cp;
      sfor<4>([&](auto t) AI { gld4_sc1<t * 1024>(kvr[t], lane * 16, src); });
      wait_min4<1>(FREE, 6);
      sfor<2>([&](auto t) AI {
        const int r = 16 * t + (lane >> 2);
        dma(ZST + j * 2048 + t * 1024, r * 1024 + (((lane & 3) ^ ((r >> 1) & 3)) << 4), rq, (64 * rt + 32 * mh) * 1024 + ocol * 2);
      });
      asm volatile("s_waitcnt vmcnt(0)" : "+v"(kvr[0]), "+v"(kvr[1]), "+v"(kvr[2]), "+v"(kvr[3])::"memory");
      if (cp && lane == 63) kvr[3].w &= 0xffffu;
      sfor<4>([&](auto t) AI { ldsw<u32x4>(kown + t * 1024 + lane * 16, kvr[t]); });
      sfor<4>([&](auto g) AI { xr[g] = ldsr<u32x2>(ZST + j * 2048 + s31 * 64 + ((g ^ ((s31 >> 1) & 3)) << 4) + 8 * hh); });
      waitl();
      pin(xr[0]); pin(xr[1]); pin(xr[2]); pin(xr[3]);
      lds_set(CVP + j, 2);
      STAMP(5);
    }
    __builtin_amdgcn_s_setprio(0);
    wait_min4<1>(FREE, CONVAFTER);
    wait_byte<0>(CVP + (j ^ 2), 2);
    __builtin_amdgcn_s_setprio(PRC);
    STAMP(6);
    f32x4 dacc[8];
    sfor<8>([&](auto i) AI { dacc[i] = f32x4{0.f, 0.f, 0.f, 0.f}; });
    u32 kv2[8];
    {
      u32 hb = cb + ((c16 & 1) ? HA : HB) + 4 * (c16 - 15) + 16 * q + 256 * cp;
      u32 kb_ = cb + ((c16 & 1) ? KA : KB) + 448 - 64 * (c16 >> 1) + 16 * q + 256 * cp;
      u32 hb2 = hb + 8;
      pin(hb); pin(hb2); pin(kb_);
      bf16x8 ta[12], kf[12];
      auto ld1 = [&](auto odd, auto n, auto w, auto off) AI {
        constexpr int r = (n < 4 && odd) ? 8 + n : n;
        if constexpr (w == 0) kf[r] = ldsr<bf16x8>(kb_ + 512 * (7 - n) + off);
        if constexpr (w == 1) { const u32x2 v = ldsr<u32x2>(hb + 512 * n + off); u32x4 t = __builtin_bit_cast(u32x4, ta[r]); t.x = v.x; t.y = v.y; ta[r] = __builtin_bit_cast(bf16x8, t); }
        if constexpr (w == 2) { const u32x2 v = ldsr<u32x2>(hb2 + 512 * n + off); u32x4 t = __builtin_bit_cast(u32x4, ta[r]); t.z = v.x; t.w = v.y; ta[r] = __builtin_bit_cast(bf16x8, t); }
      };
      auto cstep = [&](auto odd, auto off) AI {
        sfor<36>([&](auto g) AI {
          constexpr int c = cpair(35 - g) >> 3, dl = cpair(35 - g) & 7;
          constexpr int rc = (c < 4 && odd) ? 8 + c : c, rd_ = (dl < 4 && odd) ? 8 + dl : dl;
          dacc[c + dl] = __builtin_amdgcn_mfma_f32_16x16x32_bf16(kf[rc], ta[rd_], dacc[c + dl], 0, 0, 0);
          if constexpr (g < 12) ld1(IC<1 - odd>{}, IC<g / 3>{}, IC<g % 3>{}, off);
          else if constexpr (g < 24) ld1(IC<1 - odd>{}, IC<7 - (g - 12) / 3>{}, IC<g % 3>{}, off);
          GAP();
        });
      };
      sfor<24>([&](auto i) AI { ld1(IC<0>{}, IC<(i < 3 ? 0 : i < 6 ? 7 : i < 9 ? 1 : i < 12 ? 6 : i < 15 ? 2 : i < 18 ? 5 : i < 21 ? 3 : 4)>{}, IC<i % 3>{}, IC<0>{}); GAP(); });
#pragma unroll 1
      for (int i = 0; i < 2; ++i) {
        cstep(IC<0>{}, IC<64>{});
        cstep(IC<1>{}, IC<128>{});
        hb += 128; hb2 += 128; kb_ += 128;
      }
    }
    __builtin_amdgcn_s_setprio(3);
    STAMP(25);
    sfor<4>([&](auto i) AI { ldsw<f32x4>(XCH + j * 4096 + i * 1024 + lane * 16, cp ? dacc[i] : dacc[4 + i]); });
    lds_set(CVP + j, 4);
    {
      const int l = lane_id();
      u32 a = cb + KA + 2 * (2046 - 2 * (l & 15) - 64 * (l >> 4) - 1024 * cp) - 1600;
      pin(a);
      sfor<8>([&](auto i) AI { kv2[i] = ldsr<u32>(a + 64 * (1 - (i & 1)) + 512 * (3 - (i >> 1))); });
    }
    STAMP(7);
    {
      const float dv = bflo(dvv);
      sfor<8>([&](auto i) AI { kv2[i] = pk2s(bfhi(kv2[i]) * dv, bflo(kv2[i]) * dv); pin(kv2[i]); });
      wait_byte<0>(CVP + (j ^ 2), 4);
      f32x4 o[4];
      sfor<4>([&](auto i) AI { o[i] = ldsr<f32x4>(XCH + (j ^ 2) * 4096 + i * 1024 + lane * 16); });
      asm volatile("" ::: "memory");
      lds_set(CVP + j, 5);
      sfor<4>([&](auto i) AI {
        const f32x4 m = cp ? dacc[4 + i] : dacc[i];
        sfor<2>([&](auto rr) AI {
          const u32 yb = pk2s(m[2 * rr] + o[i][2 * rr], m[2 * rr + 1] + o[i][2 * rr + 1]), kd = kv2[2 * i + rr];
          ldsw<u32>(ZST + j * 2048 + 4 * c16 + 128 * q + 64 * rr + 512 * i, pk2s(bflo(yb) + bflo(kd), bfhi(yb) + bfhi(kd)));
        });
      });
      waitl();
      u32x2 zo[4];
      sfor<4>([&](auto t) AI { zo[t] = ldsr<u32x2>(ZST + j * 2048 + t * 512 + lane * 8); });
      sfor<4>([&](auto t) AI {
        __builtin_amdgcn_raw_buffer_store_b128(u32x4{zo[t].x, tag, zo[t].y, tag}, rws, lane * 16 + W_ZT + (2 * b + ch) * 8192 + cp * 4096 + t * 1024, 0, 16);
      });
    }
    STAMP(8);
    const float mean = bflo(sg), rs = __builtin_amdgcn_rcpf(bfhi(sg));
    u32 xn[8];
    sfor<8>([&](auto i) AI {
      const u32 x2 = xr[i >> 1][i & 1], n2 = pk2s(bflo(x2) - mean, bfhi(x2) - mean);
      xn[i] = pk2s(bflo(n2) * rs, bfhi(n2) * rs);
      pin(xn[i]);
    });
    if (b == 0 && j == 3) {
      sfor<4>([&](auto t) AI { poll_flags<1>(ws, W_PF + t * 4096, 64, epoch, lane); });
      if (lane == 0) __hip_atomic_store((GAS u64*)(ws + W_E), epoch, RLX_AGENT);
    }
    wait_min4<2>(FREE, ZAFTER);
    u32x4 zr[4];
#pragma unroll 1
    for (u32 it = 0; it < SPIN_CAP; ++it) {
      sfor<4>([&](auto t) AI { bld4_sc1<0>(zr[t], l8 * 8192 + c8 * 16, rws, W_ZT + (ocol + 8 * t) * 8192 + (64 * rt + 32 * mh) * 4); });
      asm volatile("s_waitcnt vmcnt(0)" : "+v"(zr[0]), "+v"(zr[1]), "+v"(zr[2]), "+v"(zr[3])::"memory");
      u32 bad = 0;
      sfor<4>([&](auto t) AI { bad |= (zr[t].y ^ tag) | (zr[t].w ^ tag); });
      if (__all(bad == 0u)) break;
      __builtin_amdgcn_s_sleep(ZSLEEP);
    }
    STAMP(10);
    sfor<4>([&](auto t) AI { ldsw<u32x2>(ZST + j * 2048 + t * 512 + lane * 8, u32x2{zr[t].x, zr[t].z}); });
    waitl();
    float tt[16];
    sfor<4>([&](auto g) AI {
      const u32x2 z4 = ldtr(ZST + j * 2048 + (8 * g + 4 * hh + ((lane & 15) >> 2)) * 64 + 32 * ((lane >> 4) & 1) + 8 * (lane & 3));
      sfor<2>([&](auto e) AI {
        const u32 x2 = xr[g][(int)e], z2 = z4[(int)e];
        u32 n2 = pk2s(bflo(xn[2 * g + e]) * bflo(z2), bfhi(xn[2 * g + e]) * bfhi(z2));
        n2 = pk2s(bflo(x2) + bflo(n2), bfhi(x2) + bfhi(n2));
        tt[4 * g + 2 * e] = bflo(n2); tt[4 * g + 2 * e + 1] = bfhi(n2);
      });
    });
    sfor<16>([&](auto i) AI { pin(tt[i]); });
    STAMP(11);
    wait_min4<0>(FREE, 12);
    STAMP(12);
    u32x2 ob[4];
    sfor<4>([&](auto g) AI {
      const f32x4 v = ldsr<f32x4>(PART + mh * 8192 + db * 4096 + g * 1024 + lane * 16) + ldsr<f32x4>(PART + (2 + mh) * 8192 + db * 4096 + g * 1024 + lane * 16);
      ob[g].x = pk2(tt[4 * g] + rs * v.x, tt[4 * g + 1] + rs * v.y);
      ob[g].y = pk2(tt[4 * g + 2] + rs * v.z, tt[4 * g + 3] + rs * v.w);
    });
    sfor<2>([&](auto gp) AI {
      auto s0 = __builtin_amdgcn_permlane32_swap(ob[2 * gp].x, ob[2 * gp + 1].x, false, false);
      auto s1 = __builtin_amdgcn_permlane32_swap(ob[2 * gp].y, ob[2 * gp + 1].y, false, false);
      const u32x4 v = {(u32)s0[0], (u32)s1[0], (u32)s0[1], (u32)s1[1]};
      *(GAS u32x4*)((GAS char*)mkp(kb[2], kb[3]) + orow * 1024 + ocol * 2 + 32 * gp + 16 * hh) = v;
    });
    STAMP(13);
    DONE();
  } else if (wave < 4) {
    u32 junk, junk2;
    asm volatile("s_load_dword %0, %2, %3\n\ts_load_dword %1, %2, %4" : "=&s"(junk), "=&s"(junk2)
                 : "s"(pc0 & ~127ull), "s"(0u), "s"(0u));
    const rsrc_t rws = mk_rsrc(ws, W_END);
    const int wm = wave >> 1, wn = wave & 1;
    const int l0 = lane_id();
    u32 wa = ((l0 >> 4) & 1) * (12288 + UPAD) + (8 * (l0 >> 5) + ((l0 & 15) >> 2)) * 192 + 96 * wn + 8 * (l0 & 3);
    u64 epoch;
    f32x16 acc[2][3];
    sfor<2>([&](auto m) AI { sfor<3>([&](auto n) AI { sfor<16>([&](auto r) AI { acc[m][n][(int)r] = 0.f; }); pin(acc[m][n]); }); });
    u32 xa[4];
    sfor<4>([&](auto ks) AI { xa[ks] = (64 * wm + (l0 & 31)) * 128 + (((2 * ks + (l0 >> 5)) ^ ((l0 >> 1) & 7)) << 4); pin(xa[ks]); });
    asm volatile("s_waitcnt lgkmcnt(0)" : "+s"(junk), "+s"(junk2));
    STAMP(10);
    if (wave == 0) {
      u32 x_ = l0;
      const u64 a_ = __builtin_amdgcn_s_memrealtime();
      asm volatile("s_waitcnt lgkmcnt(0)");
      sfor<ICT * 8>([&](auto) AI { ict_(x_); });
      const u64 b_ = __builtin_amdgcn_s_memrealtime();
      ldsw<u32>(CNT + 60, (u32)(b_ - a_) + (x_ == 12345u));
    }
    if (has) {
      bf16x8 xf[2][2], wf[2][3];
      auto ldw = [&](auto st, auto n) AI { const u32 a = wa + G_OFF + (st & 3) * 3072 + n * 32; wf[st & 1][n] = cat(ldtr(a), ldtr(a + 768)); };
      auto ldx = [&](auto st, auto m) AI { xf[st & 1][m] = ldsr<bf16x8>(xa[st & 3] + m * 4096); };
      wait_min4<1>(FULL, 1);
      u32 accw = 0, cntw = 0;
      STAMP(1);
      ldw(IC<0>{}, IC<0>{}); ldx(IC<0>{}, IC<0>{}); ldx(IC<0>{}, IC<1>{}); ldw(IC<0>{}, IC<1>{}); ldw(IC<0>{}, IC<2>{});
      ldw(IC<1>{}, IC<0>{}); ldw(IC<1>{}, IC<1>{}); ldx(IC<1>{}, IC<0>{});
      GAP();
      u32 needw = 0x01010101u, off = 0;
#pragma unroll 1
      for (int c = 0; c < 8; ++c) {
        u32 fl, adv, have;
        sfor<24>([&](auto gg) AI {
          constexpr int st = gg / 6, g = gg % 6, n = g >> 1, m = g & 1;
          acc[m][n] = __builtin_amdgcn_mfma_f32_32x32x16_bf16(wf[st & 1][n], xf[st & 1][m], acc[m][n], 0, 0, 0);
          if constexpr (g == 0) { ldw(IC<st + 1>{}, IC<2>{}); ldx(IC<st + 1>{}, IC<1>{}); }
          if constexpr (g == 2) ldw(IC<st + 2>{}, IC<0>{});
          if constexpr (g == 4) ldw(IC<st + 2>{}, IC<1>{});
          if constexpr (g == 5) ldx(IC<st + 2>{}, IC<0>{});
          if constexpr (st == 0 && g == 3) { const u32 o2 = off == 2 * SLOT ? 0u : off + SLOT; adv = o2 - off; off = o2; needw += 0x01010101u; spin(adv); spin(off); spin(needw); }
          if constexpr (st == 1 && g == 1) fl = lds_ld1(FULL);
          if constexpr (st == 1 && g == 3) { xa[0] += adv; xa[1] += adv; xa[2] += adv; pin(xa[0]); pin(xa[1]); pin(xa[2]); }
          if constexpr (st == 2 && g == 0) { have = (rfl(fl) - needw) & 0x80808080u; spin(have); }
          if constexpr (st == 2 && g == 1) {
            xa[3] += adv; wa += adv; pin(xa[3]); pin(wa);
            if (__builtin_expect(have != 0u, 0)) wait_min4<0>(FULL, c + 2);
          }
          if constexpr (st == 3 && g == 1) lds_set(FREE + wave, c + 1);
          GAP();
        });
      }
      STAMP(2);
      __builtin_amdgcn_s_setprio(PRE);
      const int lane = lane_id(), s31 = lane & 31, hh = lane >> 5;
      f32x2 st[2];
      f32x4 ca[4];
      auto ldc = [&](auto n) AI { sfor<4>([&](auto g) AI { const u32 a = CS + (wn * 96 + n * 32 + 8 * g + 4 * hh) * 4; ca[g] = ldsr<f32x4>(a); }); };
#pragma unroll 1
      for (u32 it = 0; it < SPIN_CAP * 16; ++it) {
        const u32 r1 = lds_ld1(STRDY);
        asm volatile("" ::: "memory");
        const u32x2 e = lds_ld2(EPO);
        sfor<2>([&](auto m) AI { st[m] = ldsr<f32x2>(ST + (64 * wm + 32 * m + s31) * 8); });
        ldc(IC<0>{});
        asm volatile("" ::: "memory");
        epoch = ((u64)rfl(e.y) << 32) | rfl(e.x);
        if (__builtin_expect(all_ge(rfl(r1), 1), 1)) break;
      }
      STAMP(11);
      sfor<3>([&](auto n) AI {
        const int ks = 6 * cg + 3 * wn + n;
        if (ks < 86) {
          f32x4 c4[4];
          sfor<4>([&](auto g) AI { c4[g] = ca[g]; });
          f32x2 g[8], u[8], e[8];
          sfor<8>([&](auto i) AI {
            constexpr int m = i >> 2, r = 2 * (i & 3);
            const f32x2 cg2 = {c4[r >> 2][r & 3], c4[r >> 2][(r & 3) + 1]};
            g[i] = f32x2{acc[m][n][r], acc[m][n][r + 1]} - st[m].x * cg2;
          });
          GAP();
          sfor<8>([&](auto i) AI { g[i] = g[i] * st[i >> 2].y; });
          GAP();
          sfor<8>([&](auto i) AI { e[i] = g[i] * g[i]; });
          GAP();
          sfor<8>([&](auto i) AI { e[i] = e[i] * -0.1029432397f - 2.3022081985f; });
          GAP();
          sfor<8>([&](auto i) AI { e[i] = e[i] * g[i]; });
          GAP();
          sfor<8>([&](auto i) AI {
            constexpr int m = i >> 2, r = 2 * (i & 3);
            const f32x2 cu2 = {c4[2 + (r >> 2)][r & 3], c4[2 + (r >> 2)][(r & 3) + 1]};
            e[i] = f32x2{__builtin_amdgcn_exp2f(e[i].x), __builtin_amdgcn_exp2f(e[i].y)};
            u[i] = f32x2{acc[m][n][r + 8], acc[m][n][r + 9]} - st[m].x * cu2;
            GAP();
          });
          if constexpr (n < 2) ldc(IC<n + 1>{});
          sfor<8>([&](auto i) AI { e[i] = e[i] + 1.0f; });
          GAP();
          sfor<8>([&](auto i) AI {
            e[i] = f32x2{__builtin_amdgcn_rcpf(e[i].x), __builtin_amdgcn_rcpf(e[i].y)};
            g[i] = g[i] * u[i];
            GAP();
          });
          sfor<8>([&](auto i) AI { g[i] = g[i] * e[i]; });
          GAP();
          sfor<2>([&](auto m) AI {
            const u32x4 o = {pk2(g[4 * m].x, g[4 * m].y), pk2(g[4 * m + 1].x, g[4 * m + 1].y), pk2(g[4 * m + 2].x, g[4 * m + 2].y), pk2(g[4 * m + 3].x, g[4 * m + 3].y)};
            __builtin_amdgcn_raw_buffer_store_b128(o, rws, lane * 16 + W_H + ((4 * rb + 2 * wm + m) * 86 + ks) * 1024, 0, 16);
          });
          GAP();
          if constexpr (n < 2) STAMP(20 + n);
        }
      });
      lds_set(FREE + wave, 9);
      STAMP(3);
      waitv<0>();
      __builtin_amdgcn_s_setprio(PRW);
      if (lds_arrive(CNT + 0, lane) == 4 && lane == 0) __hip_atomic_store((GAS u64*)(ws + W_HF + (rb * 16 + cg) * 64), epoch, RLX_AGENT);
      STAMP(4);
    } else {
      if (csw) {
        f32x4 cs[3];
        sfor<3>([&](auto n) AI { cs[n] = f32x4{0.f, 0.f, 0.f, 0.f}; });
        const u32 o2_ = (l0 & 15) == ((l0 >> 4) & 1) ? 0x3f803f80u : 0u;
        const bf16x8 sel = __builtin_bit_cast(bf16x8, u32x4{o2_, o2_, o2_, o2_});
        wa += wm * 6144;
#pragma unroll 1
        for (int c = 0; c < 8; ++c) {
          wait_min4<1>(FULL, c + 1);
          bf16x8 w[6];
          sfor<6>([&](auto i) AI { const u32 a = wa + (i / 3) * 3072 + (i % 3) * 32; w[i] = cat(ldtr(a), ldtr(a + 768)); });
          asm volatile("" ::: "memory");
          lds_set(FREE + wave, c < 3 ? c + 1 : 3);
          sfor<6>([&](auto i) AI { cs[i % 3] = __builtin_amdgcn_mfma_f32_16x16x32_bf16(w[i], sel, cs[i % 3], 0, 0, 0); });
          wa += c == 4 ? (u32)(-4 * (int)CSLOT) : CSLOT;
        }
        if ((l0 & 15) < 2) sfor<3>([&](auto n) AI { ldsw<f32x4>(CS + (wm * 192 + wn * 96 + n * 32 + (l0 & 15) * 16 + 4 * (l0 >> 4)) * 4, cs[n]); });
      }
      lds_set(FREE + wave, 9);
      epoch = wait_epoch();
      if (csw && wave < 3) {
        wait_min4<1>(FREE, 9);
        const u32 i = 64 * wave + l0;
        const float v = ldsr<float>(CS + i * 4) + ldsr<float>(CS + 768 + i * 4);
        __hip_atomic_store((GAS u64*)(ws + W_CG + (cg * 192 + i) * 8), ((u64)(u32)(epoch >> 17) << 32) | __float_as_uint(v), RLX_AGENT);
      }
      STAMP(4);
    }
    const int lane = lane_id(), hh = lane >> 5, g16 = lane >> 4, q4 = (lane & 15) >> 2, p4 = lane & 3;
    const int kh = wave >> 1, mh = wave & 1;
    u32x4 hf[43];
    const u32 hso = W_H + ((2 * rt + mh) * 86 + 43 * kh) * 1024, hvo = lane * 16;
    f32x16 o2[2];
    sfor<2>([&](auto n) AI { sfor<16>([&](auto r) AI { o2[n][(int)r] = 0.f; }); pin(o2[n]); });
    u32 db_[2][3];
    sfor<2>([&](auto n) AI {
      const u32 da = kh * 8192 + (4 * hh + q4) * 128 + ((4 * ((int)n ^ (q4 >> 1)) + 2 * (g16 & 1) + (p4 >> 1)) << 4) + 8 * (p4 & 1);
      sfor<3>([&](auto w) AI { db_[n][w] = da + w * 61440; pin(db_[n][w]); });
    });
    bf16x8 df[3][2];
    u32 fl;
    auto ldd = [&](auto k) AI {
      constexpr int c = k >> 2, ks = k & 3;
      constexpr u32 wb = (c < 7 ? 16384u * c : c == 7 ? CONV : c == 8 ? CONV + CH_SZ : 16384u * (c - 9)) + ks * 2048;
      constexpr int w = wb / 61440 > 2 ? 2 : wb / 61440;
      sfor<2>([&](auto n) AI { const u32 a = db_[n][w] + (wb - w * 61440); df[k % 3][n] = cat(ldtr(a), ldtr(a + 1024)); });
    };
    if (wave == 0) { poll_flags<1>(ws, W_HF + (rt >> 1) * 16 * 64, 15, epoch, lane); lds_set(CVP + 6, 1); }
    else wait_byte<WSL>(CVP + 6, 1);
    __builtin_amdgcn_s_setprio(2);
    STAMP(5);
    auto hload = [&](auto k) AI { bld4_sc1<(k & 3) * 1024>(hf[k], hvo, rws, hso + (k >> 2) * 4096); };
    sfor<PD>([&](auto k) AI { hload(k); });
    wait_min4<0>(WDRDY, 1);
    STAMP(6);
    ldd(IC<0>{}); ldd(IC<1>{});
    GAP();
    sfor<43>([&](auto k) AI {
      if constexpr (k == 10) lds_set(FREE + wave, 10);
      if constexpr (k == 18) lds_set(FREE + wave, 11);
      if constexpr (k == 20 || k == 32) fl = lds_ld1(WDRDY);
      if constexpr (k == 40) fl = lds_ld1(FREE);
      if constexpr (k == 22 || k == 34) { STAMP(k == 22 ? 12 : 14); if (__builtin_expect(!all_ge(rfl(fl), k == 22 ? 2u : 3u), 0)) wait_min4<0>(WDRDY, k == 22 ? 2 : 3); STAMP(k == 22 ? 13 : 15); GAP(); }
      constexpr int younger = (k + PD <= 43 ? PD : 43 - k) - 1;
      waitv_pin<younger>(hf[k]);
      if constexpr (k == 0) STAMP(24);
      if constexpr (k == 8) STAMP(25);
      if constexpr (k == 16) STAMP(26);
      o2[0] = __builtin_amdgcn_mfma_f32_32x32x16_bf16(df[k % 3][0], __builtin_bit_cast(bf16x8, hf[k]), o2[0], 0, 0, 0);
      if constexpr (k + 2 < 43) ldd(IC<k + 2>{});
      GAP();
      o2[1] = __builtin_amdgcn_mfma_f32_32x32x16_bf16(df[k % 3][1], __builtin_bit_cast(bf16x8, hf[k]), o2[1], 0, 0, 0);
      if constexpr (k + PD < 43) hload(IC<k + PD>{});
      GAP();
    });
    STAMP(7);
    if (!all_ge(rfl(fl), 11u)) wait_min4<0>(FREE, 11);
    sfor<2>([&](auto n) AI { sfor<4>([&](auto g) AI {
      f32x4 v = {o2[n][4 * g], o2[n][4 * g + 1], o2[n][4 * g + 2], o2[n][4 * g + 3]};
      ldsw<f32x4>(PART + wave * 8192 + n * 4096 + g * 1024 + lane * 16, v);
    }); });
    waitl();
    lds_set(FREE + wave, 12);
    STAMP(8);
    DONE();
  } else {
    __builtin_amdgcn_s_setprio(PRL);
    const rsrc_t rg = mk_rsrc(mkp(ka[2], ka[3]), D * F * 2), ru = mk_rsrc(mkp(ka[4], ka[5]), D * F * 2);
    const int crot = ROT * ((rb + cg) & 1);
    auto chx = [&](int c, u32 sb) AI { dma4(sb + 4 * j * 1024, xv[0], xv[1], xv[2], xv[3], rq, (rb * 128 + 32 * j) * 1024 + (c ^ crot) * 128); };
    auto chw = [&](int c, u32 sb) AI {
      dma3(sb + G_OFF + 3 * j * 1024, wv[0], wv[1], wv[2], rg, (c ^ crot) * 64 * F * 2);
      dma3(sb + U_OFF + 3 * j * 1024, wv[0], wv[1], wv[2], ru, (c ^ crot) * 64 * F * 2);
    };
    if (has) {
      chx(0, 0); chw(0, 0); chx(1, SLOT); chw(1, SLOT);
      int ic = 2, lc = 0;
      u32 sb = 2 * SLOT;
#pragma unroll 1
      for (u32 it = 0; it < SPIN_CAP * 16; ++it) {
        if (lc < ic && vm_pending() <= 10u * (u32)(ic - lc - 1)) {
          asm volatile("" ::: "memory");
          ++lc;
          lds_set(FULL + j, lc == 8 ? 9 : lc);
          if (lc == 1) STAMP(1);
          if (lc == 8) break;
        } else if (ic < 8 && all_ge(rfl(lds_ld1(FREE)), ic - 2)) {
          asm volatile("" ::: "memory");
          chx(ic, sb);
          if (lc < ic && vm_pending() <= 10u * (u32)(ic - lc - 1) + 4u) {
            lds_set(FULL + j, ++lc);
          }
          chw(ic, sb);
          ++ic;
          sb = sb == 2 * SLOT ? 0 : sb + SLOT;
        } else {
          __builtin_amdgcn_s_sleep(LSLEEP);
        }
      }
      waitv<0>();
    } else if (csw) {
      sfor<8>([&](auto c) AI {
        if constexpr (c >= 5) { waitv<24>(); lds_set(FULL + j, (6u * c - vm_pending()) * 43u >> 8); wait_min4<1>(FREE, c - 4); }
        chw(c, (c % 5) * CSLOT - G_OFF);
        if constexpr (c >= 2) lds_set(FULL + j, (6u * (c + 1) - vm_pending()) * 43u >> 8);
      });
      waitv<12>(); lds_set(FULL + j, 6);
      waitv<6>(); lds_set(FULL + j, 7);
      waitv<0>(); lds_set(FULL + j, 8);
    }
    STAMP(2);
    const rsrc_t rd = mk_rsrc(mkp(kb[0], kb[1]), F * D * 2);
    u32 dv[4];
    sfor<4>([&](auto t) AI { dv[t] = (7 * t + l8) * 1024 + ((c8 ^ (((l8 >> 1) & 1) << 2)) << 4) + ct * 128; });
    auto wso = [&](int g) AI { return (u32)(((g >> 1) & 1) * 688 + (g >> 2) * 64 + (g & 1) * 32) * 1024u; };
    auto wd_groups = [&](int lo, int hi) AI {
#pragma unroll 1
      for (int g = lo + j; g < hi; g += 4) { dma4(g * 4096, dv[0], dv[1], dv[2], dv[3], rd, wso(g)); waitv<WDFL>(); }
    };
    u32x4 wr[5][4];
    auto wst = [&](auto t, u32 a) AI { sfor<4>([&](auto i) AI { pin(wr[t][i]); ldsw<u32x4>(a + j * 4096 + i * 1024 + lane * 16, wr[t][i]); }); };
    sfor<5>([&](auto t) AI { const u32 so = wso(24 + 4 * t + j); sfor<4>([&](auto i) AI { bld4o<i * 1024>(wr[t][i], dv[i], rd, so); }); });
    wait_min4<1>(FREE, WD1);
    wd_groups(21, 24);
    wait_min4<1>(FREE, WD2);
    wd_groups(0, 10);
    wait_min4<1>(FREE, WD3);
    wd_groups(10, 21);
    waitv<0>();
    lds_set(WDRDY + j, 1);
    STAMP(3);
    wait_min4<4>(CVP, 5);
    wst(IC<0>{}, XCH); wst(IC<1>{}, CONV); wst(IC<2>{}, CONV + CH_SZ);
    waitl();
    lds_set(WDRDY + j, 2);
    STAMP(4);
    wait_min4<2>(FREE, 10);
    wst(IC<3>{}, 0); wst(IC<4>{}, 16384);
    waitl();
    lds_set(WDRDY + j, 3);
    STAMP(5);
    DONE();
  }
}

extern "C" void kernel_launch(void* const* d_in, const int* in_sizes, int n_in, void* d_out, int out_size, void* d_ws, size_t ws_size,
                              hipStream_t stream) {
  Args a;
  a.q = (const u16*)d_in[0]; a.k = (const u16*)d_in[1]; a.v = (const u16*)d_in[2]; a.sk = (const u16*)d_in[3];
  a.h = (const u16*)d_in[4]; a.dv = (const u16*)d_in[5];
  a.wg = (const u16*)d_in[7]; a.wu = (const u16*)d_in[8]; a.wd = (const u16*)d_in[9];
  a.out = (u16*)d_out; a.ws = (char*)d_ws; a.pad = 0;
  (void)hipFuncSetAttribute((const void*)fused, hipFuncAttributeMaxDynamicSharedMemorySize, LDS_BYTES);
  fused<<<256, 768, LDS_BYTES, stream>>>(a);
}
```

```cpp
#define WRLAST 9
#include <hip/hip_runtime.h>
#include <stdint.h>
#include <utility>

typedef unsigned short u16;
typedef unsigned int u32;
typedef unsigned long long u64;
typedef __bf16 bf16x8 __attribute__((ext_vector_type(8)));
typedef __bf16 bf16x2 __attribute__((ext_vector_type(2)));
typedef short s16x4 __attribute__((ext_vector_type(4)));
typedef short s16x8 __attribute__((ext_vector_type(8)));
typedef float f32x16 __attribute__((ext_vector_type(16)));
typedef float f32x4 __attribute__((ext_vector_type(4)));
typedef float f32x2 __attribute__((ext_vector_type(2)));
typedef u32 u32x4 __attribute__((ext_vector_type(4)));
typedef u32 u32x2 __attribute__((ext_vector_type(2)));
typedef __amdgpu_buffer_rsrc_t rsrc_t;
#define DI __device__ __forceinline__
#define AI __attribute__((always_inline))
#define LAS __attribute__((address_space(3)))
#define GAS __attribute__((address_space(1)))
#define RLX_AGENT __ATOMIC_RELAXED, __HIP_MEMORY_SCOPE_AGENT
#define RLX_WG __ATOMIC_RELAXED, __HIP_MEMORY_SCOPE_WORKGROUP

constexpr int S = 2048, D = 512, F = 1376;
#define UPAD 32
#define SLOTB (40960 + UPAD)
constexpr u32 SLOT = SLOTB, G_OFF = 16384, U_OFF = 16384 + 12288 + UPAD;
constexpr u32 CSLOT = 2 * (12288 + UPAD) + 64 - 2 * UPAD % 64;
constexpr u32 CONV = 3 * 41216;
constexpr u32 CH_SZ = 17520, HA = 64, HB = 4292, KA = 8400, KB = 12976, XB = 4304;
constexpr u32 MISC = CONV + 2 * CH_SZ;
constexpr u32 CS = MISC, ST = MISC + 1536, CNT = MISC + 2560;
constexpr u32 CKV = CNT + 8, CSP = CNT + 12, EPO = CNT + 16, CVP = CNT + 24, FULL = CNT + 32, FREE = CNT + 36, STRDY = CNT + 40, WDRDY = CNT + 44;
constexpr u32 LDS_BYTES = MISC + 2816;
static_assert(LDS_BYTES <= 163840, "LDS");
constexpr u32 XCH = 98304, ZST = 114688, PART = 32768;
constexpr u32 W_E = 0, W_PF = 4096, W_HF = W_PF + 16384, W_ZF = W_HF + 16384, W_SG = W_ZF + 16384, W_CG = W_SG + 16384;
constexpr u32 W_KV = 131072, W_ZT = W_KV + 2097152 + 4096, W_H = W_ZT + 4194304, W_END = W_H + 64 * 86 * 1024;
constexpr u32 W_DBG = 16u << 20;
constexpr u32 CODE_BYTES = 26112;
#define PRL 1
#define PRS 0
#define CONVAFTER 9
#define LSLEEP 1
#define PRA 0
#define ROT 0
#define S2AFTER 8
#define WDFL 2
#define KVSTAG 0
#define SSTOP 9
#define POLLS 4
#define PD 10
#define WD1 6
#define WD2 7
#define PFLO 8
#define PFPRIO 0
#define PFWAIT 0
#define KVMODE 0
#define WD3 8
#define PRE 2
#define PRW 0
#define WSL 1
#define PRC 3
#define ZSLEEP 4
#define ZAFTER 11
#define WRFL 40
#define KVAFTER 1
constexpr u32 SPIN_CAP = 1u << 18;

constexpr int cpair(int g) {
  int i = 0;
  for (int n = 0; n < 8; ++n) for (int m = 0; m <= n; ++m) if (m + n <= 7) {
    if (i++ == g) return n * 8 + m;
    if (m < n && i++ == g) return m * 8 + n;
  }
  return 0;
}
template <class Fn, int... I> DI void sfor_impl(Fn&& f, std::integer_sequence<int, I...>) { (f(std::integral_constant<int, I>{}), ...); }
template <int N, class Fn> DI void sfor(Fn&& f) { sfor_impl(f, std::make_integer_sequence<int, N>{}); }

DI rsrc_t mk_rsrc(const void* p, u32 bytes) { return __builtin_amdgcn_make_buffer_rsrc((void*)p, (short)0, (int)bytes, 0x00020000); }
DI void dma(u32 lds, u32 voff, rsrc_t r, u32 soff) {
  asm volatile("s_mov_b32 m0, %0\n\ts_nop 0\n\tbuffer_load_dwordx4 %1, %2, %3 offen lds" ::"s"(lds), "v"(voff), "s"(r), "s"(soff) : "memory");
}

DI void dma4(u32 lds, u32 v0, u32 v1, u32 v2, u32 v3, rsrc_t r, u32 soff) {
  asm volatile("s_mov_b32 m0, %0\n\ts_nop 0\n\tbuffer_load_dwordx4 %1, %5, %6 offen lds\n\tbuffer_load_dwordx4 %2, %5, %6 offen offset:1024 lds\n\t"
               "buffer_load_dwordx4 %3, %5, %6 offen offset:2048 lds\n\tbuffer_load_dwordx4 %4, %5, %6 offen offset:3072 lds"
               ::"s"(lds), "v"(v0), "v"(v1), "v"(v2), "v"(v3), "s"(r), "s"(soff) : "memory");
}
DI void dma3(u32 lds, u32 v0, u32 v1, u32 v2, rsrc_t r, u32 soff) {
  asm volatile("s_mov_b32 m0, %0\n\ts_nop 0\n\tbuffer_load_dwordx4 %1, %4, %5 offen lds\n\tbuffer_load_dwordx4 %2, %4, %5 offen offset:1024 lds\n\t"
               "buffer_load_dwordx4 %3, %4, %5 offen offset:2048 lds"
               ::"s"(lds), "v"(v0), "v"(v1), "v"(v2), "s"(r), "s"(soff) : "memory");
}
template <int OFF> DI void bld4_sc1(u32x4& d, u32 voff, rsrc_t r, u32 soff) {
  asm volatile("buffer_load_dwordx4 %0, %1, %2, %3 offen offset:%4 sc1" : "=v"(d) : "v"(voff), "s"(r), "s"(soff), "i"(OFF));
}
template <int OFF> DI void bld4o(u32x4& d, u32 voff, rsrc_t r, u32 soff) { asm volatile("buffer_load_dwordx4 %0, %1, %2, %3 offen offset:%4" : "=v"(d) : "v"(voff), "s"(r), "s"(soff), "i"(OFF)); }
DI void bld4(u32x4& d, u32 voff, rsrc_t r, u32 soff) { asm volatile("buffer_load_dwordx4 %0, %1, %2, %3 offen" : "=v"(d) : "v"(voff), "s"(r), "s"(soff)); }
template <int OFF> DI void bld2(u32x2& d, u32 voff, rsrc_t r) { asm volatile("buffer_load_dwordx2 %0, %1, %2, 0 offen offset:%3" : "=v"(d) : "v"(voff), "s"(r), "i"(OFF)); }
template <int OFF> DI void gld4_sc1(u32x4& d, u32 voff, const char* base) {
  asm volatile("global_load_dwordx4 %0, %1, %2 offset:%3 sc1" : "=v"(d) : "v"(voff), "s"(base), "i"(OFF));
}
template <int N> DI void waitv_pin(u32x4& d) { asm volatile("s_waitcnt vmcnt(%1)" : "+v"(d) : "i"(N)); }
template <int N> DI void waitv() { asm volatile("s_waitcnt vmcnt(%0)" ::"i"(N) : "memory"); }
DI u32 vm_pending() { u32 r; asm volatile("s_getreg_b32 %0, hwreg(HW_REG_IB_STS)" : "=s"(r)::"memory"); return (r & 15u) | ((r >> 18) & 48u); }
DI void bar() { asm volatile("s_waitcnt lgkmcnt(0)\n\ts_barrier" ::: "memory"); }
DI void waitl() { asm volatile("s_waitcnt lgkmcnt(0)" ::: "memory"); }
DI float bflo(u32 x) { return __uint_as_float(x << 16); }
DI float bfhi(u32 x) { return __uint_as_float(x & 0xffff0000u); }
DI u32 pk2(float lo, float hi) { f32x2 v = {lo, hi}; return __builtin_bit_cast(u32, __builtin_convertvector(v, bf16x2)); }
DI u32 pk2s(float lo, float hi) { asm("" : "+v"(lo)); asm("" : "+v"(hi)); return pk2(lo, hi); }
DI float rbf(float x) { return bflo(pk2(x, x)); }
template <class T> DI T ldsr(u32 a) { return *(const LAS T*)a; }
template <class T> DI void ldsw(u32 a, T v) { *(LAS T*)a = v; }
DI u32x2 ldtr(u32 a) { return __builtin_bit_cast(u32x2, __builtin_amdgcn_ds_read_tr16_b64_v4i16((LAS s16x4*)a)); }
DI bf16x8 cat(u32x2 a, u32x2 b) { u32x4 v = {a.x, a.y, b.x, b.y}; return __builtin_bit_cast(bf16x8, v); }
template <int CTRL, int RM> DI float dpp_add(float v) {
  return v + __builtin_bit_cast(float, __builtin_amdgcn_update_dpp(0, __builtin_bit_cast(int, v), CTRL, RM, 0xf, false));
}
DI float wave_sum(float v) {
  v = dpp_add<0x111, 0xf>(v); v = dpp_add<0x112, 0xf>(v); v = dpp_add<0x114, 0xf>(v); v = dpp_add<0x118, 0xf>(v);
  v = dpp_add<0x142, 0xa>(v); v = dpp_add<0x143, 0xc>(v);
  return __builtin_bit_cast(float, __builtin_amdgcn_readlane(__builtin_bit_cast(int, v), 63));
}
DI void wave_sum2(float& a, float& c) {
  a = dpp_add<0x111, 0xf>(a); c = dpp_add<0x111, 0xf>(c); a = dpp_add<0x112, 0xf>(a); c = dpp_add<0x112, 0xf>(c);
  a = dpp_add<0x114, 0xf>(a); c = dpp_add<0x114, 0xf>(c); a = dpp_add<0x118, 0xf>(a); c = dpp_add<0x118, 0xf>(c);
  a = dpp_add<0x142, 0xa>(a); c = dpp_add<0x142, 0xa>(c); a = dpp_add<0x143, 0xc>(a); c = dpp_add<0x143, 0xc>(c);
  a = __builtin_bit_cast(float, __builtin_amdgcn_readlane(__builtin_bit_cast(int, a), 63));
  c = __builtin_bit_cast(float, __builtin_amdgcn_readlane(__builtin_bit_cast(int, c), 63));
}
DI void gld8_sc1(u64& d, const char* p) { asm volatile("global_load_dwordx2 %0, %1, off sc1" : "=v"(d) : "v"(p)); }
DI void gld_u16(u32& d, u32 voff, const char* base) { asm volatile("global_load_ushort %0, %1, %2" : "=v"(d) : "v"(voff), "s"(base)); }
template <int SLEEP> DI void poll_flags(const char* ws, u32 off, int n, u64 epoch, int lane) {
  const GAS u64* p = (const GAS u64*)(ws + off + (u32)(lane < n ? lane : 0) * 64u);
#pragma unroll 1
  for (u32 it = 0; it < SPIN_CAP; ++it) {
    u64 v = __hip_atomic_load(p, RLX_AGENT);
    if (__all(v == epoch)) break;
    __builtin_amdgcn_s_sleep(SLEEP);
  }
  asm volatile("" ::: "memory");
}
DI u32 poll_granule(const char* ws, u32 row, u32 tag) {
  const GAS u64* p = (const GAS u64*)(ws + W_SG + row * 8u);
  u64 v = 0;
#pragma unroll 1
  for (u32 it = 0; it < SPIN_CAP; ++it) {
    v = __hip_atomic_load(p, RLX_AGENT);
    if (__all((u32)(v >> 32) == tag)) break;
    __builtin_amdgcn_s_sleep(1);
  }
  return (u32)v;
}
DI u32 lds_arrive(u32 a, int lane) {
  u32 r = 0;
  waitl();
  if (lane == 0) r = __hip_atomic_fetch_add((LAS u32*)a, 1u, RLX_WG);
  return (u32)__builtin_amdgcn_readfirstlane(r) + 1u;
}
DI void lds_wait(u32 a, u32 want) {
#pragma unroll 1
  for (u32 it = 0; it < SPIN_CAP * 16; ++it) {
    u32 v = __hip_atomic_load((LAS u32*)a, RLX_WG);
    if ((u32)__builtin_amdgcn_readfirstlane(v) >= want) break;
  }
  asm volatile("" ::: "memory");
}

DI int lane_id() { int l; asm volatile("v_mbcnt_lo_u32_b32 %0, -1, 0\n\tv_mbcnt_hi_u32_b32 %0, -1, %0" : "=v"(l)); return l; }
DI u32 rfl(u32 x) { return (u32)__builtin_amdgcn_readfirstlane(x); }
DI u32x2 lds_ld2(u32 a) { return __builtin_bit_cast(u32x2, __hip_atomic_load((LAS u64*)a, RLX_WG)); }
template <int SLEEP> DI void wait_min2(u32 a, u32 want) {
#pragma unroll 1
  for (u32 it = 0; it < SPIN_CAP * 16; ++it) {
    const u32x2 v = lds_ld2(a);
    if (rfl(min(v.x, v.y)) >= want) break;
    if (SLEEP) __builtin_amdgcn_s_sleep(SLEEP);
  }
  asm volatile("" ::: "memory");
}
DI u32 lds_ld1(u32 a) { return __hip_atomic_load((LAS u32*)a, RLX_WG); }
DI bool all_ge(u32 w, u32 want) { return ((w - want * 0x01010101u) & 0x80808080u) == 0u; }
template <int SLEEP> DI void wait_min4(u32 a, u32 want) {
#pragma unroll 1
  for (u32 it = 0; it < SPIN_CAP * 16; ++it) {
    if (all_ge(rfl(lds_ld1(a)), want)) break;
    if (SLEEP) __builtin_amdgcn_s_sleep(SLEEP);
  }
  asm volatile("" ::: "memory");
}
template <int SLEEP> DI void wait_byte(u32 a, u32 want) {
#pragma unroll 1
  for (u32 it = 0; it < SPIN_CAP * 16; ++it) {
    if (rfl(__hip_atomic_load((LAS unsigned char*)a, RLX_WG)) >= want) break;
    if (SLEEP) __builtin_amdgcn_s_sleep(SLEEP);
  }
  asm volatile("" ::: "memory");
}
DI u64 wait_epoch() {
  u32x2 e;
#pragma unroll 1
  for (u32 it = 0; it < SPIN_CAP * 16; ++it) {
    e = lds_ld2(EPO);
    if (rfl(e.x) != 0u) break;
    __builtin_amdgcn_s_sleep(2);
  }
  return ((u64)rfl(e.y) << 32) | rfl(e.x);
}
DI u64 gld_agent(const char* p) { return __hip_atomic_load((const GAS u64*)p, RLX_AGENT); }
DI void lds_set(u32 a, u32 v) { __hip_atomic_store((LAS unsigned char*)a, (unsigned char)v, RLX_WG); }
DI void gather(u32& junk, u32 voff, rsrc_t r, u32 soff) { asm volatile("buffer_load_dword %0, %1, %2, %3 offen" : "=v"(junk) : "v"(voff), "s"(r), "s"(soff)); }
template <int N> using IC = std::integral_constant<int, N>;

#define DONE() do {} while (0)
#define STAMP(id) do {} while (0)

struct Args { const u16 *q, *wg, *wu, *k, *v, *sk, *h, *dv, *wd; u16* out; char* ws; u64 pad; };
typedef u32 u32x16 __attribute__((ext_vector_type(16)));
typedef u32 u32x8 __attribute__((ext_vector_type(8)));
DI char* mkp(u32 lo, u32 hi) { return (char*)(((u64)hi << 32) | lo); }
#define GAP() __builtin_amdgcn_sched_barrier(0)
DI void barx() { asm volatile("s_barrier" ::: "memory"); }
template <class T> DI void spin(T& x) { asm volatile("" : "+s"(x)); }
template <class T> DI void pin(T& x) { asm volatile("" : "+v"(x)); }

__global__ void __launch_bounds__(768, 3) fused(Args) {
  u64 pc0; asm volatile("s_getpc_b64 %0" : "=s"(pc0));
  u32x16 ka; u32x8 kb;
  asm volatile("s_load_dwordx16 %0, %2, 0x0\n\ts_load_dwordx8 %1, %2, 0x40" : "=&s"(ka), "=&s"(kb) : "s"(__builtin_amdgcn_kernarg_segment_ptr()));
  GAP();
  const int tid = threadIdx.x, lane = tid & 63;
  const int wave = __builtin_amdgcn_readfirstlane(tid >> 6);
  const int b = blockIdx.x, xcd = b & 7, li = b >> 3;
  if (tid < 16) ldsw<u32>(CNT + tid * 4, 0u);
  const bool has = li < 30;
  const int id = 30 * xcd + (has ? li : 0), rr_ = id % 120, rb = 8 * (id / 120) + (rr_ & 7);
  const int cg = has ? rr_ >> 3 : 4 * (xcd & 3) + 2 * (xcd >> 2) + (li - 30);
  const bool csw = !has && cg < 15;
  const int rt = 4 * xcd + (li & 3), ct = li >> 2;
  const int j = wave & 3, l8 = lane >> 3, c8 = lane & 7;
  u32 xv[4], wv[3];
  sfor<4>([&](auto t) AI { xv[t] = (7 * t + l8) * 1024 + ((c8 ^ ((4 * t + (l8 >> 1)) & 7)) << 4); pin(xv[t]); });
  sfor<3>([&](auto t) AI {
    const u32 byte = t * 1024 + lane * 16, row = byte / 192u;
    const u32 seg = byte - row * 192u;
    wv[t] = cg * 192 + seg >= F * 2 ? 0x40000000u : (16 * j + row) * (F * 2) + cg * 192 + seg - t * 1024;
    pin(wv[t]);
  });
  asm volatile("s_waitcnt lgkmcnt(0)\n\ts_barrier" : "+s"(ka), "+s"(kb)::"memory");
  STAMP(9);
  char* const ws = mkp(kb[4], kb[5]);
  const rsrc_t rq = mk_rsrc(mkp(ka[0], ka[1]), S * D * 2);
  if (wave >= 8) {
    __builtin_amdgcn_s_setprio(3);
    u64 epoch;
    asm volatile("s_load_dwordx2 %0, %1, 0x0" : "=s"(epoch) : "s"(ws));
    const int lane = lane_id(), l8 = lane >> 3, c8 = lane & 7;
    const int ch = j & 1, cp = j >> 1, c16 = lane & 15, q = lane >> 4;
    const rsrc_t rws = mk_rsrc(ws, W_END);
    const u32 cb = CONV + ch * CH_SZ, hown = cb + (cp ? XB : HA), kown = cb + (cp ? KB : KA);
    const int srow = 256 * xcd + 8 * li + 2 * j;
    if (SSTOP == 0) wait_min4<8>(FREE, 8);
    u32x4 sx[2];
    sfor<2>([&](auto r) AI { bld4(sx[r], lane * 16, rq, (srow + r) * 1024); });
    STAMP(23);
    const int pa = b & 31, pg = b >> 5, s0 = 64 * pa + 16 * j;
    u32x2 skw[2];
    u32 dvv;
    auto kvload = [&]() AI {
      const rsrc_t rk = mk_rsrc(mkp(ka[6], ka[7]), S * D * 2), rv = mk_rsrc(mkp(ka[8], ka[9]), S * D * 2), rs_ = mk_rsrc(mkp(ka[10], ka[11]), D * 8);
      const u32 kvv = l8 * 1024 + c8 * 16 + pg * 128;
      sfor<3>([&](auto t) AI { const int r = s0 - 8 + 8 * t; dma(hown + t * 1024, kvv, rk, (r < 0 ? 0 : r) * 1024); });
      sfor<2>([&](auto t) AI { dma(kown + t * 1024, kvv, rv, (s0 + 8 * t) * 1024); });
      sfor<2>([&](auto t) AI { bld2<256 * t>(skw[t], (64 * pg + 16 * (q >> 1) + c16) * 8, rs_); });
      gld_u16(dvv, (2 * b + ch) * 2, mkp(ka[14], ka[15]));
    };
    asm volatile("s_waitcnt lgkmcnt(0)" : "+s"(epoch));
    epoch += 0x9E3779B97F4A7C15ull;
    epoch = (epoch ^ (epoch >> 30)) * 0xBF58476D1CE4E5B9ull; epoch = (epoch ^ (epoch >> 27)) * 0x94D049BB133111EBull;
    epoch = (epoch ^ (epoch >> 31)) | 1ull;
    ldsw<u64>(EPO, epoch);
    STAMP(18);
    const u32 tag = (u32)(epoch >> 17);
    if (has) wait_min4<2>(FULL, KVAFTER + KVSTAG * (li & 3));
    STAMP(19);
    kvload();
    STAMP(20);
    asm volatile("s_waitcnt vmcnt(8)" : "+v"(sx[0]), "+v"(sx[1]));
    STAMP(21);
    __builtin_amdgcn_s_setprio(0);
    {
      float x[2][8], mean[2], var[2];
      sfor<2>([&](auto r) AI {
        sfor<4>([&](auto i) AI { x[r][2 * i] = bflo(sx[r][(int)i]); x[r][2 * i + 1] = bfhi(sx[r][(int)i]); });
        mean[r] = x[r][0];
        sfor<7>([&](auto i) AI { mean[r] += x[r][i + 1]; });
      });
      wave_sum2(mean[0], mean[1]);
      sfor<2>([&](auto r) AI {
        mean[r] *= 1.0f / D; var[r] = 0.f;
        sfor<8>([&](auto i) AI { float d = x[r][i] - mean[r]; asm("" : "+v"(d)); var[r] += d * d; });
      });
      wave_sum2(var[0], var[1]);
      STAMP(22);
      const float mn = lane ? mean[1] : mean[0], vr = rbf((lane ? var[1] : var[0]) * (1.0f / D));
      const u32 g = pk2s(mn, __builtin_sqrtf(rbf(vr + 1e-5f)));
      if (lane < 2) __hip_atomic_store((GAS u64*)(ws + W_SG + (srow + lane) * 8), ((u64)tag << 32) | g, RLX_AGENT);
    }
    STAMP(1);
    asm volatile("s_waitcnt vmcnt(1)" : "+v"(skw[0]), "+v"(skw[1]), "+v"(dvv)::"memory");
    STAMP(2);
    {
      const bool first = s0 == 0 && (q & 1) == 0;
      u32x2 kr[2][5];
      sfor<2>([&](auto gi) AI {
        const u32 a = (8 * (q & 1) + (c16 >> 2)) * 128 + 64 * gi + 32 * (q >> 1) + 8 * (lane & 3);
        kr[gi][0] = ldtr(hown + a + 512); kr[gi][1] = ldtr(hown + a + 1024); kr[gi][2] = ldtr(hown + a + 1536); kr[gi][3] = ldtr(kown + a); kr[gi][4] = ldtr(kown + a + 512);
      });
      GAP();
      if (SSTOP == 1) wait_min4<8>(FREE, 8);
      sfor<2>([&](auto gi) AI {
        const float w0 = bflo(skw[gi].x), w1 = bfhi(skw[gi].x), w2 = bflo(skw[gi].y), w3 = bfhi(skw[gi].y);
        const u32x2 k0 = kr[gi][0], k1 = kr[gi][1], k2 = kr[gi][2], v0 = kr[gi][3], v1 = kr[gi][4];
        const float kk[11] = {first ? 0.f : bfhi(k0.x), first ? 0.f : bflo(k0.y), first ? 0.f : bfhi(k0.y), bflo(k1.x), bfhi(k1.x), bflo(k1.y), bfhi(k1.y), bflo(k2.x), bfhi(k2.x), bflo(k2.y), bfhi(k2.y)};
        const float vv[8] = {bflo(v0.x), bfhi(v0.x), bflo(v0.y), bfhi(v0.y), bflo(v1.x), bfhi(v1.x), bflo(v1.y), bfhi(v1.y)};
        u32 a4[4], m4[4];
        sfor<4>([&](auto t) AI { a4[t] = pk2s(w3 * kk[2 * t], w3 * kk[2 * t + 1]); m4[t] = pk2s(w2 * kk[2 * t + 1], w2 * kk[2 * t + 2]); });
        GAP();
        sfor<4>([&](auto t) AI { a4[t] = pk2s(bflo(a4[t]) + bflo(m4[t]), bfhi(a4[t]) + bfhi(m4[t])); m4[t] = pk2s(w1 * kk[2 * t + 2], w1 * kk[2 * t + 3]); });
        GAP();
        sfor<4>([&](auto t) AI { a4[t] = pk2s(bflo(a4[t]) + bflo(m4[t]), bfhi(a4[t]) + bfhi(m4[t])); m4[t] = pk2s(w0 * kk[2 * t + 3], w0 * kk[2 * t + 4]); });
        GAP();
        sfor<4>([&](auto t) AI { a4[t] = pk2s(bflo(a4[t]) + bflo(m4[t]), bfhi(a4[t]) + bfhi(m4[t])); });
        GAP();
        u32x4 ov;
        sfor<4>([&](auto t) AI { ov[3 - t] = pk2s(bfhi(a4[t]) * vv[2 * t + 1], bflo(a4[t]) * vv[2 * t]); });
        const int dg = 2 * gi + (q >> 1);
        ldsw<u32x4>(CONV + (dg & 1) * CH_SZ + (dg >> 1 ? KB : KA) + 2048 + c16 * 128 + (((7 - 2 * j - (q & 1)) ^ (c16 & 7)) << 4), ov);
      });
    }
    waitl();
    lds_set(CVP + j, 1);
    __builtin_amdgcn_s_setprio(3);
    STAMP(17);
    wait_min4<0>(CVP, 1);
    {
      u32x4 o[2];
      sfor<2>([&](auto t) AI { o[t] = ldsr<u32x4>(kown + 2048 + (8 * t + l8) * 128 + ((c8 ^ l8) << 4)); });
      sfor<2>([&](auto t) AI { __builtin_amdgcn_raw_buffer_store_b128(o[t], rws, W_KV + (64 * pg + 16 * j + 8 * t + l8) * 4096 + (1984 - 64 * pa) * 2 + c8 * 16, 0, 16); });
    }
    const int mh = j & 1, db = j >> 1, s31 = lane & 31, hh = lane >> 5;
    const int orow = 64 * rt + 32 * mh + s31, ocol = 64 * ct + 32 * db;
    u32x2 xr[4];
    u32 sg;
    {
      const char* p1 = ws + W_SG + (hh ? orow : rb * 128 + 32 * j + s31) * 8;
      const char* p2 = ws + W_CG + ((has ? cg : 0) * 192 + 48 * j + (lane < 48 ? lane : 0)) * 8;
      u64 g1, g2;
      gld8_sc1(g1, p1); gld8_sc1(g2, p2);
      asm volatile("s_waitcnt vmcnt(0)" : "+v"(g1), "+v"(g2)::"memory");
      if (lds_arrive(CKV, lane) == 4 && lane == 0) __hip_atomic_store((GAS u64*)(ws + W_PF + b * 64), epoch, RLX_AGENT);
      STAMP(3);
#pragma unroll 1
      for (u32 it = 0; it < SPIN_CAP; ++it) {
        if (__all((u32)(g1 >> 32) == tag && (u32)(g2 >> 32) == tag)) break;
        __builtin_amdgcn_s_sleep(4);
        g1 = gld_agent(p1); g2 = gld_agent(p2);
      }
      sg = (u32)g1;
      if (has) {
        if (lane < 32) ldsw<f32x2>(ST + (32 * j + lane) * 8, f32x2{bflo(sg), __builtin_amdgcn_rcpf(bfhi(sg))});
        if (lane < 48) ldsw<u32>(CS + (48 * j + lane) * 4, (u32)g2);
      }
      lds_set(STRDY + j, 1);
      STAMP(4);
    }
    if (has) wait_min4<4>(FULL, S2AFTER);
    STAMP(24);
    {
      u64 f;
      gld8_sc1(f, ws + W_PF + ((b >> 5) * 32 + (j == 0 ? s31 : 0)) * 64);
      dma4(cb + (cp ? HB : HA), lane * 16, lane * 16, lane * 16, lane * 16, mk_rsrc(mkp(ka[12], ka[13]), D * S * 2), (2 * b + ch) * 4096);
      if (lane < (cp ? 33 : 16)) ldsw<u32>(cb + (cp ? 4160 : 0) + lane * 4, 0u);
      if (lane < (cp ? 28 : 30)) ldsw<u32x4>(kown + 4096 + lane * 16, u32x4{0u, 0u, 0u, 0u});
      asm volatile("s_waitcnt vmcnt(4)" : "+v"(f));
      sg = (u32)__builtin_amdgcn_permlane32_swap(sg, sg, false, false)[1];
      if (j == 0) { if (!__all(f == epoch)) poll_flags<2>(ws, W_PF + (b >> 5) * 32 * 64, 32, epoch, lane); lds_set(CVP + 4, 1); }
      wait_byte<1>(CVP + 4, 1);
      u32x4 kvr[4];
      const char* src = ws + W_KV + (2 * b + ch) * 4096 + 2 * cp;
      sfor<4>([&](auto t) AI { gld4_sc1<t * 1024>(kvr[t], lane * 16, src); });
      wait_min4<1>(FREE, 6);
      sfor<2>([&](auto t) AI {
        const int r = 16 * t + (lane >> 2);
        dma(ZST + j * 2048 + t * 1024, r * 1024 + (((lane & 3) ^ ((r >> 1) & 3)) << 4), rq, (64 * rt + 32 * mh) * 1024 + ocol * 2);
      });
      asm volatile("s_waitcnt vmcnt(0)" : "+v"(kvr[0]), "+v"(kvr[1]), "+v"(kvr[2]), "+v"(kvr[3])::"memory");
      if (cp && lane == 63) kvr[3].w &= 0xffffu;
      sfor<4>([&](auto t) AI { ldsw<u32x4>(kown + t * 1024 + lane * 16, kvr[t]); });
      sfor<4>([&](auto g) AI { xr[g] = ldsr<u32x2>(ZST + j * 2048 + s31 * 64 + ((g ^ ((s31 >> 1) & 3)) << 4) + 8 * hh); });
      waitl();
      pin(xr[0]); pin(xr[1]); pin(xr[2]); pin(xr[3]);
      lds_set(CVP + j, 2);
      STAMP(5);
    }
    __builtin_amdgcn_s_setprio(0);
    wait_min4<1>(FREE, CONVAFTER);
    wait_byte<0>(CVP + (j ^ 2), 2);
    __builtin_amdgcn_s_setprio(PRC);
    STAMP(6);
    f32x4 dacc[8];
    sfor<8>([&](auto i) AI { dacc[i] = f32x4{0.f, 0.f, 0.f, 0.f}; });
    u32 kv2[8];
    {
      u32 hb = cb + ((c16 & 1) ? HA : HB) + 4 * (c16 - 15) + 16 * q + 256 * cp;
      u32 kb_ = cb + ((c16 & 1) ? KA : KB) + 448 - 64 * (c16 >> 1) + 16 * q + 256 * cp;
      u32 hb2 = hb + 8;
      pin(hb); pin(hb2); pin(kb_);
      bf16x8 ta[12], kf[12];
      auto ld1 = [&](auto odd, auto n, auto w, auto off) AI {
        constexpr int r = (n < 4 && odd) ? 8 + n : n;
        if constexpr (w == 0) kf[r] = ldsr<bf16x8>(kb_ + 512 * (7 - n) + off);
        if constexpr (w == 1) { const u32x2 v = ldsr<u32x2>(hb + 512 * n + off); u32x4 t = __builtin_bit_cast(u32x4, ta[r]); t.x = v.x; t.y = v.y; ta[r] = __builtin_bit_cast(bf16x8, t); }
        if constexpr (w == 2) { const u32x2 v = ldsr<u32x2>(hb2 + 512 * n + off); u32x4 t = __builtin_bit_cast(u32x4, ta[r]); t.z = v.x; t.w = v.y; ta[r] = __builtin_bit_cast(bf16x8, t); }
      };
      auto cstep = [&](auto odd, auto off) AI {
        sfor<36>([&](auto g) AI {
          constexpr int c = cpair(35 - g) >> 3, dl = cpair(35 - g) & 7;
          constexpr int rc = (c < 4 && odd) ? 8 + c : c, rd_ = (dl < 4 && odd) ? 8 + dl : dl;
          dacc[c + dl] = __builtin_amdgcn_mfma_f32_16x16x32_bf16(kf[rc], ta[rd_], dacc[c + dl], 0, 0, 0);
          if constexpr (g < 12) ld1(IC<1 - odd>{}, IC<g / 3>{}, IC<g % 3>{}, off);
          else if constexpr (g < 24) ld1(IC<1 - odd>{}, IC<7 - (g - 12) / 3>{}, IC<g % 3>{}, off);
          GAP();
        });
      };
      sfor<24>([&](auto i) AI { ld1(IC<0>{}, IC<(i < 3 ? 0 : i < 6 ? 7 : i < 9 ? 1 : i < 12 ? 6 : i < 15 ? 2 : i < 18 ? 5 : i < 21 ? 3 : 4)>{}, IC<i % 3>{}, IC<0>{}); GAP(); });
#pragma unroll 1
      for (int i = 0; i < 2; ++i) {
        cstep(IC<0>{}, IC<64>{});
        cstep(IC<1>{}, IC<128>{});
        hb += 128; hb2 += 128; kb_ += 128;
      }
    }
    __builtin_amdgcn_s_setprio(3);
    STAMP(25);
    sfor<4>([&](auto i) AI { ldsw<f32x4>(XCH + j * 4096 + i * 1024 + lane * 16, cp ? dacc[i] : dacc[4 + i]); });
    lds_set(CVP + j, 4);
    {
      const int l = lane_id();
      u32 a = cb + KA + 2 * (2046 - 2 * (l & 15) - 64 * (l >> 4) - 1024 * cp) - 1600;
      pin(a);
      sfor<8>([&](auto i) AI { kv2[i] = ldsr<u32>(a + 64 * (1 - (i & 1)) + 512 * (3 - (i >> 1))); });
    }
    STAMP(7);
    {
      const float dv = bflo(dvv);
      sfor<8>([&](auto i) AI { kv2[i] = pk2s(bfhi(kv2[i]) * dv, bflo(kv2[i]) * dv); pin(kv2[i]); });
      wait_byte<0>(CVP + (j ^ 2), 4);
      f32x4 o[4];
      sfor<4>([&](auto i) AI { o[i] = ldsr<f32x4>(XCH + (j ^ 2) * 4096 + i * 1024 + lane * 16); });
      asm volatile("" ::: "memory");
      lds_set(CVP + j, 5);
      sfor<4>([&](auto i) AI {
        const f32x4 m = cp ? dacc[4 + i] : dacc[i];
        sfor<2>([&](auto rr) AI {
          const u32 yb = pk2s(m[2 * rr] + o[i][2 * rr], m[2 * rr + 1] + o[i][2 * rr + 1]), kd = kv2[2 * i + rr];
          ldsw<u32>(ZST + j * 2048 + 4 * c16 + 128 * q + 64 * rr + 512 * i, pk2s(bflo(yb) + bflo(kd), bfhi(yb) + bfhi(kd)));
        });
      });
      waitl();
      u32x2 zo[4];
      sfor<4>([&](auto t) AI { zo[t] = ldsr<u32x2>(ZST + j * 2048 + t * 512 + lane * 8); });
      sfor<4>([&](auto t) AI {
        __builtin_amdgcn_raw_buffer_store_b128(u32x4{zo[t].x, tag, zo[t].y, tag}, rws, lane * 16 + W_ZT + (2 * b + ch) * 8192 + cp * 4096 + t * 1024, 0, 16);
      });
    }
    STAMP(8);
    const float mean = bflo(sg), rs = __builtin_amdgcn_rcpf(bfhi(sg));
    u32 xn[8];
    sfor<8>([&](auto i) AI {
      const u32 x2 = xr[i >> 1][i & 1], n2 = pk2s(bflo(x2) - mean, bfhi(x2) - mean);
      xn[i] = pk2s(bflo(n2) * rs, bfhi(n2) * rs);
      pin(xn[i]);
    });
    if (b == 0 && j == 3) {
      sfor<4>([&](auto t) AI { poll_flags<1>(ws, W_PF + t * 4096, 64, epoch, lane); });
      if (lane == 0) __hip_atomic_store((GAS u64*)(ws + W_E), epoch, RLX_AGENT);
    }
    wait_min4<2>(FREE, ZAFTER);
    u32x4 zr[4];
#pragma unroll 1
    for (u32 it = 0; it < SPIN_CAP; ++it) {
      sfor<4>([&](auto t) AI { bld4_sc1<0>(zr[t], l8 * 8192 + c8 * 16, rws, W_ZT + (ocol + 8 * t) * 8192 + (64 * rt + 32 * mh) * 4); });
      asm volatile("s_waitcnt vmcnt(0)" : "+v"(zr[0]), "+v"(zr[1]), "+v"(zr[2]), "+v"(zr[3])::"memory");
      u32 bad = 0;
      sfor<4>([&](auto t) AI { bad |= (zr[t].y ^ tag) | (zr[t].w ^ tag); });
      if (__all(bad == 0u)) break;
      __builtin_amdgcn_s_sleep(ZSLEEP);
    }
    STAMP(10);
    sfor<4>([&](auto t) AI { ldsw<u32x2>(ZST + j * 2048 + t * 512 + lane * 8, u32x2{zr[t].x, zr[t].z}); });
    waitl();
    float tt[16];
    sfor<4>([&](auto g) AI {
      const u32x2 z4 = ldtr(ZST + j * 2048 + (8 * g + 4 * hh + ((lane & 15) >> 2)) * 64 + 32 * ((lane >> 4) & 1) + 8 * (lane & 3));
      sfor<2>([&](auto e) AI {
        const u32 x2 = xr[g][(int)e], z2 = z4[(int)e];
        u32 n2 = pk2s(bflo(xn[2 * g + e]) * bflo(z2), bfhi(xn[2 * g + e]) * bfhi(z2));
        n2 = pk2s(bflo(x2) + bflo(n2), bfhi(x2) + bfhi(n2));
        tt[4 * g + 2 * e] = bflo(n2); tt[4 * g + 2 * e + 1] = bfhi(n2);
      });
    });
    sfor<16>([&](auto i) AI { pin(tt[i]); });
    STAMP(11);
    wait_min4<0>(FREE, 12);
    STAMP(12);
    u32x2 ob[4];
    sfor<4>([&](auto g) AI {
      const f32x4 v = ldsr<f32x4>(PART + mh * 8192 + db * 4096 + g * 1024 + lane * 16) + ldsr<f32x4>(PART + (2 + mh) * 8192 + db * 4096 + g * 1024 + lane * 16);
      ob[g].x = pk2(tt[4 * g] + rs * v.x, tt[4 * g + 1] + rs * v.y);
      ob[g].y = pk2(tt[4 * g + 2] + rs * v.z, tt[4 * g + 3] + rs * v.w);
    });
    sfor<2>([&](auto gp) AI {
      auto s0 = __builtin_amdgcn_permlane32_swap(ob[2 * gp].x, ob[2 * gp + 1].x, false, false);
      auto s1 = __builtin_amdgcn_permlane32_swap(ob[2 * gp].y, ob[2 * gp + 1].y, false, false);
      const u32x4 v = {(u32)s0[0], (u32)s1[0], (u32)s0[1], (u32)s1[1]};
      *(GAS u32x4*)((GAS char*)mkp(kb[2], kb[3]) + orow * 1024 + ocol * 2 + 32 * gp + 16 * hh) = v;
    });
    STAMP(13);
    DONE();
  } else if (wave < 4) {
    u32 junk, junk2;
    asm volatile("s_load_dword %0, %2, %3\n\ts_load_dword %1, %2, %4" : "=&s"(junk), "=&s"(junk2)
                 : "s"(pc0 & ~127ull), "s"((u32)(li + 32 * j) * 128u), "s"(min((u32)(li + 32 * (4 + j)) * 128u, CODE_BYTES)));
    const rsrc_t rws = mk_rsrc(ws, W_END);
    const int wm = wave >> 1, wn = wave & 1;
    const int l0 = lane_id();
    u32 wa = ((l0 >> 4) & 1) * (12288 + UPAD) + (8 * (l0 >> 5) + ((l0 & 15) >> 2)) * 192 + 96 * wn + 8 * (l0 & 3);
    u64 epoch;
    f32x16 acc[2][3];
    sfor<2>([&](auto m) AI { sfor<3>([&](auto n) AI { sfor<16>([&](auto r) AI { acc[m][n][(int)r] = 0.f; }); pin(acc[m][n]); }); });
    u32 xa[4];
    sfor<4>([&](auto ks) AI { xa[ks] = (64 * wm + (l0 & 31)) * 128 + (((2 * ks + (l0 >> 5)) ^ ((l0 >> 1) & 7)) << 4); pin(xa[ks]); });
    asm volatile("s_waitcnt lgkmcnt(0)" : "+s"(junk), "+s"(junk2));
    STAMP(10);
    if (has) {
      bf16x8 xf[2][2], wf[2][3];
      auto ldw = [&](auto st, auto n) AI { const u32 a = wa + G_OFF + (st & 3) * 3072 + n * 32; wf[st & 1][n] = cat(ldtr(a), ldtr(a + 768)); };
      auto ldx = [&](auto st, auto m) AI { xf[st & 1][m] = ldsr<bf16x8>(xa[st & 3] + m * 4096); };
      wait_min4<1>(FULL, 1);
      u32 accw = 0, cntw = 0;
      STAMP(1);
      ldw(IC<0>{}, IC<0>{}); ldx(IC<0>{}, IC<0>{}); ldx(IC<0>{}, IC<1>{}); ldw(IC<0>{}, IC<1>{}); ldw(IC<0>{}, IC<2>{});
      ldw(IC<1>{}, IC<0>{}); ldw(IC<1>{}, IC<1>{}); ldx(IC<1>{}, IC<0>{});
      GAP();
      u32 needw = 0x01010101u, off = 0;
#pragma unroll 1
      for (int c = 0; c < 8; ++c) {
        u32 fl, adv, have;
        sfor<24>([&](auto gg) AI {
          constexpr int st = gg / 6, g = gg % 6, n = g >> 1, m = g & 1;
          acc[m][n] = __builtin_amdgcn_mfma_f32_32x32x16_bf16(wf[st & 1][n], xf[st & 1][m], acc[m][n], 0, 0, 0);
          if constexpr (g == 0) { ldw(IC<st + 1>{}, IC<2>{}); ldx(IC<st + 1>{}, IC<1>{}); }
          if constexpr (g == 2) ldw(IC<st + 2>{}, IC<0>{});
          if constexpr (g == 4) ldw(IC<st + 2>{}, IC<1>{});
          if constexpr (g == 5) ldx(IC<st + 2>{}, IC<0>{});
          if constexpr (st == 0 && g == 3) { const u32 o2 = off == 2 * SLOT ? 0u : off + SLOT; adv = o2 - off; off = o2; needw += 0x01010101u; spin(adv); spin(off); spin(needw); }
          if constexpr (st == 1 && g == 1) fl = lds_ld1(FULL);
          if constexpr (st == 1 && g == 3) { xa[0] += adv; xa[1] += adv; xa[2] += adv; pin(xa[0]); pin(xa[1]); pin(xa[2]); }
          if constexpr (st == 2 && g == 0) { have = (rfl(fl) - needw) & 0x80808080u; spin(have); }
          if constexpr (st == 2 && g == 1) {
            xa[3] += adv; wa += adv; pin(xa[3]); pin(wa);
            if (__builtin_expect(have != 0u, 0)) wait_min4<0>(FULL, c + 2);
          }
          if constexpr (st == 3 && g == 1) lds_set(FREE + wave, c + 1);
          GAP();
        });
      }
      STAMP(2);
      __builtin_amdgcn_s_setprio(PRE);
      const int lane = lane_id(), s31 = lane & 31, hh = lane >> 5;
      f32x2 st[2];
      f32x4 ca[4];
      auto ldc = [&](auto n) AI { sfor<4>([&](auto g) AI { const u32 a = CS + (wn * 96 + n * 32 + 8 * g + 4 * hh) * 4; ca[g] = ldsr<f32x4>(a); }); };
#pragma unroll 1
      for (u32 it = 0; it < SPIN_CAP * 16; ++it) {
        const u32 r1 = lds_ld1(STRDY);
        asm volatile("" ::: "memory");
        const u32x2 e = lds_ld2(EPO);
        sfor<2>([&](auto m) AI { st[m] = ldsr<f32x2>(ST + (64 * wm + 32 * m + s31) * 8); });
        ldc(IC<0>{});
        asm volatile("" ::: "memory");
        epoch = ((u64)rfl(e.y) << 32) | rfl(e.x);
        if (__builtin_expect(all_ge(rfl(r1), 1), 1)) break;
      }
      STAMP(11);
      sfor<3>([&](auto n) AI {
        const int ks = 6 * cg + 3 * wn + n;
        if (ks < 86) {
          f32x4 c4[4];
          sfor<4>([&](auto g) AI { c4[g] = ca[g]; });
          f32x2 g[8], u[8], e[8];
          sfor<8>([&](auto i) AI {
            constexpr int m = i >> 2, r = 2 * (i & 3);
            const f32x2 cg2 = {c4[r >> 2][r & 3], c4[r >> 2][(r & 3) + 1]};
            g[i] = f32x2{acc[m][n][r], acc[m][n][r + 1]} - st[m].x * cg2;
          });
          GAP();
          sfor<8>([&](auto i) AI { g[i] = g[i] * st[i >> 2].y; });
          GAP();
          sfor<8>([&](auto i) AI { e[i] = g[i] * g[i]; });
          GAP();
          sfor<8>([&](auto i) AI { e[i] = e[i] * -0.1029432397f - 2.3022081985f; });
          GAP();
          sfor<8>([&](auto i) AI { e[i] = e[i] * g[i]; });
          GAP();
          sfor<8>([&](auto i) AI {
            constexpr int m = i >> 2, r = 2 * (i & 3);
            const f32x2 cu2 = {c4[2 + (r >> 2)][r & 3], c4[2 + (r >> 2)][(r & 3) + 1]};
            e[i] = f32x2{__builtin_amdgcn_exp2f(e[i].x), __builtin_amdgcn_exp2f(e[i].y)};
            u[i] = f32x2{acc[m][n][r + 8], acc[m][n][r + 9]} - st[m].x * cu2;
            GAP();
          });
          if constexpr (n < 2) ldc(IC<n + 1>{});
          sfor<8>([&](auto i) AI { e[i] = e[i] + 1.0f; });
          GAP();
          sfor<8>([&](auto i) AI {
            e[i] = f32x2{__builtin_amdgcn_rcpf(e[i].x), __builtin_amdgcn_rcpf(e[i].y)};
            g[i] = g[i] * u[i];
            GAP();
          });
          sfor<8>([&](auto i) AI { g[i] = g[i] * e[i]; });
          GAP();
          sfor<2>([&](auto m) AI {
            const u32x4 o = {pk2(g[4 * m].x, g[4 * m].y), pk2(g[4 * m + 1].x, g[4 * m + 1].y), pk2(g[4 * m + 2].x, g[4 * m + 2].y), pk2(g[4 * m + 3].x, g[4 * m + 3].y)};
            __builtin_amdgcn_raw_buffer_store_b128(o, rws, lane * 16 + W_H + ((4 * rb + 2 * wm + m) * 86 + ks) * 1024, 0, 16);
          });
          GAP();
          if constexpr (n < 2) STAMP(20 + n);
        }
      });
      lds_set(FREE + wave, 9);
      STAMP(3);
      waitv<0>();
      __builtin_amdgcn_s_setprio(PRW);
      if (lds_arrive(CNT + 0, lane) == 4 && lane == 0) __hip_atomic_store((GAS u64*)(ws + W_HF + (rb * 16 + cg) * 64), epoch, RLX_AGENT);
      STAMP(4);
    } else {
      if (csw) {
        f32x4 cs[3];
        sfor<3>([&](auto n) AI { cs[n] = f32x4{0.f, 0.f, 0.f, 0.f}; });
        const u32 o2_ = (l0 & 15) == ((l0 >> 4) & 1) ? 0x3f803f80u : 0u;
        const bf16x8 sel = __builtin_bit_cast(bf16x8, u32x4{o2_, o2_, o2_, o2_});
        wa += wm * 6144;
#pragma unroll 1
        for (int c = 0; c < 8; ++c) {
          wait_min4<1>(FULL, c + 1);
          bf16x8 w[6];
          sfor<6>([&](auto i) AI { const u32 a = wa + (i / 3) * 3072 + (i % 3) * 32; w[i] = cat(ldtr(a), ldtr(a + 768)); });
          asm volatile("" ::: "memory");
          lds_set(FREE + wave, c < 3 ? c + 1 : 3);
          sfor<6>([&](auto i) AI { cs[i % 3] = __builtin_amdgcn_mfma_f32_16x16x32_bf16(w[i], sel, cs[i % 3], 0, 0, 0); });
          wa += c == 4 ? (u32)(-4 * (int)CSLOT) : CSLOT;
        }
        if ((l0 & 15) < 2) sfor<3>([&](auto n) AI { ldsw<f32x4>(CS + (wm * 192 + wn * 96 + n * 32 + (l0 & 15) * 16 + 4 * (l0 >> 4)) * 4, cs[n]); });
      }
      lds_set(FREE + wave, 9);
      epoch = wait_epoch();
      if (csw && wave < 3) {
        wait_min4<1>(FREE, 9);
        const u32 i = 64 * wave + l0;
        const float v = ldsr<float>(CS + i * 4) + ldsr<float>(CS + 768 + i * 4);
        __hip_atomic_store((GAS u64*)(ws + W_CG + (cg * 192 + i) * 8), ((u64)(u32)(epoch >> 17) << 32) | __float_as_uint(v), RLX_AGENT);
      }
      STAMP(4);
    }
    const int lane = lane_id(), hh = lane >> 5, g16 = lane >> 4, q4 = (lane & 15) >> 2, p4 = lane & 3;
    const int kh = wave >> 1, mh = wave & 1;
    u32x4 hf[43];
    const u32 hso = W_H + ((2 * rt + mh) * 86 + 43 * kh) * 1024, hvo = lane * 16;
    f32x16 o2[2];
    sfor<2>([&](auto n) AI { sfor<16>([&](auto r) AI { o2[n][(int)r] = 0.f; }); pin(o2[n]); });
    u32 db_[2][3];
    sfor<2>([&](auto n) AI {
      const u32 da = kh * 8192 + (4 * hh + q4) * 128 + ((4 * ((int)n ^ (q4 >> 1)) + 2 * (g16 & 1) + (p4 >> 1)) << 4) + 8 * (p4 & 1);
      sfor<3>([&](auto w) AI { db_[n][w] = da + w * 61440; pin(db_[n][w]); });
    });
    bf16x8 df[3][2];
    u32 fl;
    auto ldd = [&](auto k) AI {
      constexpr int c = k >> 2, ks = k & 3;
      constexpr u32 wb = (c < 7 ? 16384u * c : c == 7 ? CONV : c == 8 ? CONV + CH_SZ : 16384u * (c - 9)) + ks * 2048;
      constexpr int w = wb / 61440 > 2 ? 2 : wb / 61440;
      sfor<2>([&](auto n) AI { const u32 a = db_[n][w] + (wb - w * 61440); df[k % 3][n] = cat(ldtr(a), ldtr(a + 1024)); });
    };
    if (wave == 0) { poll_flags<1>(ws, W_HF + (rt >> 1) * 16 * 64, 15, epoch, lane); lds_set(CVP + 6, 1); }
    else wait_byte<WSL>(CVP + 6, 1);
    __builtin_amdgcn_s_setprio(2);
    STAMP(5);
    auto hload = [&](auto k) AI { bld4_sc1<(k & 3) * 1024>(hf[k], hvo, rws, hso + (k >> 2) * 4096); };
    sfor<PD>([&](auto k) AI { hload(k); });
    wait_min4<0>(WDRDY, 1);
    STAMP(6);
    ldd(IC<0>{}); ldd(IC<1>{});
    GAP();
    sfor<43>([&](auto k) AI {
      if constexpr (k == 10) lds_set(FREE + wave, 10);
      if constexpr (k == 18) lds_set(FREE + wave, 11);
      if constexpr (k == 20 || k == 32) fl = lds_ld1(WDRDY);
      if constexpr (k == 40) fl = lds_ld1(FREE);
      if constexpr (k == 22 || k == 34) { STAMP(k == 22 ? 12 : 14); if (__builtin_expect(!all_ge(rfl(fl), k == 22 ? 2u : 3u), 0)) wait_min4<0>(WDRDY, k == 22 ? 2 : 3); STAMP(k == 22 ? 13 : 15); GAP(); }
      constexpr int younger = (k + PD <= 43 ? PD : 43 - k) - 1;
      waitv_pin<younger>(hf[k]);
      if constexpr (k == 0) STAMP(24);
      if constexpr (k == 8) STAMP(25);
      if constexpr (k == 16) STAMP(26);
      o2[0] = __builtin_amdgcn_mfma_f32_32x32x16_bf16(df[k % 3][0], __builtin_bit_cast(bf16x8, hf[k]), o2[0], 0, 0, 0);
      if constexpr (k + 2 < 43) ldd(IC<k + 2>{});
      GAP();
      o2[1] = __builtin_amdgcn_mfma_f32_32x32x16_bf16(df[k % 3][1], __builtin_bit_cast(bf16x8, hf[k]), o2[1], 0, 0, 0);
      if constexpr (k + PD < 43) hload(IC<k + PD>{});
      GAP();
    });
    STAMP(7);
    if (!all_ge(rfl(fl), 11u)) wait_min4<0>(FREE, 11);
    sfor<2>([&](auto n) AI { sfor<4>([&](auto g) AI {
      f32x4 v = {o2[n][4 * g], o2[n][4 * g + 1], o2[n][4 * g + 2], o2[n][4 * g + 3]};
      ldsw<f32x4>(PART + wave * 8192 + n * 4096 + g * 1024 + lane * 16, v);
    }); });
    waitl();
    lds_set(FREE + wave, 12);
    STAMP(8);
    DONE();
  } else {
    __builtin_amdgcn_s_setprio(PRL);
    const rsrc_t rg = mk_rsrc(mkp(ka[2], ka[3]), D * F * 2), ru = mk_rsrc(mkp(ka[4], ka[5]), D * F * 2);
    const int crot = ROT * ((rb + cg) & 1);
    auto chx = [&](int c, u32 sb) AI { dma4(sb + 4 * j * 1024, xv[0], xv[1], xv[2], xv[3], rq, (rb * 128 + 32 * j) * 1024 + (c ^ crot) * 128); };
    auto chw = [&](int c, u32 sb) AI {
      dma3(sb + G_OFF + 3 * j * 1024, wv[0], wv[1], wv[2], rg, (c ^ crot) * 64 * F * 2);
      dma3(sb + U_OFF + 3 * j * 1024, wv[0], wv[1], wv[2], ru, (c ^ crot) * 64 * F * 2);
    };
    if (has) {
      chx(0, 0); chw(0, 0); chx(1, SLOT); chw(1, SLOT);
      int ic = 2, lc = 0;
      u32 sb = 2 * SLOT;
#pragma unroll 1
      for (u32 it = 0; it < SPIN_CAP * 16; ++it) {
        if (lc < ic && vm_pending() <= 10u * (u32)(ic - lc - 1)) {
          asm volatile("" ::: "memory");
          ++lc;
          lds_set(FULL + j, lc == 8 ? 9 : lc);
          if (lc == 1) STAMP(1);
          if (lc == 8) break;
        } else if (ic < 8 && all_ge(rfl(lds_ld1(FREE)), ic - 2)) {
          asm volatile("" ::: "memory");
          chx(ic, sb);
          if (lc < ic && vm_pending() <= 10u * (u32)(ic - lc - 1) + 4u) {
            lds_set(FULL + j, ++lc);
          }
          chw(ic, sb);
          ++ic;
          sb = sb == 2 * SLOT ? 0 : sb + SLOT;
        } else {
          __builtin_amdgcn_s_sleep(LSLEEP);
        }
      }
      waitv<0>();
    } else if (csw) {
      sfor<8>([&](auto c) AI {
        if constexpr (c >= 5) { waitv<24>(); lds_set(FULL + j, (6u * c - vm_pending()) * 43u >> 8); wait_min4<1>(FREE, c - 4); }
        chw(c, (c % 5) * CSLOT - G_OFF);
        if constexpr (c >= 2) lds_set(FULL + j, (6u * (c + 1) - vm_pending()) * 43u >> 8);
      });
      waitv<12>(); lds_set(FULL + j, 6);
      waitv<6>(); lds_set(FULL + j, 7);
      waitv<0>(); lds_set(FULL + j, 8);
    }
    STAMP(2);
    const rsrc_t rd = mk_rsrc(mkp(kb[0], kb[1]), F * D * 2);
    u32 dv[4];
    sfor<4>([&](auto t) AI { dv[t] = (7 * t + l8) * 1024 + ((c8 ^ (((l8 >> 1) & 1) << 2)) << 4) + ct * 128; });
    auto wso = [&](int g) AI { return (u32)(((g >> 1) & 1) * 688 + (g >> 2) * 64 + (g & 1) * 32) * 1024u; };
    auto wd_groups = [&](int lo, int hi) AI {
#pragma unroll 1
      for (int g = lo + j; g < hi; g += 4) { dma4(g * 4096, dv[0], dv[1], dv[2], dv[3], rd, wso(g)); waitv<WDFL>(); }
    };
    u32x4 wr[5][4];
    auto wst = [&](auto t, u32 a) AI { sfor<4>([&](auto i) AI { pin(wr[t][i]); ldsw<u32x4>(a + j * 4096 + i * 1024 + lane * 16, wr[t][i]); }); };
    auto wrl = [&]() AI { sfor<5>([&](auto t) AI { const u32 so = wso(24 + 4 * t + j); sfor<4>([&](auto i) AI { bld4o<i * 1024>(wr[t][i], dv[i], rd, so); }); waitv<WRFL>(); }); };
    wait_min4<1>(FREE, WD1);
    wd_groups(21, 24);
    wait_min4<1>(FREE, WD2);
    wd_groups(0, 10);
    wait_min4<1>(FREE, WD3);
    wd_groups(10, 21);
    waitv<0>();
    lds_set(WDRDY + j, 1);
    wait_min4<1>(FREE, WRLAST);
    wrl();
    waitv<0>();
    lds_set(WDRDY + j, 1);
    STAMP(3);
    wait_min4<4>(CVP, 5);
    wst(IC<0>{}, XCH); wst(IC<1>{}, CONV); wst(IC<2>{}, CONV + CH_SZ);
    waitl();
    lds_set(WDRDY + j, 2);
    STAMP(4);
    wait_min4<2>(FREE, 10);
    wst(IC<3>{}, 0); wst(IC<4>{}, 16384);
    waitl();
    lds_set(WDRDY + j, 3);
    STAMP(5);
    DONE();
  }
}

extern "C" void kernel_launch(void* const* d_in, const int* in_sizes, int n_in, void* d_out, int out_size, void* d_ws, size_t ws_size,
                              hipStream_t stream) {
  Args a;
  a.q = (const u16*)d_in[0]; a.k = (const u16*)d_in[1]; a.v = (const u16*)d_in[2]; a.sk = (const u16*)d_in[3];
  a.h = (const u16*)d_in[4]; a.dv = (const u16*)d_in[5];
  a.wg = (const u16*)d_in[7]; a.wu = (const u16*)d_in[8]; a.wd = (const u16*)d_in[9];
  a.out = (u16*)d_out; a.ws = (char*)d_ws; a.pad = 0;
  (void)hipFuncSetAttribute((const void*)fused, hipFuncAttributeMaxDynamicSharedMemorySize, LDS_BYTES);
  fused<<<256, 768, LDS_BYTES, stream>>>(a);
}
```

```cpp
#define AMPID 3
#define AMPWAVE 0
#define WRFL 2
#include <hip/hip_runtime.h>
#include <stdint.h>
#include <utility>

typedef unsigned short u16;
typedef unsigned int u32;
typedef unsigned long long u64;
typedef __bf16 bf16x8 __attribute__((ext_vector_type(8)));
typedef __bf16 bf16x2 __attribute__((ext_vector_type(2)));
typedef short s16x4 __attribute__((ext_vector_type(4)));
typedef short s16x8 __attribute__((ext_vector_type(8)));
typedef float f32x16 __attribute__((ext_vector_type(16)));
typedef float f32x4 __attribute__((ext_vector_type(4)));
typedef float f32x2 __attribute__((ext_vector_type(2)));
typedef u32 u32x4 __attribute__((ext_vector_type(4)));
typedef u32 u32x2 __attribute__((ext_vector_type(2)));
typedef __amdgpu_buffer_rsrc_t rsrc_t;
#define DI __device__ __forceinline__
#define AI __attribute__((always_inline))
#define LAS __attribute__((address_space(3)))
#define GAS __attribute__((address_space(1)))
#define RLX_AGENT __ATOMIC_RELAXED, __HIP_MEMORY_SCOPE_AGENT
#define RLX_WG __ATOMIC_RELAXED, __HIP_MEMORY_SCOPE_WORKGROUP

constexpr int S = 2048, D = 512, F = 1376;
#define UPAD 32
#define SLOTB (40960 + UPAD)
constexpr u32 SLOT = SLOTB, G_OFF = 16384, U_OFF = 16384 + 12288 + UPAD;
constexpr u32 CSLOT = 2 * (12288 + UPAD) + 64 - 2 * UPAD % 64;
constexpr u32 CONV = 3 * 41216;
constexpr u32 CH_SZ = 17520, HA = 64, HB = 4292, KA = 8400, KB = 12976, XB = 4304;
constexpr u32 MISC = CONV + 2 * CH_SZ;
constexpr u32 CS = MISC, ST = MISC + 1536, CNT = MISC + 2560;
constexpr u32 CKV = CNT + 8, CSP = CNT + 12, EPO = CNT + 16, CVP = CNT + 24, FULL = CNT + 32, FREE = CNT + 36, STRDY = CNT + 40, WDRDY = CNT + 44;
constexpr u32 LDS_BYTES = MISC + 2816;
static_assert(LDS_BYTES <= 163840, "LDS");
constexpr u32 XCH = 98304, ZST = 114688, PART = 32768;
constexpr u32 W_E = 0, W_PF = 4096, W_HF = W_PF + 16384, W_ZF = W_HF + 16384, W_SG = W_ZF + 16384, W_CG = W_SG + 16384;
constexpr u32 W_KV = 131072, W_ZT = W_KV + 2097152 + 4096, W_H = W_ZT + 4194304, W_END = W_H + 64 * 86 * 1024;
constexpr u32 W_DBG = 16u << 20;
constexpr u32 CODE_BYTES = 26112;
#define PRL 1
#define PRS 0
#define CONVAFTER 9
#define LSLEEP 1
#define PRA 0
#define ROT 0
#define S2AFTER 8
#define WDFL 2
#define KVSTAG 0
#define SSTOP 9
#define POLLS 4
#define PD 10
#define WD1 6
#define WD2 7
#define PFLO 8
#define PFPRIO 0
#define PFWAIT 0
#define KVMODE 0
#define WD3 8
#define PRE 2
#define PRW 0
#define WSL 1
#define PRC 3
#define ZSLEEP 4
#define ZAFTER 11
#define KVAFTER 1
constexpr u32 SPIN_CAP = 1u << 18;

constexpr int cpair(int g) {
  int i = 0;
  for (int n = 0; n < 8; ++n) for (int m = 0; m <= n; ++m) if (m + n <= 7) {
    if (i++ == g) return n * 8 + m;
    if (m < n && i++ == g) return m * 8 + n;
  }
  return 0;
}
template <class Fn, int... I> DI void sfor_impl(Fn&& f, std::integer_sequence<int, I...>) { (f(std::integral_constant<int, I>{}), ...); }
template <int N, class Fn> DI void sfor(Fn&& f) { sfor_impl(f, std::make_integer_sequence<int, N>{}); }

DI rsrc_t mk_rsrc(const void* p, u32 bytes) { return __builtin_amdgcn_make_buffer_rsrc((void*)p, (short)0, (int)bytes, 0x00020000); }
DI void dma(u32 lds, u32 voff, rsrc_t r, u32 soff) {
  asm volatile("s_mov_b32 m0, %0\n\ts_nop 0\n\tbuffer_load_dwordx4 %1, %2, %3 offen lds" ::"s"(lds), "v"(voff), "s"(r), "s"(soff) : "memory");
}

DI void dma4(u32 lds, u32 v0, u32 v1, u32 v2, u32 v3, rsrc_t r, u32 soff) {
  asm volatile("s_mov_b32 m0, %0\n\ts_nop 0\n\tbuffer_load_dwordx4 %1, %5, %6 offen lds\n\tbuffer_load_dwordx4 %2, %5, %6 offen offset:1024 lds\n\t"
               "buffer_load_dwordx4 %3, %5, %6 offen offset:2048 lds\n\tbuffer_load_dwordx4 %4, %5, %6 offen offset:3072 lds"
               ::"s"(lds), "v"(v0), "v"(v1), "v"(v2), "v"(v3), "s"(r), "s"(soff) : "memory");
}
DI void dma3(u32 lds, u32 v0, u32 v1, u32 v2, rsrc_t r, u32 soff) {
  asm volatile("s_mov_b32 m0, %0\n\ts_nop 0\n\tbuffer_load_dwordx4 %1, %4, %5 offen lds\n\tbuffer_load_dwordx4 %2, %4, %5 offen offset:1024 lds\n\t"
               "buffer_load_dwordx4 %3, %4, %5 offen offset:2048 lds"
               ::"s"(lds), "v"(v0), "v"(v1), "v"(v2), "s"(r), "s"(soff) : "memory");
}
template <int OFF> DI void bld4_sc1(u32x4& d, u32 voff, rsrc_t r, u32 soff) {
  asm volatile("buffer_load_dwordx4 %0, %1, %2, %3 offen offset:%4 sc1" : "=v"(d) : "v"(voff), "s"(r), "s"(soff), "i"(OFF));
}
template <int OFF> DI void bld4o(u32x4& d, u32 voff, rsrc_t r, u32 soff) { asm volatile("buffer_load_dwordx4 %0, %1, %2, %3 offen offset:%4" : "=v"(d) : "v"(voff), "s"(r), "s"(soff), "i"(OFF)); }
DI void bld4(u32x4& d, u32 voff, rsrc_t r, u32 soff) { asm volatile("buffer_load_dwordx4 %0, %1, %2, %3 offen" : "=v"(d) : "v"(voff), "s"(r), "s"(soff)); }
template <int OFF> DI void bld2(u32x2& d, u32 voff, rsrc_t r) { asm volatile("buffer_load_dwordx2 %0, %1, %2, 0 offen offset:%3" : "=v"(d) : "v"(voff), "s"(r), "i"(OFF)); }
template <int OFF> DI void gld4_sc1(u32x4& d, u32 voff, const char* base) {
  asm volatile("global_load_dwordx4 %0, %1, %2 offset:%3 sc1" : "=v"(d) : "v"(voff), "s"(base), "i"(OFF));
}
template <int N> DI void waitv_pin(u32x4& d) { asm volatile("s_waitcnt vmcnt(%1)" : "+v"(d) : "i"(N)); }
template <int N> DI void waitv() { asm volatile("s_waitcnt vmcnt(%0)" ::"i"(N) : "memory"); }
DI u32 vm_pending() { u32 r; asm volatile("s_getreg_b32 %0, hwreg(HW_REG_IB_STS)" : "=s"(r)::"memory"); return (r & 15u) | ((r >> 18) & 48u); }
DI void bar() { asm volatile("s_waitcnt lgkmcnt(0)\n\ts_barrier" ::: "memory"); }
DI void waitl() { asm volatile("s_waitcnt lgkmcnt(0)" ::: "memory"); }
DI float bflo(u32 x) { return __uint_as_float(x << 16); }
DI float bfhi(u32 x) { return __uint_as_float(x & 0xffff0000u); }
DI u32 pk2(float lo, float hi) { f32x2 v = {lo, hi}; return __builtin_bit_cast(u32, __builtin_convertvector(v, bf16x2)); }
DI u32 pk2s(float lo, float hi) { asm("" : "+v"(lo)); asm("" : "+v"(hi)); return pk2(lo, hi); }
DI float rbf(float x) { return bflo(pk2(x, x)); }
template <class T> DI T ldsr(u32 a) { return *(const LAS T*)a; }
template <class T> DI void ldsw(u32 a, T v) { *(LAS T*)a = v; }
DI u32x2 ldtr(u32 a) { return __builtin_bit_cast(u32x2, __builtin_amdgcn_ds_read_tr16_b64_v4i16((LAS s16x4*)a)); }
DI bf16x8 cat(u32x2 a, u32x2 b) { u32x4 v = {a.x, a.y, b.x, b.y}; return __builtin_bit_cast(bf16x8, v); }
template <int CTRL, int RM> DI float dpp_add(float v) {
  return v + __builtin_bit_cast(float, __builtin_amdgcn_update_dpp(0, __builtin_bit_cast(int, v), CTRL, RM, 0xf, false));
}
DI float wave_sum(float v) {
  v = dpp_add<0x111, 0xf>(v); v = dpp_add<0x112, 0xf>(v); v = dpp_add<0x114, 0xf>(v); v = dpp_add<0x118, 0xf>(v);
  v = dpp_add<0x142, 0xa>(v); v = dpp_add<0x143, 0xc>(v);
  return __builtin_bit_cast(float, __builtin_amdgcn_readlane(__builtin_bit_cast(int, v), 63));
}
DI void wave_sum2(float& a, float& c) {
  a = dpp_add<0x111, 0xf>(a); c = dpp_add<0x111, 0xf>(c); a = dpp_add<0x112, 0xf>(a); c = dpp_add<0x112, 0xf>(c);
  a = dpp_add<0x114, 0xf>(a); c = dpp_add<0x114, 0xf>(c); a = dpp_add<0x118, 0xf>(a); c = dpp_add<0x118, 0xf>(c);
  a = dpp_add<0x142, 0xa>(a); c = dpp_add<0x142, 0xa>(c); a = dpp_add<0x143, 0xc>(a); c = dpp_add<0x143, 0xc>(c);
  a = __builtin_bit_cast(float, __builtin_amdgcn_readlane(__builtin_bit_cast(int, a), 63));
  c = __builtin_bit_cast(float, __builtin_amdgcn_readlane(__builtin_bit_cast(int, c), 63));
}
DI void gld8_sc1(u64& d, const char* p) { asm volatile("global_load_dwordx2 %0, %1, off sc1" : "=v"(d) : "v"(p)); }
DI void gld_u16(u32& d, u32 voff, const char* base) { asm volatile("global_load_ushort %0, %1, %2" : "=v"(d) : "v"(voff), "s"(base)); }
template <int SLEEP> DI void poll_flags(const char* ws, u32 off, int n, u64 epoch, int lane) {
  const GAS u64* p = (const GAS u64*)(ws + off + (u32)(lane < n ? lane : 0) * 64u);
#pragma unroll 1
  for (u32 it = 0; it < SPIN_CAP; ++it) {
    u64 v = __hip_atomic_load(p, RLX_AGENT);
    if (__all(v == epoch)) break;
    __builtin_amdgcn_s_sleep(SLEEP);
  }
  asm volatile("" ::: "memory");
}
DI u32 poll_granule(const char* ws, u32 row, u32 tag) {
  const GAS u64* p = (const GAS u64*)(ws + W_SG + row * 8u);
  u64 v = 0;
#pragma unroll 1
  for (u32 it = 0; it < SPIN_CAP; ++it) {
    v = __hip_atomic_load(p, RLX_AGENT);
    if (__all((u32)(v >> 32) == tag)) break;
    __builtin_amdgcn_s_sleep(1);
  }
  return (u32)v;
}
DI u32 lds_arrive(u32 a, int lane) {
  u32 r = 0;
  waitl();
  if (lane == 0) r = __hip_atomic_fetch_add((LAS u32*)a, 1u, RLX_WG);
  return (u32)__builtin_amdgcn_readfirstlane(r) + 1u;
}
DI void lds_wait(u32 a, u32 want) {
#pragma unroll 1
  for (u32 it = 0; it < SPIN_CAP * 16; ++it) {
    u32 v = __hip_atomic_load((LAS u32*)a, RLX_WG);
    if ((u32)__builtin_amdgcn_readfirstlane(v) >= want) break;
  }
  asm volatile("" ::: "memory");
}

DI int lane_id() { int l; asm volatile("v_mbcnt_lo_u32_b32 %0, -1, 0\n\tv_mbcnt_hi_u32_b32 %0, -1, %0" : "=v"(l)); return l; }
DI u32 rfl(u32 x) { return (u32)__builtin_amdgcn_readfirstlane(x); }
DI u32x2 lds_ld2(u32 a) { return __builtin_bit_cast(u32x2, __hip_atomic_load((LAS u64*)a, RLX_WG)); }
template <int SLEEP> DI void wait_min2(u32 a, u32 want) {
#pragma unroll 1
  for (u32 it = 0; it < SPIN_CAP * 16; ++it) {
    const u32x2 v = lds_ld2(a);
    if (rfl(min(v.x, v.y)) >= want) break;
    if (SLEEP) __builtin_amdgcn_s_sleep(SLEEP);
  }
  asm volatile("" ::: "memory");
}
DI u32 lds_ld1(u32 a) { return __hip_atomic_load((LAS u32*)a, RLX_WG); }
DI bool all_ge(u32 w, u32 want) { return ((w - want * 0x01010101u) & 0x80808080u) == 0u; }
template <int SLEEP> DI void wait_min4(u32 a, u32 want) {
#pragma unroll 1
  for (u32 it = 0; it < SPIN_CAP * 16; ++it) {
    if (all_ge(rfl(lds_ld1(a)), want)) break;
    if (SLEEP) __builtin_amdgcn_s_sleep(SLEEP);
  }
  asm volatile("" ::: "memory");
}
template <int SLEEP> DI void wait_byte(u32 a, u32 want) {
#pragma unroll 1
  for (u32 it = 0; it < SPIN_CAP * 16; ++it) {
    if (rfl(__hip_atomic_load((LAS unsigned char*)a, RLX_WG)) >= want) break;
    if (SLEEP) __builtin_amdgcn_s_sleep(SLEEP);
  }
  asm volatile("" ::: "memory");
}
DI u64 wait_epoch() {
  u32x2 e;
#pragma unroll 1
  for (u32 it = 0; it < SPIN_CAP * 16; ++it) {
    e = lds_ld2(EPO);
    if (rfl(e.x) != 0u) break;
    __builtin_amdgcn_s_sleep(2);
  }
  return ((u64)rfl(e.y) << 32) | rfl(e.x);
}
DI u64 gld_agent(const char* p) { return __hip_atomic_load((const GAS u64*)p, RLX_AGENT); }
DI void lds_set(u32 a, u32 v) { __hip_atomic_store((LAS unsigned char*)a, (unsigned char)v, RLX_WG); }
DI void gather(u32& junk, u32 voff, rsrc_t r, u32 soff) { asm volatile("buffer_load_dword %0, %1, %2, %3 offen" : "=v"(junk) : "v"(voff), "s"(r), "s"(soff)); }
template <int N> using IC = std::integral_constant<int, N>;

#define DONE() do { if (wave >= 8) { const u64 d_ = rfl(ldsr<u32>(CNT + 60)); while (__builtin_amdgcn_s_memrealtime() - tstart_ < 40 * d_) __builtin_amdgcn_s_sleep(1); } } while (0)
#define STAMP(id) do { if ((id) == AMPID && wave == AMPWAVE) ldsw<u32>(CNT + 60, (u32)(__builtin_amdgcn_s_memrealtime() - tstart_)); } while (0)

struct Args { const u16 *q, *wg, *wu, *k, *v, *sk, *h, *dv, *wd; u16* out; char* ws; u64 pad; };
typedef u32 u32x16 __attribute__((ext_vector_type(16)));
typedef u32 u32x8 __attribute__((ext_vector_type(8)));
DI char* mkp(u32 lo, u32 hi) { return (char*)(((u64)hi << 32) | lo); }
#define GAP() __builtin_amdgcn_sched_barrier(0)
DI void barx() { asm volatile("s_barrier" ::: "memory"); }
template <class T> DI void spin(T& x) { asm volatile("" : "+s"(x)); }
template <class T> DI void pin(T& x) { asm volatile("" : "+v"(x)); }

__global__ void __launch_bounds__(768, 3) fused(Args) {
  u64 pc0; asm volatile("s_getpc_b64 %0" : "=s"(pc0));
  const u64 tstart_ = __builtin_amdgcn_s_memrealtime();
  asm volatile("s_waitcnt lgkmcnt(0)");
  u32x16 ka; u32x8 kb;
  asm volatile("s_load_dwordx16 %0, %2, 0x0\n\ts_load_dwordx8 %1, %2, 0x40" : "=&s"(ka), "=&s"(kb) : "s"(__builtin_amdgcn_kernarg_segment_ptr()));
  GAP();
  const int tid = threadIdx.x, lane = tid & 63;
  const int wave = __builtin_amdgcn_readfirstlane(tid >> 6);
  const int b = blockIdx.x, xcd = b & 7, li = b >> 3;
  if (tid < 16) ldsw<u32>(CNT + tid * 4, 0u);
  const bool has = li < 30;
  const int id = 30 * xcd + (has ? li : 0), rr_ = id % 120, rb = 8 * (id / 120) + (rr_ & 7);
  const int cg = has ? rr_ >> 3 : 4 * (xcd & 3) + 2 * (xcd >> 2) + (li - 30);
  const bool csw = !has && cg < 15;
  const int rt = 4 * xcd + (li & 3), ct = li >> 2;
  const int j = wave & 3, l8 = lane >> 3, c8 = lane & 7;
  u32 xv[4], wv[3];
  sfor<4>([&](auto t) AI { xv[t] = (7 * t + l8) * 1024 + ((c8 ^ ((4 * t + (l8 >> 1)) & 7)) << 4); pin(xv[t]); });
  sfor<3>([&](auto t) AI {
    const u32 byte = t * 1024 + lane * 16, row = byte / 192u;
    const u32 seg = byte - row * 192u;
    wv[t] = cg * 192 + seg >= F * 2 ? 0x40000000u : (16 * j + row) * (F * 2) + cg * 192 + seg - t * 1024;
    pin(wv[t]);
  });
  asm volatile("s_waitcnt lgkmcnt(0)\n\ts_barrier" : "+s"(ka), "+s"(kb)::"memory");
  STAMP(9);
  char* const ws = mkp(kb[4], kb[5]);
  const rsrc_t rq = mk_rsrc(mkp(ka[0], ka[1]), S * D * 2);
  if (wave >= 8) {
    __builtin_amdgcn_s_setprio(3);
    u64 epoch;
    asm volatile("s_load_dwordx2 %0, %1, 0x0" : "=s"(epoch) : "s"(ws));
    const int lane = lane_id(), l8 = lane >> 3, c8 = lane & 7;
    const int ch = j & 1, cp = j >> 1, c16 = lane & 15, q = lane >> 4;
    const rsrc_t rws = mk_rsrc(ws, W_END);
    const u32 cb = CONV + ch * CH_SZ, hown = cb + (cp ? XB : HA), kown = cb + (cp ? KB : KA);
    const int srow = 256 * xcd + 8 * li + 2 * j;
    if (SSTOP == 0) wait_min4<8>(FREE, 8);
    u32x4 sx[2];
    sfor<2>([&](auto r) AI { bld4(sx[r], lane * 16, rq, (srow + r) * 1024); });
    STAMP(23);
    const int pa = b & 31, pg = b >> 5, s0 = 64 * pa + 16 * j;
    u32x2 skw[2];
    u32 dvv;
    auto kvload = [&]() AI {
      const rsrc_t rk = mk_rsrc(mkp(ka[6], ka[7]), S * D * 2), rv = mk_rsrc(mkp(ka[8], ka[9]), S * D * 2), rs_ = mk_rsrc(mkp(ka[10], ka[11]), D * 8);
      const u32 kvv = l8 * 1024 + c8 * 16 + pg * 128;
      sfor<3>([&](auto t) AI { const int r = s0 - 8 + 8 * t; dma(hown + t * 1024, kvv, rk, (r < 0 ? 0 : r) * 1024); });
      sfor<2>([&](auto t) AI { dma(kown + t * 1024, kvv, rv, (s0 + 8 * t) * 1024); });
      sfor<2>([&](auto t) AI { bld2<256 * t>(skw[t], (64 * pg + 16 * (q >> 1) + c16) * 8, rs_); });
      gld_u16(dvv, (2 * b + ch) * 2, mkp(ka[14], ka[15]));
    };
    asm volatile("s_waitcnt lgkmcnt(0)" : "+s"(epoch));
    epoch += 0x9E3779B97F4A7C15ull;
    epoch = (epoch ^ (epoch >> 30)) * 0xBF58476D1CE4E5B9ull; epoch = (epoch ^ (epoch >> 27)) * 0x94D049BB133111EBull;
    epoch = (epoch ^ (epoch >> 31)) | 1ull;
    ldsw<u64>(EPO, epoch);
    STAMP(18);
    const u32 tag = (u32)(epoch >> 17);
    if (has) wait_min4<2>(FULL, KVAFTER + KVSTAG * (li & 3));
    STAMP(19);
    kvload();
    STAMP(20);
    asm volatile("s_waitcnt vmcnt(8)" : "+v"(sx[0]), "+v"(sx[1]));
    STAMP(21);
    __builtin_amdgcn_s_setprio(0);
    {
      float x[2][8], mean[2], var[2];
      sfor<2>([&](auto r) AI {
        sfor<4>([&](auto i) AI { x[r][2 * i] = bflo(sx[r][(int)i]); x[r][2 * i + 1] = bfhi(sx[r][(int)i]); });
        mean[r] = x[r][0];
        sfor<7>([&](auto i) AI { mean[r] += x[r][i + 1]; });
      });
      wave_sum2(mean[0], mean[1]);
      sfor<2>([&](auto r) AI {
        mean[r] *= 1.0f / D; var[r] = 0.f;
        sfor<8>([&](auto i) AI { float d = x[r][i] - mean[r]; asm("" : "+v"(d)); var[r] += d * d; });
      });
      wave_sum2(var[0], var[1]);
      STAMP(22);
      const float mn = lane ? mean[1] : mean[0], vr = rbf((lane ? var[1] : var[0]) * (1.0f / D));
      const u32 g = pk2s(mn, __builtin_sqrtf(rbf(vr + 1e-5f)));
      if (lane < 2) __hip_atomic_store((GAS u64*)(ws + W_SG + (srow + lane) * 8), ((u64)tag << 32) | g, RLX_AGENT);
    }
    STAMP(1);
    asm volatile("s_waitcnt vmcnt(1)" : "+v"(skw[0]), "+v"(skw[1]), "+v"(dvv)::"memory");
    STAMP(2);
    {
      const bool first = s0 == 0 && (q & 1) == 0;
      u32x2 kr[2][5];
      sfor<2>([&](auto gi) AI {
        const u32 a = (8 * (q & 1) + (c16 >> 2)) * 128 + 64 * gi + 32 * (q >> 1) + 8 * (lane & 3);
        kr[gi][0] = ldtr(hown + a + 512); kr[gi][1] = ldtr(hown + a + 1024); kr[gi][2] = ldtr(hown + a + 1536); kr[gi][3] = ldtr(kown + a); kr[gi][4] = ldtr(kown + a + 512);
      });
      GAP();
      if (SSTOP == 1) wait_min4<8>(FREE, 8);
      sfor<2>([&](auto gi) AI {
        const float w0 = bflo(skw[gi].x), w1 = bfhi(skw[gi].x), w2 = bflo(skw[gi].y), w3 = bfhi(skw[gi].y);
        const u32x2 k0 = kr[gi][0], k1 = kr[gi][1], k2 = kr[gi][2], v0 = kr[gi][3], v1 = kr[gi][4];
        const float kk[11] = {first ? 0.f : bfhi(k0.x), first ? 0.f : bflo(k0.y), first ? 0.f : bfhi(k0.y), bflo(k1.x), bfhi(k1.x), bflo(k1.y), bfhi(k1.y), bflo(k2.x), bfhi(k2.x), bflo(k2.y), bfhi(k2.y)};
        const float vv[8] = {bflo(v0.x), bfhi(v0.x), bflo(v0.y), bfhi(v0.y), bflo(v1.x), bfhi(v1.x), bflo(v1.y), bfhi(v1.y)};
        u32 a4[4], m4[4];
        sfor<4>([&](auto t) AI { a4[t] = pk2s(w3 * kk[2 * t], w3 * kk[2 * t + 1]); m4[t] = pk2s(w2 * kk[2 * t + 1], w2 * kk[2 * t + 2]); });
        GAP();
        sfor<4>([&](auto t) AI { a4[t] = pk2s(bflo(a4[t]) + bflo(m4[t]), bfhi(a4[t]) + bfhi(m4[t])); m4[t] = pk2s(w1 * kk[2 * t + 2], w1 * kk[2 * t + 3]); });
        GAP();
        sfor<4>([&](auto t) AI { a4[t] = pk2s(bflo(a4[t]) + bflo(m4[t]), bfhi(a4[t]) + bfhi(m4[t])); m4[t] = pk2s(w0 * kk[2 * t + 3], w0 * kk[2 * t + 4]); });
        GAP();
        sfor<4>([&](auto t) AI { a4[t] = pk2s(bflo(a4[t]) + bflo(m4[t]), bfhi(a4[t]) + bfhi(m4[t])); });
        GAP();
        u32x4 ov;
        sfor<4>([&](auto t) AI { ov[3 - t] = pk2s(bfhi(a4[t]) * vv[2 * t + 1], bflo(a4[t]) * vv[2 * t]); });
        const int dg = 2 * gi + (q >> 1);
        ldsw<u32x4>(CONV + (dg & 1) * CH_SZ + (dg >> 1 ? KB : KA) + 2048 + c16 * 128 + (((7 - 2 * j - (q & 1)) ^ (c16 & 7)) << 4), ov);
      });
    }
    waitl();
    lds_set(CVP + j, 1);
    __builtin_amdgcn_s_setprio(3);
    STAMP(17);
    wait_min4<0>(CVP, 1);
    {
      u32x4 o[2];
      sfor<2>([&](auto t) AI { o[t] = ldsr<u32x4>(kown + 2048 + (8 * t + l8) * 128 + ((c8 ^ l8) << 4)); });
      sfor<2>([&](auto t) AI { __builtin_amdgcn_raw_buffer_store_b128(o[t], rws, W_KV + (64 * pg + 16 * j + 8 * t + l8) * 4096 + (1984 - 64 * pa) * 2 + c8 * 16, 0, 16); });
    }
    const int mh = j & 1, db = j >> 1, s31 = lane & 31, hh = lane >> 5;
    const int orow = 64 * rt + 32 * mh + s31, ocol = 64 * ct + 32 * db;
    u32x2 xr[4];
    u32 sg;
    {
      const char* p1 = ws + W_SG + (hh ? orow : rb * 128 + 32 * j + s31) * 8;
      const char* p2 = ws + W_CG + ((has ? cg : 0) * 192 + 48 * j + (lane < 48 ? lane : 0)) * 8;
      u64 g1, g2;
      gld8_sc1(g1, p1); gld8_sc1(g2, p2);
      asm volatile("s_waitcnt vmcnt(0)" : "+v"(g1), "+v"(g2)::"memory");
      if (lds_arrive(CKV, lane) == 4 && lane == 0) __hip_atomic_store((GAS u64*)(ws + W_PF + b * 64), epoch, RLX_AGENT);
      STAMP(3);
#pragma unroll 1
      for (u32 it = 0; it < SPIN_CAP; ++it) {
        if (__all((u32)(g1 >> 32) == tag && (u32)(g2 >> 32) == tag)) break;
        __builtin_amdgcn_s_sleep(4);
        g1 = gld_agent(p1); g2 = gld_agent(p2);
      }
      sg = (u32)g1;
      if (has) {
        if (lane < 32) ldsw<f32x2>(ST + (32 * j + lane) * 8, f32x2{bflo(sg), __builtin_amdgcn_rcpf(bfhi(sg))});
        if (lane < 48) ldsw<u32>(CS + (48 * j + lane) * 4, (u32)g2);
      }
      lds_set(STRDY + j, 1);
      STAMP(4);
    }
    if (has) wait_min4<4>(FULL, S2AFTER);
    STAMP(24);
    {
      u64 f;
      gld8_sc1(f, ws + W_PF + ((b >> 5) * 32 + (j == 0 ? s31 : 0)) * 64);
      dma4(cb + (cp ? HB : HA), lane * 16, lane * 16, lane * 16, lane * 16, mk_rsrc(mkp(ka[12], ka[13]), D * S * 2), (2 * b + ch) * 4096);
      if (lane < (cp ? 33 : 16)) ldsw<u32>(cb + (cp ? 4160 : 0) + lane * 4, 0u);
      if (lane < (cp ? 28 : 30)) ldsw<u32x4>(kown + 4096 + lane * 16, u32x4{0u, 0u, 0u, 0u});
      asm volatile("s_waitcnt vmcnt(4)" : "+v"(f));
      sg = (u32)__builtin_amdgcn_permlane32_swap(sg, sg, false, false)[1];
      if (j == 0) { if (!__all(f == epoch)) poll_flags<2>(ws, W_PF + (b >> 5) * 32 * 64, 32, epoch, lane); lds_set(CVP + 4, 1); }
      wait_byte<1>(CVP + 4, 1);
      u32x4 kvr[4];
      const char* src = ws + W_KV + (2 * b + ch) * 4096 + 2 * cp;
      sfor<4>([&](auto t) AI { gld4_sc1<t * 1024>(kvr[t], lane * 16, src); });
      wait_min4<1>(FREE, 6);
      sfor<2>([&](auto t) AI {
        const int r = 16 * t + (lane >> 2);
        dma(ZST + j * 2048 + t * 1024, r * 1024 + (((lane & 3) ^ ((r >> 1) & 3)) << 4), rq, (64 * rt + 32 * mh) * 1024 + ocol * 2);
      });
      asm volatile("s_waitcnt vmcnt(0)" : "+v"(kvr[0]), "+v"(kvr[1]), "+v"(kvr[2]), "+v"(kvr[3])::"memory");
      if (cp && lane == 63) kvr[3].w &= 0xffffu;
      sfor<4>([&](auto t) AI { ldsw<u32x4>(kown + t * 1024 + lane * 16, kvr[t]); });
      sfor<4>([&](auto g) AI { xr[g] = ldsr<u32x2>(ZST + j * 2048 + s31 * 64 + ((g ^ ((s31 >> 1) & 3)) << 4) + 8 * hh); });
      waitl();
      pin(xr[0]); pin(xr[1]); pin(xr[2]); pin(xr[3]);
      lds_set(CVP + j, 2);
      STAMP(5);
    }
    __builtin_amdgcn_s_setprio(0);
    wait_min4<1>(FREE, CONVAFTER);
    wait_byte<0>(CVP + (j ^ 2), 2);
    __builtin_amdgcn_s_setprio(PRC);
    STAMP(6);
    f32x4 dacc[8];
    sfor<8>([&](auto i) AI { dacc[i] = f32x4{0.f, 0.f, 0.f, 0.f}; });
    u32 kv2[8];
    {
      u32 hb = cb + ((c16 & 1) ? HA : HB) + 4 * (c16 - 15) + 16 * q + 256 * cp;
      u32 kb_ = cb + ((c16 & 1) ? KA : KB) + 448 - 64 * (c16 >> 1) + 16 * q + 256 * cp;
      u32 hb2 = hb + 8;
      pin(hb); pin(hb2); pin(kb_);
      bf16x8 ta[12], kf[12];
      auto ld1 = [&](auto odd, auto n, auto w, auto off) AI {
        constexpr int r = (n < 4 && odd) ? 8 + n : n;
        if constexpr (w == 0) kf[r] = ldsr<bf16x8>(kb_ + 512 * (7 - n) + off);
        if constexpr (w == 1) { const u32x2 v = ldsr<u32x2>(hb + 512 * n + off); u32x4 t = __builtin_bit_cast(u32x4, ta[r]); t.x = v.x; t.y = v.y; ta[r] = __builtin_bit_cast(bf16x8, t); }
        if constexpr (w == 2) { const u32x2 v = ldsr<u32x2>(hb2 + 512 * n + off); u32x4 t = __builtin_bit_cast(u32x4, ta[r]); t.z = v.x; t.w = v.y; ta[r] = __builtin_bit_cast(bf16x8, t); }
      };
      auto cstep = [&](auto odd, auto off) AI {
        sfor<36>([&](auto g) AI {
          constexpr int c = cpair(35 - g) >> 3, dl = cpair(35 - g) & 7;
          constexpr int rc = (c < 4 && odd) ? 8 + c : c, rd_ = (dl < 4 && odd) ? 8 + dl : dl;
          dacc[c + dl] = __builtin_amdgcn_mfma_f32_16x16x32_bf16(kf[rc], ta[rd_], dacc[c + dl], 0, 0, 0);
          if constexpr (g < 12) ld1(IC<1 - odd>{}, IC<g / 3>{}, IC<g % 3>{}, off);
          else if constexpr (g < 24) ld1(IC<1 - odd>{}, IC<7 - (g - 12) / 3>{}, IC<g % 3>{}, off);
          GAP();
        });
      };
      sfor<24>([&](auto i) AI { ld1(IC<0>{}, IC<(i < 3 ? 0 : i < 6 ? 7 : i < 9 ? 1 : i < 12 ? 6 : i < 15 ? 2 : i < 18 ? 5 : i < 21 ? 3 : 4)>{}, IC<i % 3>{}, IC<0>{}); GAP(); });
#pragma unroll 1
      for (int i = 0; i < 2; ++i) {
        cstep(IC<0>{}, IC<64>{});
        cstep(IC<1>{}, IC<128>{});
        hb += 128; hb2 += 128; kb_ += 128;
      }
    }
    __builtin_amdgcn_s_setprio(3);
    STAMP(25);
    sfor<4>([&](auto i) AI { ldsw<f32x4>(XCH + j * 4096 + i * 1024 + lane * 16, cp ? dacc[i] : dacc[4 + i]); });
    lds_set(CVP + j, 4);
    {
      const int l = lane_id();
      u32 a = cb + KA + 2 * (2046 - 2 * (l & 15) - 64 * (l >> 4) - 1024 * cp) - 1600;
      pin(a);
      sfor<8>([&](auto i) AI { kv2[i] = ldsr<u32>(a + 64 * (1 - (i & 1)) + 512 * (3 - (i >> 1))); });
    }
    STAMP(7);
    {
      const float dv = bflo(dvv);
      sfor<8>([&](auto i) AI { kv2[i] = pk2s(bfhi(kv2[i]) * dv, bflo(kv2[i]) * dv); pin(kv2[i]); });
      wait_byte<0>(CVP + (j ^ 2), 4);
      f32x4 o[4];
      sfor<4>([&](auto i) AI { o[i] = ldsr<f32x4>(XCH + (j ^ 2) * 4096 + i * 1024 + lane * 16); });
      asm volatile("" ::: "memory");
      lds_set(CVP + j, 5);
      sfor<4>([&](auto i) AI {
        const f32x4 m = cp ? dacc[4 + i] : dacc[i];
        sfor<2>([&](auto rr) AI {
          const u32 yb = pk2s(m[2 * rr] + o[i][2 * rr], m[2 * rr + 1] + o[i][2 * rr + 1]), kd = kv2[2 * i + rr];
          ldsw<u32>(ZST + j * 2048 + 4 * c16 + 128 * q + 64 * rr + 512 * i, pk2s(bflo(yb) + bflo(kd), bfhi(yb) + bfhi(kd)));
        });
      });
      waitl();
      u32x2 zo[4];
      sfor<4>([&](auto t) AI { zo[t] = ldsr<u32x2>(ZST + j * 2048 + t * 512 + lane * 8); });
      sfor<4>([&](auto t) AI {
        __builtin_amdgcn_raw_buffer_store_b128(u32x4{zo[t].x, tag, zo[t].y, tag}, rws, lane * 16 + W_ZT + (2 * b + ch) * 8192 + cp * 4096 + t * 1024, 0, 16);
      });
    }
    STAMP(8);
    const float mean = bflo(sg), rs = __builtin_amdgcn_rcpf(bfhi(sg));
    u32 xn[8];
    sfor<8>([&](auto i) AI {
      const u32 x2 = xr[i >> 1][i & 1], n2 = pk2s(bflo(x2) - mean, bfhi(x2) - mean);
      xn[i] = pk2s(bflo(n2) * rs, bfhi(n2) * rs);
      pin(xn[i]);
    });
    if (b == 0 && j == 3) {
      sfor<4>([&](auto t) AI { poll_flags<1>(ws, W_PF + t * 4096, 64, epoch, lane); });
      if (lane == 0) __hip_atomic_store((GAS u64*)(ws + W_E), epoch, RLX_AGENT);
    }
    wait_min4<2>(FREE, ZAFTER);
    u32x4 zr[4];
#pragma unroll 1
    for (u32 it = 0; it < SPIN_CAP; ++it) {
      sfor<4>([&](auto t) AI { bld4_sc1<0>(zr[t], l8 * 8192 + c8 * 16, rws, W_ZT + (ocol + 8 * t) * 8192 + (64 * rt + 32 * mh) * 4); });
      asm volatile("s_waitcnt vmcnt(0)" : "+v"(zr[0]), "+v"(zr[1]), "+v"(zr[2]), "+v"(zr[3])::"memory");
      u32 bad = 0;
      sfor<4>([&](auto t) AI { bad |= (zr[t].y ^ tag) | (zr[t].w ^ tag); });
      if (__all(bad == 0u)) break;
      __builtin_amdgcn_s_sleep(ZSLEEP);
    }
    STAMP(10);
    sfor<4>([&](auto t) AI { ldsw<u32x2>(ZST + j * 2048 + t * 512 + lane * 8, u32x2{zr[t].x, zr[t].z}); });
    waitl();
    float tt[16];
    sfor<4>([&](auto g) AI {
      const u32x2 z4 = ldtr(ZST + j * 2048 + (8 * g + 4 * hh + ((lane & 15) >> 2)) * 64 + 32 * ((lane >> 4) & 1) + 8 * (lane & 3));
      sfor<2>([&](auto e) AI {
        const u32 x2 = xr[g][(int)e], z2 = z4[(int)e];
        u32 n2 = pk2s(bflo(xn[2 * g + e]) * bflo(z2), bfhi(xn[2 * g + e]) * bfhi(z2));
        n2 = pk2s(bflo(x2) + bflo(n2), bfhi(x2) + bfhi(n2));
        tt[4 * g + 2 * e] = bflo(n2); tt[4 * g + 2 * e + 1] = bfhi(n2);
      });
    });
    sfor<16>([&](auto i) AI { pin(tt[i]); });
    STAMP(11);
    wait_min4<0>(FREE, 12);
    STAMP(12);
    u32x2 ob[4];
    sfor<4>([&](auto g) AI {
      const f32x4 v = ldsr<f32x4>(PART + mh * 8192 + db * 4096 + g * 1024 + lane * 16) + ldsr<f32x4>(PART + (2 + mh) * 8192 + db * 4096 + g * 1024 + lane * 16);
      ob[g].x = pk2(tt[4 * g] + rs * v.x, tt[4 * g + 1] + rs * v.y);
      ob[g].y = pk2(tt[4 * g + 2] + rs * v.z, tt[4 * g + 3] + rs * v.w);
    });
    sfor<2>([&](auto gp) AI {
      auto s0 = __builtin_amdgcn_permlane32_swap(ob[2 * gp].x, ob[2 * gp + 1].x, false, false);
      auto s1 = __builtin_amdgcn_permlane32_swap(ob[2 * gp].y, ob[2 * gp + 1].y, false, false);
      const u32x4 v = {(u32)s0[0], (u32)s1[0], (u32)s0[1], (u32)s1[1]};
      *(GAS u32x4*)((GAS char*)mkp(kb[2], kb[3]) + orow * 1024 + ocol * 2 + 32 * gp + 16 * hh) = v;
    });
    STAMP(13);
    DONE();
  } else if (wave < 4) {
    u32 junk, junk2;
    asm volatile("s_load_dword %0, %2, %3\n\ts_load_dword %1, %2, %4" : "=&s"(junk), "=&s"(junk2)
                 : "s"(pc0 & ~127ull), "s"((u32)(li + 32 * j) * 128u), "s"(min((u32)(li + 32 * (4 + j)) * 128u, CODE_BYTES)));
    const rsrc_t rws = mk_rsrc(ws, W_END);
    const int wm = wave >> 1, wn = wave & 1;
    const int l0 = lane_id();
    u32 wa = ((l0 >> 4) & 1) * (12288 + UPAD) + (8 * (l0 >> 5) + ((l0 & 15) >> 2)) * 192 + 96 * wn + 8 * (l0 & 3);
    u64 epoch;
    f32x16 acc[2][3];
    sfor<2>([&](auto m) AI { sfor<3>([&](auto n) AI { sfor<16>([&](auto r) AI { acc[m][n][(int)r] = 0.f; }); pin(acc[m][n]); }); });
    u32 xa[4];
    sfor<4>([&](auto ks) AI { xa[ks] = (64 * wm + (l0 & 31)) * 128 + (((2 * ks + (l0 >> 5)) ^ ((l0 >> 1) & 7)) << 4); pin(xa[ks]); });
    asm volatile("s_waitcnt lgkmcnt(0)" : "+s"(junk), "+s"(junk2));
    STAMP(10);
    if (has) {
      bf16x8 xf[2][2], wf[2][3];
      auto ldw = [&](auto st, auto n) AI { const u32 a = wa + G_OFF + (st & 3) * 3072 + n * 32; wf[st & 1][n] = cat(ldtr(a), ldtr(a + 768)); };
      auto ldx = [&](auto st, auto m) AI { xf[st & 1][m] = ldsr<bf16x8>(xa[st & 3] + m * 4096); };
      wait_min4<1>(FULL, 1);
      u32 accw = 0, cntw = 0;
      STAMP(1);
      ldw(IC<0>{}, IC<0>{}); ldx(IC<0>{}, IC<0>{}); ldx(IC<0>{}, IC<1>{}); ldw(IC<0>{}, IC<1>{}); ldw(IC<0>{}, IC<2>{});
      ldw(IC<1>{}, IC<0>{}); ldw(IC<1>{}, IC<1>{}); ldx(IC<1>{}, IC<0>{});
      GAP();
      u32 needw = 0x01010101u, off = 0;
#pragma unroll 1
      for (int c = 0; c < 8; ++c) {
        u32 fl, adv, have;
        sfor<24>([&](auto gg) AI {
          constexpr int st = gg / 6, g = gg % 6, n = g >> 1, m = g & 1;
          acc[m][n] = __builtin_amdgcn_mfma_f32_32x32x16_bf16(wf[st & 1][n], xf[st & 1][m], acc[m][n], 0, 0, 0);
          if constexpr (g == 0) { ldw(IC<st + 1>{}, IC<2>{}); ldx(IC<st + 1>{}, IC<1>{}); }
          if constexpr (g == 2) ldw(IC<st + 2>{}, IC<0>{});
          if constexpr (g == 4) ldw(IC<st + 2>{}, IC<1>{});
          if constexpr (g == 5) ldx(IC<st + 2>{}, IC<0>{});
          if constexpr (st == 0 && g == 3) { const u32 o2 = off == 2 * SLOT ? 0u : off + SLOT; adv = o2 - off; off = o2; needw += 0x01010101u; spin(adv); spin(off); spin(needw); }
          if constexpr (st == 1 && g == 1) fl = lds_ld1(FULL);
          if constexpr (st == 1 && g == 3) { xa[0] += adv; xa[1] += adv; xa[2] += adv; pin(xa[0]); pin(xa[1]); pin(xa[2]); }
          if constexpr (st == 2 && g == 0) { have = (rfl(fl) - needw) & 0x80808080u; spin(have); }
          if constexpr (st == 2 && g == 1) {
            xa[3] += adv; wa += adv; pin(xa[3]); pin(wa);
            if (__builtin_expect(have != 0u, 0)) wait_min4<0>(FULL, c + 2);
          }
          if constexpr (st == 3 && g == 1) lds_set(FREE + wave, c + 1);
          GAP();
        });
      }
      STAMP(2);
      __builtin_amdgcn_s_setprio(PRE);
      const int lane = lane_id(), s31 = lane & 31, hh = lane >> 5;
      f32x2 st[2];
      f32x4 ca[4];
      auto ldc = [&](auto n) AI { sfor<4>([&](auto g) AI { const u32 a = CS + (wn * 96 + n * 32 + 8 * g + 4 * hh) * 4; ca[g] = ldsr<f32x4>(a); }); };
#pragma unroll 1
      for (u32 it = 0; it < SPIN_CAP * 16; ++it) {
        const u32 r1 = lds_ld1(STRDY);
        asm volatile("" ::: "memory");
        const u32x2 e = lds_ld2(EPO);
        sfor<2>([&](auto m) AI { st[m] = ldsr<f32x2>(ST + (64 * wm + 32 * m + s31) * 8); });
        ldc(IC<0>{});
        asm volatile("" ::: "memory");
        epoch = ((u64)rfl(e.y) << 32) | rfl(e.x);
        if (__builtin_expect(all_ge(rfl(r1), 1), 1)) break;
      }
      STAMP(11);
      sfor<3>([&](auto n) AI {
        const int ks = 6 * cg + 3 * wn + n;
        if (ks < 86) {
          f32x4 c4[4];
          sfor<4>([&](auto g) AI { c4[g] = ca[g]; });
          f32x2 g[8], u[8], e[8];
          sfor<8>([&](auto i) AI {
            constexpr int m = i >> 2, r = 2 * (i & 3);
            const f32x2 cg2 = {c4[r >> 2][r & 3], c4[r >> 2][(r & 3) + 1]};
            g[i] = f32x2{acc[m][n][r], acc[m][n][r + 1]} - st[m].x * cg2;
          });
          GAP();
          sfor<8>([&](auto i) AI { g[i] = g[i] * st[i >> 2].y; });
          GAP();
          sfor<8>([&](auto i) AI { e[i] = g[i] * g[i]; });
          GAP();
          sfor<8>([&](auto i) AI { e[i] = e[i] * -0.1029432397f - 2.3022081985f; });
          GAP();
          sfor<8>([&](auto i) AI { e[i] = e[i] * g[i]; });
          GAP();
          sfor<8>([&](auto i) AI {
            constexpr int m = i >> 2, r = 2 * (i & 3);
            const f32x2 cu2 = {c4[2 + (r >> 2)][r & 3], c4[2 + (r >> 2)][(r & 3) + 1]};
            e[i] = f32x2{__builtin_amdgcn_exp2f(e[i].x), __builtin_amdgcn_exp2f(e[i].y)};
            u[i] = f32x2{acc[m][n][r + 8], acc[m][n][r + 9]} - st[m].x * cu2;
            GAP();
          });
          if constexpr (n < 2) ldc(IC<n + 1>{});
          sfor<8>([&](auto i) AI { e[i] = e[i] + 1.0f; });
          GAP();
          sfor<8>([&](auto i) AI {
            e[i] = f32x2{__builtin_amdgcn_rcpf(e[i].x), __builtin_amdgcn_rcpf(e[i].y)};
            g[i] = g[i] * u[i];
            GAP();
          });
          sfor<8>([&](auto i) AI { g[i] = g[i] * e[i]; });
          GAP();
          sfor<2>([&](auto m) AI {
            const u32x4 o = {pk2(g[4 * m].x, g[4 * m].y), pk2(g[4 * m + 1].x, g[4 * m + 1].y), pk2(g[4 * m + 2].x, g[4 * m + 2].y), pk2(g[4 * m + 3].x, g[4 * m + 3].y)};
            __builtin_amdgcn_raw_buffer_store_b128(o, rws, lane * 16 + W_H + ((4 * rb + 2 * wm + m) * 86 + ks) * 1024, 0, 16);
          });
          GAP();
          if constexpr (n < 2) STAMP(20 + n);
        }
      });
      lds_set(FREE + wave, 9);
      STAMP(3);
      waitv<0>();
      __builtin_amdgcn_s_setprio(PRW);
      if (lds_arrive(CNT + 0, lane) == 4 && lane == 0) __hip_atomic_store((GAS u64*)(ws + W_HF + (rb * 16 + cg) * 64), epoch, RLX_AGENT);
      STAMP(4);
    } else {
      if (csw) {
        f32x4 cs[3];
        sfor<3>([&](auto n) AI { cs[n] = f32x4{0.f, 0.f, 0.f, 0.f}; });
        const u32 o2_ = (l0 & 15) == ((l0 >> 4) & 1) ? 0x3f803f80u : 0u;
        const bf16x8 sel = __builtin_bit_cast(bf16x8, u32x4{o2_, o2_, o2_, o2_});
        wa += wm * 6144;
#pragma unroll 1
        for (int c = 0; c < 8; ++c) {
          wait_min4<1>(FULL, c + 1);
          bf16x8 w[6];
          sfor<6>([&](auto i) AI { const u32 a = wa + (i / 3) * 3072 + (i % 3) * 32; w[i] = cat(ldtr(a), ldtr(a + 768)); });
          asm volatile("" ::: "memory");
          lds_set(FREE + wave, c < 3 ? c + 1 : 3);
          sfor<6>([&](auto i) AI { cs[i % 3] = __builtin_amdgcn_mfma_f32_16x16x32_bf16(w[i], sel, cs[i % 3], 0, 0, 0); });
          wa += c == 4 ? (u32)(-4 * (int)CSLOT) : CSLOT;
        }
        if ((l0 & 15) < 2) sfor<3>([&](auto n) AI { ldsw<f32x4>(CS + (wm * 192 + wn * 96 + n * 32 + (l0 & 15) * 16 + 4 * (l0 >> 4)) * 4, cs[n]); });
      }
      lds_set(FREE + wave, 9);
      epoch = wait_epoch();
      if (csw && wave < 3) {
        wait_min4<1>(FREE, 9);
        const u32 i = 64 * wave + l0;
        const float v = ldsr<float>(CS + i * 4) + ldsr<float>(CS + 768 + i * 4);
        __hip_atomic_store((GAS u64*)(ws + W_CG + (cg * 192 + i) * 8), ((u64)(u32)(epoch >> 17) << 32) | __float_as_uint(v), RLX_AGENT);
      }
      STAMP(4);
    }
    const int lane = lane_id(), hh = lane >> 5, g16 = lane >> 4, q4 = (lane & 15) >> 2, p4 = lane & 3;
    const int kh = wave >> 1, mh = wave & 1;
    u32x4 hf[43];
    const u32 hso = W_H + ((2 * rt + mh) * 86 + 43 * kh) * 1024, hvo = lane * 16;
    f32x16 o2[2];
    sfor<2>([&](auto n) AI { sfor<16>([&](auto r) AI { o2[n][(int)r] = 0.f; }); pin(o2[n]); });
    u32 db_[2][3];
    sfor<2>([&](auto n) AI {
      const u32 da = kh * 8192 + (4 * hh + q4) * 128 + ((4 * ((int)n ^ (q4 >> 1)) + 2 * (g16 & 1) + (p4 >> 1)) << 4) + 8 * (p4 & 1);
      sfor<3>([&](auto w) AI { db_[n][w] = da + w * 61440; pin(db_[n][w]); });
    });
    bf16x8 df[3][2];
    u32 fl;
    auto ldd = [&](auto k) AI {
      constexpr int c = k >> 2, ks = k & 3;
      constexpr u32 wb = (c < 7 ? 16384u * c : c == 7 ? CONV : c == 8 ? CONV + CH_SZ : 16384u * (c - 9)) + ks * 2048;
      constexpr int w = wb / 61440 > 2 ? 2 : wb / 61440;
      sfor<2>([&](auto n) AI { const u32 a = db_[n][w] + (wb - w * 61440); df[k % 3][n] = cat(ldtr(a), ldtr(a + 1024)); });
    };
    if (wave == 0) { poll_flags<1>(ws, W_HF + (rt >> 1) * 16 * 64, 15, epoch, lane); lds_set(CVP + 6, 1); }
    else wait_byte<WSL>(CVP + 6, 1);
    __builtin_amdgcn_s_setprio(2);
    STAMP(5);
    auto hload = [&](auto k) AI { bld4_sc1<(k & 3) * 1024>(hf[k], hvo, rws, hso + (k >> 2) * 4096); };
    sfor<PD>([&](auto k) AI { hload(k); });
    wait_min4<0>(WDRDY, 1);
    STAMP(6);
    ldd(IC<0>{}); ldd(IC<1>{});
    GAP();
    sfor<43>([&](auto k) AI {
      if constexpr (k == 10) lds_set(FREE + wave, 10);
      if constexpr (k == 18) lds_set(FREE + wave, 11);
      if constexpr (k == 20 || k == 32) fl = lds_ld1(WDRDY);
      if constexpr (k == 40) fl = lds_ld1(FREE);
      if constexpr (k == 22 || k == 34) { STAMP(k == 22 ? 12 : 14); if (__builtin_expect(!all_ge(rfl(fl), k == 22 ? 2u : 3u), 0)) wait_min4<0>(WDRDY, k == 22 ? 2 : 3); STAMP(k == 22 ? 13 : 15); GAP(); }
      constexpr int younger = (k + PD <= 43 ? PD : 43 - k) - 1;
      waitv_pin<younger>(hf[k]);
      if constexpr (k == 0) STAMP(24);
      if constexpr (k == 8) STAMP(25);
      if constexpr (k == 16) STAMP(26);
      o2[0] = __builtin_amdgcn_mfma_f32_32x32x16_bf16(df[k % 3][0], __builtin_bit_cast(bf16x8, hf[k]), o2[0], 0, 0, 0);
      if constexpr (k + 2 < 43) ldd(IC<k + 2>{});
      GAP();
      o2[1] = __builtin_amdgcn_mfma_f32_32x32x16_bf16(df[k % 3][1], __builtin_bit_cast(bf16x8, hf[k]), o2[1], 0, 0, 0);
      if constexpr (k + PD < 43) hload(IC<k + PD>{});
      GAP();
    });
    STAMP(7);
    if (!all_ge(rfl(fl), 11u)) wait_min4<0>(FREE, 11);
    sfor<2>([&](auto n) AI { sfor<4>([&](auto g) AI {
      f32x4 v = {o2[n][4 * g], o2[n][4 * g + 1], o2[n][4 * g + 2], o2[n][4 * g + 3]};
      ldsw<f32x4>(PART + wave * 8192 + n * 4096 + g * 1024 + lane * 16, v);
    }); });
    waitl();
    lds_set(FREE + wave, 12);
    STAMP(8);
    DONE();
  } else {
    __builtin_amdgcn_s_setprio(PRL);
    const rsrc_t rg = mk_rsrc(mkp(ka[2], ka[3]), D * F * 2), ru = mk_rsrc(mkp(ka[4], ka[5]), D * F * 2);
    const int crot = ROT * ((rb + cg) & 1);
    auto chx = [&](int c, u32 sb) AI { dma4(sb + 4 * j * 1024, xv[0], xv[1], xv[2], xv[3], rq, (rb * 128 + 32 * j) * 1024 + (c ^ crot) * 128); };
    auto chw = [&](int c, u32 sb) AI {
      dma3(sb + G_OFF + 3 * j * 1024, wv[0], wv[1], wv[2], rg, (c ^ crot) * 64 * F * 2);
      dma3(sb + U_OFF + 3 * j * 1024, wv[0], wv[1], wv[2], ru, (c ^ crot) * 64 * F * 2);
    };
    if (has) {
      chx(0, 0); chw(0, 0); chx(1, SLOT); chw(1, SLOT);
      int ic = 2, lc = 0;
      u32 sb = 2 * SLOT;
#pragma unroll 1
      for (u32 it = 0; it < SPIN_CAP * 16; ++it) {
        if (lc < ic && vm_pending() <= 10u * (u32)(ic - lc - 1)) {
          asm volatile("" ::: "memory");
          ++lc;
          lds_set(FULL + j, lc == 8 ? 9 : lc);
          if (lc == 1) STAMP(1);
          if (lc == 8) break;
        } else if (ic < 8 && all_ge(rfl(lds_ld1(FREE)), ic - 2)) {
          asm volatile("" ::: "memory");
          chx(ic, sb);
          if (lc < ic && vm_pending() <= 10u * (u32)(ic - lc - 1) + 4u) {
            lds_set(FULL + j, ++lc);
          }
          chw(ic, sb);
          ++ic;
          sb = sb == 2 * SLOT ? 0 : sb + SLOT;
        } else {
          __builtin_amdgcn_s_sleep(LSLEEP);
        }
      }
      waitv<0>();
    } else if (csw) {
      sfor<8>([&](auto c) AI {
        if constexpr (c >= 5) { waitv<24>(); lds_set(FULL + j, (6u * c - vm_pending()) * 43u >> 8); wait_min4<1>(FREE, c - 4); }
        chw(c, (c % 5) * CSLOT - G_OFF);
        if constexpr (c >= 2) lds_set(FULL + j, (6u * (c + 1) - vm_pending()) * 43u >> 8);
      });
      waitv<12>(); lds_set(FULL + j, 6);
      waitv<6>(); lds_set(FULL + j, 7);
      waitv<0>(); lds_set(FULL + j, 8);
    }
    STAMP(2);
    const rsrc_t rd = mk_rsrc(mkp(kb[0], kb[1]), F * D * 2);
    u32 dv[4];
    sfor<4>([&](auto t) AI { dv[t] = (7 * t + l8) * 1024 + ((c8 ^ (((l8 >> 1) & 1) << 2)) << 4) + ct * 128; });
    auto wso = [&](int g) AI { return (u32)(((g >> 1) & 1) * 688 + (g >> 2) * 64 + (g & 1) * 32) * 1024u; };
    auto wd_groups = [&](int lo, int hi) AI {
#pragma unroll 1
      for (int g = lo + j; g < hi; g += 4) { dma4(g * 4096, dv[0], dv[1], dv[2], dv[3], rd, wso(g)); waitv<WDFL>(); }
    };
    u32x4 wr[5][4];
    auto wst = [&](auto t, u32 a) AI { sfor<4>([&](auto i) AI { pin(wr[t][i]); ldsw<u32x4>(a + j * 4096 + i * 1024 + lane * 16, wr[t][i]); }); };
    auto wrl = [&]() AI { sfor<5>([&](auto t) AI { const u32 so = wso(24 + 4 * t + j); sfor<4>([&](auto i) AI { bld4o<i * 1024>(wr[t][i], dv[i], rd, so); }); waitv<WRFL>(); }); };
    wrl();
    wait_min4<1>(FREE, WD1);
    wd_groups(21, 24);
    wait_min4<1>(FREE, WD2);
    wd_groups(0, 10);
    wait_min4<1>(FREE, WD3);
    wd_groups(10, 21);
    waitv<0>();
    lds_set(WDRDY + j, 1);
    STAMP(3);
    wait_min4<4>(CVP, 5);
    wst(IC<0>{}, XCH); wst(IC<1>{}, CONV); wst(IC<2>{}, CONV + CH_SZ);
    waitl();
    lds_set(WDRDY + j, 2);
    STAMP(4);
    wait_min4<2>(FREE, 10);
    wst(IC<3>{}, 0); wst(IC<4>{}, 16384);
    waitl();
    lds_set(WDRDY + j, 3);
    STAMP(5);
    DONE();
  }
}

extern "C" void kernel_launch(void* const* d_in, const int* in_sizes, int n_in, void* d_out, int out_size, void* d_ws, size_t ws_size,
                              hipStream_t stream) {
  Args a;
  a.q = (const u16*)d_in[0]; a.k = (const u16*)d_in[1]; a.v = (const u16*)d_in[2]; a.sk = (const u16*)d_in[3];
  a.h = (const u16*)d_in[4]; a.dv = (const u16*)d_in[5];
  a.wg = (const u16*)d_in[7]; a.wu = (const u16*)d_in[8]; a.wd = (const u16*)d_in[9];
  a.out = (u16*)d_out; a.ws = (char*)d_ws; a.pad = 0;
  (void)hipFuncSetAttribute((const void*)fused, hipFuncAttributeMaxDynamicSharedMemorySize, LDS_BYTES);
  fused<<<256, 768, LDS_BYTES, stream>>>(a);
}
```

```cpp
#define AMPID 3
#define AMPWAVE 0
#define WRLAST 9
#define WD1 9
#define WD2 9
#define WD3 9
#include <hip/hip_runtime.h>
#include <stdint.h>
#include <utility>

typedef unsigned short u16;
typedef unsigned int u32;
typedef unsigned long long u64;
typedef __bf16 bf16x8 __attribute__((ext_vector_type(8)));
typedef __bf16 bf16x2 __attribute__((ext_vector_type(2)));
typedef short s16x4 __attribute__((ext_vector_type(4)));
typedef short s16x8 __attribute__((ext_vector_type(8)));
typedef float f32x16 __attribute__((ext_vector_type(16)));
typedef float f32x4 __attribute__((ext_vector_type(4)));
typedef float f32x2 __attribute__((ext_vector_type(2)));
typedef u32 u32x4 __attribute__((ext_vector_type(4)));
typedef u32 u32x2 __attribute__((ext_vector_type(2)));
typedef __amdgpu_buffer_rsrc_t rsrc_t;
#define DI __device__ __forceinline__
#define AI __attribute__((always_inline))
#define LAS __attribute__((address_space(3)))
#define GAS __attribute__((address_space(1)))
#define RLX_AGENT __ATOMIC_RELAXED, __HIP_MEMORY_SCOPE_AGENT
#define RLX_WG __ATOMIC_RELAXED, __HIP_MEMORY_SCOPE_WORKGROUP

constexpr int S = 2048, D = 512, F = 1376;
#define UPAD 32
#define SLOTB (40960 + UPAD)
constexpr u32 SLOT = SLOTB, G_OFF = 16384, U_OFF = 16384 + 12288 + UPAD;
constexpr u32 CSLOT = 2 * (12288 + UPAD) + 64 - 2 * UPAD % 64;
constexpr u32 CONV = 3 * 41216;
constexpr u32 CH_SZ = 17520, HA = 64, HB = 4292, KA = 8400, KB = 12976, XB = 4304;
constexpr u32 MISC = CONV + 2 * CH_SZ;
constexpr u32 CS = MISC, ST = MISC + 1536, CNT = MISC + 2560;
constexpr u32 CKV = CNT + 8, CSP = CNT + 12, EPO = CNT + 16, CVP = CNT + 24, FULL = CNT + 32, FREE = CNT + 36, STRDY = CNT + 40, WDRDY = CNT + 44;
constexpr u32 LDS_BYTES = MISC + 2816;
static_assert(LDS_BYTES <= 163840, "LDS");
constexpr u32 XCH = 98304, ZST = 114688, PART = 32768;
constexpr u32 W_E = 0, W_PF = 4096, W_HF = W_PF + 16384, W_ZF = W_HF + 16384, W_SG = W_ZF + 16384, W_CG = W_SG + 16384;
constexpr u32 W_KV = 131072, W_ZT = W_KV + 2097152 + 4096, W_H = W_ZT + 4194304, W_END = W_H + 64 * 86 * 1024;
constexpr u32 W_DBG = 16u << 20;
constexpr u32 CODE_BYTES = 26112;
#define PRL 1
#define PRS 0
#define CONVAFTER 9
#define LSLEEP 1
#define PRA 0
#define ROT 0
#define S2AFTER 8
#define WDFL 2
#define KVSTAG 0
#define SSTOP 9
#define POLLS 4
#define PD 10
#define PFLO 8
#define PFPRIO 0
#define PFWAIT 0
#define KVMODE 0
#define PRE 2
#define PRW 0
#define WSL 1
#define PRC 3
#define ZSLEEP 4
#define ZAFTER 11
#define WRFL 40
#define KVAFTER 1
constexpr u32 SPIN_CAP = 1u << 18;

constexpr int cpair(int g) {
  int i = 0;
  for (int n = 0; n < 8; ++n) for (int m = 0; m <= n; ++m) if (m + n <= 7) {
    if (i++ == g) return n * 8 + m;
    if (m < n && i++ == g) return m * 8 + n;
  }
  return 0;
}
template <class Fn, int... I> DI void sfor_impl(Fn&& f, std::integer_sequence<int, I...>) { (f(std::integral_constant<int, I>{}), ...); }
template <int N, class Fn> DI void sfor(Fn&& f) { sfor_impl(f, std::make_integer_sequence<int, N>{}); }

DI rsrc_t mk_rsrc(const void* p, u32 bytes) { return __builtin_amdgcn_make_buffer_rsrc((void*)p, (short)0, (int)bytes, 0x00020000); }
DI void dma(u32 lds, u32 voff, rsrc_t r, u32 soff) {
  asm volatile("s_mov_b32 m0, %0\n\ts_nop 0\n\tbuffer_load_dwordx4 %1, %2, %3 offen lds" ::"s"(lds), "v"(voff), "s"(r), "s"(soff) : "memory");
}

DI void dma4(u32 lds, u32 v0, u32 v1, u32 v2, u32 v3, rsrc_t r, u32 soff) {
  asm volatile("s_mov_b32 m0, %0\n\ts_nop 0\n\tbuffer_load_dwordx4 %1, %5, %6 offen lds\n\tbuffer_load_dwordx4 %2, %5, %6 offen offset:1024 lds\n\t"
               "buffer_load_dwordx4 %3, %5, %6 offen offset:2048 lds\n\tbuffer_load_dwordx4 %4, %5, %6 offen offset:3072 lds"
               ::"s"(lds), "v"(v0), "v"(v1), "v"(v2), "v"(v3), "s"(r), "s"(soff) : "memory");
}
DI void dma3(u32 lds, u32 v0, u32 v1, u32 v2, rsrc_t r, u32 soff) {
  asm volatile("s_mov_b32 m0, %0\n\ts_nop 0\n\tbuffer_load_dwordx4 %1, %4, %5 offen lds\n\tbuffer_load_dwordx4 %2, %4, %5 offen offset:1024 lds\n\t"
               "buffer_load_dwordx4 %3, %4, %5 offen offset:2048 lds"
               ::"s"(lds), "v"(v0), "v"(v1), "v"(v2), "s"(r), "s"(soff) : "memory");
}
template <int OFF> DI void bld4_sc1(u32x4& d, u32 voff, rsrc_t r, u32 soff) {
  asm volatile("buffer_load_dwordx4 %0, %1, %2, %3 offen offset:%4 sc1" : "=v"(d) : "v"(voff), "s"(r), "s"(soff), "i"(OFF));
}
template <int OFF> DI void bld4o(u32x4& d, u32 voff, rsrc_t r, u32 soff) { asm volatile("buffer_load_dwordx4 %0, %1, %2, %3 offen offset:%4" : "=v"(d) : "v"(voff), "s"(r), "s"(soff), "i"(OFF)); }
DI void bld4(u32x4& d, u32 voff, rsrc_t r, u32 soff) { asm volatile("buffer_load_dwordx4 %0, %1, %2, %3 offen" : "=v"(d) : "v"(voff), "s"(r), "s"(soff)); }
template <int OFF> DI void bld2(u32x2& d, u32 voff, rsrc_t r) { asm volatile("buffer_load_dwordx2 %0, %1, %2, 0 offen offset:%3" : "=v"(d) : "v"(voff), "s"(r), "i"(OFF)); }
template <int OFF> DI void gld4_sc1(u32x4& d, u32 voff, const char* base) {
  asm volatile("global_load_dwordx4 %0, %1, %2 offset:%3 sc1" : "=v"(d) : "v"(voff), "s"(base), "i"(OFF));
}
template <int N> DI void waitv_pin(u32x4& d) { asm volatile("s_waitcnt vmcnt(%1)" : "+v"(d) : "i"(N)); }
template <int N> DI void waitv() { asm volatile("s_waitcnt vmcnt(%0)" ::"i"(N) : "memory"); }
DI u32 vm_pending() { u32 r; asm volatile("s_getreg_b32 %0, hwreg(HW_REG_IB_STS)" : "=s"(r)::"memory"); return (r & 15u) | ((r >> 18) & 48u); }
DI void bar() { asm volatile("s_waitcnt lgkmcnt(0)\n\ts_barrier" ::: "memory"); }
DI void waitl() { asm volatile("s_waitcnt lgkmcnt(0)" ::: "memory"); }
DI float bflo(u32 x) { return __uint_as_float(x << 16); }
DI float bfhi(u32 x) { return __uint_as_float(x & 0xffff0000u); }
DI u32 pk2(float lo, float hi) { f32x2 v = {lo, hi}; return __builtin_bit_cast(u32, __builtin_convertvector(v, bf16x2)); }
DI u32 pk2s(float lo, float hi) { asm("" : "+v"(lo)); asm("" : "+v"(hi)); return pk2(lo, hi); }
DI float rbf(float x) { return bflo(pk2(x, x)); }
template <class T> DI T ldsr(u32 a) { return *(const LAS T*)a; }
template <class T> DI void ldsw(u32 a, T v) { *(LAS T*)a = v; }
DI u32x2 ldtr(u32 a) { return __builtin_bit_cast(u32x2, __builtin_amdgcn_ds_read_tr16_b64_v4i16((LAS s16x4*)a)); }
DI bf16x8 cat(u32x2 a, u32x2 b) { u32x4 v = {a.x, a.y, b.x, b.y}; return __builtin_bit_cast(bf16x8, v); }
template <int CTRL, int RM> DI float dpp_add(float v) {
  return v + __builtin_bit_cast(float, __builtin_amdgcn_update_dpp(0, __builtin_bit_cast(int, v), CTRL, RM, 0xf, false));
}
DI float wave_sum(float v) {
  v = dpp_add<0x111, 0xf>(v); v = dpp_add<0x112, 0xf>(v); v = dpp_add<0x114, 0xf>(v); v = dpp_add<0x118, 0xf>(v);
  v = dpp_add<0x142, 0xa>(v); v = dpp_add<0x143, 0xc>(v);
  return __builtin_bit_cast(float, __builtin_amdgcn_readlane(__builtin_bit_cast(int, v), 63));
}
DI void wave_sum2(float& a, float& c) {
  a = dpp_add<0x111, 0xf>(a); c = dpp_add<0x111, 0xf>(c); a = dpp_add<0x112, 0xf>(a); c = dpp_add<0x112, 0xf>(c);
  a = dpp_add<0x114, 0xf>(a); c = dpp_add<0x114, 0xf>(c); a = dpp_add<0x118, 0xf>(a); c = dpp_add<0x118, 0xf>(c);
  a = dpp_add<0x142, 0xa>(a); c = dpp_add<0x142, 0xa>(c); a = dpp_add<0x143, 0xc>(a); c = dpp_add<0x143, 0xc>(c);
  a = __builtin_bit_cast(float, __builtin_amdgcn_readlane(__builtin_bit_cast(int, a), 63));
  c = __builtin_bit_cast(float, __builtin_amdgcn_readlane(__builtin_bit_cast(int, c), 63));
}
DI void gld8_sc1(u64& d, const char* p) { asm volatile("global_load_dwordx2 %0, %1, off sc1" : "=v"(d) : "v"(p)); }
DI void gld_u16(u32& d, u32 voff, const char* base) { asm volatile("global_load_ushort %0, %1, %2" : "=v"(d) : "v"(voff), "s"(base)); }
template <int SLEEP> DI void poll_flags(const char* ws, u32 off, int n, u64 epoch, int lane) {
  const GAS u64* p = (const GAS u64*)(ws + off + (u32)(lane < n ? lane : 0) * 64u);
#pragma unroll 1
  for (u32 it = 0; it < SPIN_CAP; ++it) {
    u64 v = __hip_atomic_load(p, RLX_AGENT);
    if (__all(v == epoch)) break;
    __builtin_amdgcn_s_sleep(SLEEP);
  }
  asm volatile("" ::: "memory");
}
DI u32 poll_granule(const char* ws, u32 row, u32 tag) {
  const GAS u64* p = (const GAS u64*)(ws + W_SG + row * 8u);
  u64 v = 0;
#pragma unroll 1
  for (u32 it = 0; it < SPIN_CAP; ++it) {
    v = __hip_atomic_load(p, RLX_AGENT);
    if (__all((u32)(v >> 32) == tag)) break;
    __builtin_amdgcn_s_sleep(1);
  }
  return (u32)v;
}
DI u32 lds_arrive(u32 a, int lane) {
  u32 r = 0;
  waitl();
  if (lane == 0) r = __hip_atomic_fetch_add((LAS u32*)a, 1u, RLX_WG);
  return (u32)__builtin_amdgcn_readfirstlane(r) + 1u;
}
DI void lds_wait(u32 a, u32 want) {
#pragma unroll 1
  for (u32 it = 0; it < SPIN_CAP * 16; ++it) {
    u32 v = __hip_atomic_load((LAS u32*)a, RLX_WG);
    if ((u32)__builtin_amdgcn_readfirstlane(v) >= want) break;
  }
  asm volatile("" ::: "memory");
}

DI int lane_id() { int l; asm volatile("v_mbcnt_lo_u32_b32 %0, -1, 0\n\tv_mbcnt_hi_u32_b32 %0, -1, %0" : "=v"(l)); return l; }
DI u32 rfl(u32 x) { return (u32)__builtin_amdgcn_readfirstlane(x); }
DI u32x2 lds_ld2(u32 a) { return __builtin_bit_cast(u32x2, __hip_atomic_load((LAS u64*)a, RLX_WG)); }
template <int SLEEP> DI void wait_min2(u32 a, u32 want) {
#pragma unroll 1
  for (u32 it = 0; it < SPIN_CAP * 16; ++it) {
    const u32x2 v = lds_ld2(a);
    if (rfl(min(v.x, v.y)) >= want) break;
    if (SLEEP) __builtin_amdgcn_s_sleep(SLEEP);
  }
  asm volatile("" ::: "memory");
}
DI u32 lds_ld1(u32 a) { return __hip_atomic_load((LAS u32*)a, RLX_WG); }
DI bool all_ge(u32 w, u32 want) { return ((w - want * 0x01010101u) & 0x80808080u) == 0u; }
template <int SLEEP> DI void wait_min4(u32 a, u32 want) {
#pragma unroll 1
  for (u32 it = 0; it < SPIN_CAP * 16; ++it) {
    if (all_ge(rfl(lds_ld1(a)), want)) break;
    if (SLEEP) __builtin_amdgcn_s_sleep(SLEEP);
  }
  asm volatile("" ::: "memory");
}
template <int SLEEP> DI void wait_byte(u32 a, u32 want) {
#pragma unroll 1
  for (u32 it = 0; it < SPIN_CAP * 16; ++it) {
    if (rfl(__hip_atomic_load((LAS unsigned char*)a, RLX_WG)) >= want) break;
    if (SLEEP) __builtin_amdgcn_s_sleep(SLEEP);
  }
  asm volatile("" ::: "memory");
}
DI u64 wait_epoch() {
  u32x2 e;
#pragma unroll 1
  for (u32 it = 0; it < SPIN_CAP * 16; ++it) {
    e = lds_ld2(EPO);
    if (rfl(e.x) != 0u) break;
    __builtin_amdgcn_s_sleep(2);
  }
  return ((u64)rfl(e.y) << 32) | rfl(e.x);
}
DI u64 gld_agent(const char* p) { return __hip_atomic_load((const GAS u64*)p, RLX_AGENT); }
DI void lds_set(u32 a, u32 v) { __hip_atomic_store((LAS unsigned char*)a, (unsigned char)v, RLX_WG); }
DI void gather(u32& junk, u32 voff, rsrc_t r, u32 soff) { asm volatile("buffer_load_dword %0, %1, %2, %3 offen" : "=v"(junk) : "v"(voff), "s"(r), "s"(soff)); }
template <int N> using IC = std::integral_constant<int, N>;

#define DONE() do { if (wave >= 8) { const u64 d_ = rfl(ldsr<u32>(CNT + 60)); while (__builtin_amdgcn_s_memrealtime() - tstart_ < 40 * d_) __builtin_amdgcn_s_sleep(1); } } while (0)
#define STAMP(id) do { if ((id) == AMPID && wave == AMPWAVE) ldsw<u32>(CNT + 60, (u32)(__builtin_amdgcn_s_memrealtime() - tstart_)); } while (0)

struct Args { const u16 *q, *wg, *wu, *k, *v, *sk, *h, *dv, *wd; u16* out; char* ws; u64 pad; };
typedef u32 u32x16 __attribute__((ext_vector_type(16)));
typedef u32 u32x8 __attribute__((ext_vector_type(8)));
DI char* mkp(u32 lo, u32 hi) { return (char*)(((u64)hi << 32) | lo); }
#define GAP() __builtin_amdgcn_sched_barrier(0)
DI void barx() { asm volatile("s_barrier" ::: "memory"); }
template <class T> DI void spin(T& x) { asm volatile("" : "+s"(x)); }
template <class T> DI void pin(T& x) { asm volatile("" : "+v"(x)); }

__global__ void __launch_bounds__(768, 3) fused(Args) {
  u64 pc0; asm volatile("s_getpc_b64 %0" : "=s"(pc0));
  const u64 tstart_ = __builtin_amdgcn_s_memrealtime();
  asm volatile("s_waitcnt lgkmcnt(0)");
  u32x16 ka; u32x8 kb;
  asm volatile("s_load_dwordx16 %0, %2, 0x0\n\ts_load_dwordx8 %1, %2, 0x40" : "=&s"(ka), "=&s"(kb) : "s"(__builtin_amdgcn_kernarg_segment_ptr()));
  GAP();
  const int tid = threadIdx.x, lane = tid & 63;
  const int wave = __builtin_amdgcn_readfirstlane(tid >> 6);
  const int b = blockIdx.x, xcd = b & 7, li = b >> 3;
  if (tid < 16) ldsw<u32>(CNT + tid * 4, 0u);
  const bool has = li < 30;
  const int id = 30 * xcd + (has ? li : 0), rr_ = id % 120, rb = 8 * (id / 120) + (rr_ & 7);
  const int cg = has ? rr_ >> 3 : 4 * (xcd & 3) + 2 * (xcd >> 2) + (li - 30);
  const bool csw = !has && cg < 15;
  const int rt = 4 * xcd + (li & 3), ct = li >> 2;
  const int j = wave & 3, l8 = lane >> 3, c8 = lane & 7;
  u32 xv[4], wv[3];
  sfor<4>([&](auto t) AI { xv[t] = (7 * t + l8) * 1024 + ((c8 ^ ((4 * t + (l8 >> 1)) & 7)) << 4); pin(xv[t]); });
  sfor<3>([&](auto t) AI {
    const u32 byte = t * 1024 + lane * 16, row = byte / 192u;
    const u32 seg = byte - row * 192u;
    wv[t] = cg * 192 + seg >= F * 2 ? 0x40000000u : (16 * j + row) * (F * 2) + cg * 192 + seg - t * 1024;
    pin(wv[t]);
  });
  asm volatile("s_waitcnt lgkmcnt(0)\n\ts_barrier" : "+s"(ka), "+s"(kb)::"memory");
  STAMP(9);
  char* const ws = mkp(kb[4], kb[5]);
  const rsrc_t rq = mk_rsrc(mkp(ka[0], ka[1]), S * D * 2);
  if (wave >= 8) {
    __builtin_amdgcn_s_setprio(3);
    u64 epoch;
    asm volatile("s_load_dwordx2 %0, %1, 0x0" : "=s"(epoch) : "s"(ws));
    const int lane = lane_id(), l8 = lane >> 3, c8 = lane & 7;
    const int ch = j & 1, cp = j >> 1, c16 = lane & 15, q = lane >> 4;
    const rsrc_t rws = mk_rsrc(ws, W_END);
    const u32 cb = CONV + ch * CH_SZ, hown = cb + (cp ? XB : HA), kown = cb + (cp ? KB : KA);
    const int srow = 256 * xcd + 8 * li + 2 * j;
    if (SSTOP == 0) wait_min4<8>(FREE, 8);
    u32x4 sx[2];
    sfor<2>([&](auto r) AI { bld4(sx[r], lane * 16, rq, (srow + r) * 1024); });
    STAMP(23);
    const int pa = b & 31, pg = b >> 5, s0 = 64 * pa + 16 * j;
    u32x2 skw[2];
    u32 dvv;
    auto kvload = [&]() AI {
      const rsrc_t rk = mk_rsrc(mkp(ka[6], ka[7]), S * D * 2), rv = mk_rsrc(mkp(ka[8], ka[9]), S * D * 2), rs_ = mk_rsrc(mkp(ka[10], ka[11]), D * 8);
      const u32 kvv = l8 * 1024 + c8 * 16 + pg * 128;
      sfor<3>([&](auto t) AI { const int r = s0 - 8 + 8 * t; dma(hown + t * 1024, kvv, rk, (r < 0 ? 0 : r) * 1024); });
      sfor<2>([&](auto t) AI { dma(kown + t * 1024, kvv, rv, (s0 + 8 * t) * 1024); });
      sfor<2>([&](auto t) AI { bld2<256 * t>(skw[t], (64 * pg + 16 * (q >> 1) + c16) * 8, rs_); });
      gld_u16(dvv, (2 * b + ch) * 2, mkp(ka[14], ka[15]));
    };
    asm volatile("s_waitcnt lgkmcnt(0)" : "+s"(epoch));
    epoch += 0x9E3779B97F4A7C15ull;
    epoch = (epoch ^ (epoch >> 30)) * 0xBF58476D1CE4E5B9ull; epoch = (epoch ^ (epoch >> 27)) * 0x94D049BB133111EBull;
    epoch = (epoch ^ (epoch >> 31)) | 1ull;
    ldsw<u64>(EPO, epoch);
    STAMP(18);
    const u32 tag = (u32)(epoch >> 17);
    if (has) wait_min4<2>(FULL, KVAFTER + KVSTAG * (li & 3));
    STAMP(19);
    kvload();
    STAMP(20);
    asm volatile("s_waitcnt vmcnt(8)" : "+v"(sx[0]), "+v"(sx[1]));
    STAMP(21);
    __builtin_amdgcn_s_setprio(0);
    {
      float x[2][8], mean[2], var[2];
      sfor<2>([&](auto r) AI {
        sfor<4>([&](auto i) AI { x[r][2 * i] = bflo(sx[r][(int)i]); x[r][2 * i + 1] = bfhi(sx[r][(int)i]); });
        mean[r] = x[r][0];
        sfor<7>([&](auto i) AI { mean[r] += x[r][i + 1]; });
      });
      wave_sum2(mean[0], mean[1]);
      sfor<2>([&](auto r) AI {
        mean[r] *= 1.0f / D; var[r] = 0.f;
        sfor<8>([&](auto i) AI { float d = x[r][i] - mean[r]; asm("" : "+v"(d)); var[r] += d * d; });
      });
      wave_sum2(var[0], var[1]);
      STAMP(22);
      const float mn = lane ? mean[1] : mean[0], vr = rbf((lane ? var[1] : var[0]) * (1.0f / D));
      const u32 g = pk2s(mn, __builtin_sqrtf(rbf(vr + 1e-5f)));
      if (lane < 2) __hip_atomic_store((GAS u64*)(ws + W_SG + (srow + lane) * 8), ((u64)tag << 32) | g, RLX_AGENT);
    }
    STAMP(1);
    asm volatile("s_waitcnt vmcnt(1)" : "+v"(skw[0]), "+v"(skw[1]), "+v"(dvv)::"memory");
    STAMP(2);
    {
      const bool first = s0 == 0 && (q & 1) == 0;
      u32x2 kr[2][5];
      sfor<2>([&](auto gi) AI {
        const u32 a = (8 * (q & 1) + (c16 >> 2)) * 128 + 64 * gi + 32 * (q >> 1) + 8 * (lane & 3);
        kr[gi][0] = ldtr(hown + a + 512); kr[gi][1] = ldtr(hown + a + 1024); kr[gi][2] = ldtr(hown + a + 1536); kr[gi][3] = ldtr(kown + a); kr[gi][4] = ldtr(kown + a + 512);
      });
      GAP();
      if (SSTOP == 1) wait_min4<8>(FREE, 8);
      sfor<2>([&](auto gi) AI {
        const float w0 = bflo(skw[gi].x), w1 = bfhi(skw[gi].x), w2 = bflo(skw[gi].y), w3 = bfhi(skw[gi].y);
        const u32x2 k0 = kr[gi][0], k1 = kr[gi][1], k2 = kr[gi][2], v0 = kr[gi][3], v1 = kr[gi][4];
        const float kk[11] = {first ? 0.f : bfhi(k0.x), first ? 0.f : bflo(k0.y), first ? 0.f : bfhi(k0.y), bflo(k1.x), bfhi(k1.x), bflo(k1.y), bfhi(k1.y), bflo(k2.x), bfhi(k2.x), bflo(k2.y), bfhi(k2.y)};
        const float vv[8] = {bflo(v0.x), bfhi(v0.x), bflo(v0.y), bfhi(v0.y), bflo(v1.x), bfhi(v1.x), bflo(v1.y), bfhi(v1.y)};
        u32 a4[4], m4[4];
        sfor<4>([&](auto t) AI { a4[t] = pk2s(w3 * kk[2 * t], w3 * kk[2 * t + 1]); m4[t] = pk2s(w2 * kk[2 * t + 1], w2 * kk[2 * t + 2]); });
        GAP();
        sfor<4>([&](auto t) AI { a4[t] = pk2s(bflo(a4[t]) + bflo(m4[t]), bfhi(a4[t]) + bfhi(m4[t])); m4[t] = pk2s(w1 * kk[2 * t + 2], w1 * kk[2 * t + 3]); });
        GAP();
        sfor<4>([&](auto t) AI { a4[t] = pk2s(bflo(a4[t]) + bflo(m4[t]), bfhi(a4[t]) + bfhi(m4[t])); m4[t] = pk2s(w0 * kk[2 * t + 3], w0 * kk[2 * t + 4]); });
        GAP();
        sfor<4>([&](auto t) AI { a4[t] = pk2s(bflo(a4[t]) + bflo(m4[t]), bfhi(a4[t]) + bfhi(m4[t])); });
        GAP();
        u32x4 ov;
        sfor<4>([&](auto t) AI { ov[3 - t] = pk2s(bfhi(a4[t]) * vv[2 * t + 1], bflo(a4[t]) * vv[2 * t]); });
        const int dg = 2 * gi + (q >> 1);
        ldsw<u32x4>(CONV + (dg & 1) * CH_SZ + (dg >> 1 ? KB : KA) + 2048 + c16 * 128 + (((7 - 2 * j - (q & 1)) ^ (c16 & 7)) << 4), ov);
      });
    }
    waitl();
    lds_set(CVP + j, 1);
    __builtin_amdgcn_s_setprio(3);
    STAMP(17);
    wait_min4<0>(CVP, 1);
    {
      u32x4 o[2];
      sfor<2>([&](auto t) AI { o[t] = ldsr<u32x4>(kown + 2048 + (8 * t + l8) * 128 + ((c8 ^ l8) << 4)); });
      sfor<2>([&](auto t) AI { __builtin_amdgcn_raw_buffer_store_b128(o[t], rws, W_KV + (64 * pg + 16 * j + 8 * t + l8) * 4096 + (1984 - 64 * pa) * 2 + c8 * 16, 0, 16); });
    }
    const int mh = j & 1, db = j >> 1, s31 = lane & 31, hh = lane >> 5;
    const int orow = 64 * rt + 32 * mh + s31, ocol = 64 * ct + 32 * db;
    u32x2 xr[4];
    u32 sg;
    {
      const char* p1 = ws + W_SG + (hh ? orow : rb * 128 + 32 * j + s31) * 8;
      const char* p2 = ws + W_CG + ((has ? cg : 0) * 192 + 48 * j + (lane < 48 ? lane : 0)) * 8;
      u64 g1, g2;
      gld8_sc1(g1, p1); gld8_sc1(g2, p2);
      asm volatile("s_waitcnt vmcnt(0)" : "+v"(g1), "+v"(g2)::"memory");
      if (lds_arrive(CKV, lane) == 4 && lane == 0) __hip_atomic_store((GAS u64*)(ws + W_PF + b * 64), epoch, RLX_AGENT);
      STAMP(3);
#pragma unroll 1
      for (u32 it = 0; it < SPIN_CAP; ++it) {
        if (__all((u32)(g1 >> 32) == tag && (u32)(g2 >> 32) == tag)) break;
        __builtin_amdgcn_s_sleep(4);
        g1 = gld_agent(p1); g2 = gld_agent(p2);
      }
      sg = (u32)g1;
      if (has) {
        if (lane < 32) ldsw<f32x2>(ST + (32 * j + lane) * 8, f32x2{bflo(sg), __builtin_amdgcn_rcpf(bfhi(sg))});
        if (lane < 48) ldsw<u32>(CS + (48 * j + lane) * 4, (u32)g2);
      }
      lds_set(STRDY + j, 1);
      STAMP(4);
    }
    if (has) wait_min4<4>(FULL, S2AFTER);
    STAMP(24);
    {
      u64 f;
      gld8_sc1(f, ws + W_PF + ((b >> 5) * 32 + (j == 0 ? s31 : 0)) * 64);
      dma4(cb + (cp ? HB : HA), lane * 16, lane * 16, lane * 16, lane * 16, mk_rsrc(mkp(ka[12], ka[13]), D * S * 2), (2 * b + ch) * 4096);
      if (lane < (cp ? 33 : 16)) ldsw<u32>(cb + (cp ? 4160 : 0) + lane * 4, 0u);
      if (lane < (cp ? 28 : 30)) ldsw<u32x4>(kown + 4096 + lane * 16, u32x4{0u, 0u, 0u, 0u});
      asm volatile("s_waitcnt vmcnt(4)" : "+v"(f));
      sg = (u32)__builtin_amdgcn_permlane32_swap(sg, sg, false, false)[1];
      if (j == 0) { if (!__all(f == epoch)) poll_flags<2>(ws, W_PF + (b >> 5) * 32 * 64, 32, epoch, lane); lds_set(CVP + 4, 1); }
      wait_byte<1>(CVP + 4, 1);
      u32x4 kvr[4];
      const char* src = ws + W_KV + (2 * b + ch) * 4096 + 2 * cp;
      sfor<4>([&](auto t) AI { gld4_sc1<t * 1024>(kvr[t], lane * 16, src); });
      wait_min4<1>(FREE, 6);
      sfor<2>([&](auto t) AI {
        const int r = 16 * t + (lane >> 2);
        dma(ZST + j * 2048 + t * 1024, r * 1024 + (((lane & 3) ^ ((r >> 1) & 3)) << 4), rq, (64 * rt + 32 * mh) * 1024 + ocol * 2);
      });
      asm volatile("s_waitcnt vmcnt(0)" : "+v"(kvr[0]), "+v"(kvr[1]), "+v"(kvr[2]), "+v"(kvr[3])::"memory");
      if (cp && lane == 63) kvr[3].w &= 0xffffu;
      sfor<4>([&](auto t) AI { ldsw<u32x4>(kown + t * 1024 + lane * 16, kvr[t]); });
      sfor<4>([&](auto g) AI { xr[g] = ldsr<u32x2>(ZST + j * 2048 + s31 * 64 + ((g ^ ((s31 >> 1) & 3)) << 4) + 8 * hh); });
      waitl();
      pin(xr[0]); pin(xr[1]); pin(xr[2]); pin(xr[3]);
      lds_set(CVP + j, 2);
      STAMP(5);
    }
    __builtin_amdgcn_s_setprio(0);
    wait_min4<1>(FREE, CONVAFTER);
    wait_byte<0>(CVP + (j ^ 2), 2);
    __builtin_amdgcn_s_setprio(PRC);
    STAMP(6);
    f32x4 dacc[8];
    sfor<8>([&](auto i) AI { dacc[i] = f32x4{0.f, 0.f, 0.f, 0.f}; });
    u32 kv2[8];
    {
      u32 hb = cb + ((c16 & 1) ? HA : HB) + 4 * (c16 - 15) + 16 * q + 256 * cp;
      u32 kb_ = cb + ((c16 & 1) ? KA : KB) + 448 - 64 * (c16 >> 1) + 16 * q + 256 * cp;
      u32 hb2 = hb + 8;
      pin(hb); pin(hb2); pin(kb_);
      bf16x8 ta[12], kf[12];
      auto ld1 = [&](auto odd, auto n, auto w, auto off) AI {
        constexpr int r = (n < 4 && odd) ? 8 + n : n;
        if constexpr (w == 0) kf[r] = ldsr<bf16x8>(kb_ + 512 * (7 - n) + off);
        if constexpr (w == 1) { const u32x2 v = ldsr<u32x2>(hb + 512 * n + off); u32x4 t = __builtin_bit_cast(u32x4, ta[r]); t.x = v.x; t.y = v.y; ta[r] = __builtin_bit_cast(bf16x8, t); }
        if constexpr (w == 2) { const u32x2 v = ldsr<u32x2>(hb2 + 512 * n + off); u32x4 t = __builtin_bit_cast(u32x4, ta[r]); t.z = v.x; t.w = v.y; ta[r] = __builtin_bit_cast(bf16x8, t); }
      };
      auto cstep = [&](auto odd, auto off) AI {
        sfor<36>([&](auto g) AI {
          constexpr int c = cpair(35 - g) >> 3, dl = cpair(35 - g) & 7;
          constexpr int rc = (c < 4 && odd) ? 8 + c : c, rd_ = (dl < 4 && odd) ? 8 + dl : dl;
          dacc[c + dl] = __builtin_amdgcn_mfma_f32_16x16x32_bf16(kf[rc], ta[rd_], dacc[c + dl], 0, 0, 0);
          if constexpr (g < 12) ld1(IC<1 - odd>{}, IC<g / 3>{}, IC<g % 3>{}, off);
          else if constexpr (g < 24) ld1(IC<1 - odd>{}, IC<7 - (g - 12) / 3>{}, IC<g % 3>{}, off);
          GAP();
        });
      };
      sfor<24>([&](auto i) AI { ld1(IC<0>{}, IC<(i < 3 ? 0 : i < 6 ? 7 : i < 9 ? 1 : i < 12 ? 6 : i < 15 ? 2 : i < 18 ? 5 : i < 21 ? 3 : 4)>{}, IC<i % 3>{}, IC<0>{}); GAP(); });
#pragma unroll 1
      for (int i = 0; i < 2; ++i) {
        cstep(IC<0>{}, IC<64>{});
        cstep(IC<1>{}, IC<128>{});
        hb += 128; hb2 += 128; kb_ += 128;
      }
    }
    __builtin_amdgcn_s_setprio(3);
    STAMP(25);
    sfor<4>([&](auto i) AI { ldsw<f32x4>(XCH + j * 4096 + i * 1024 + lane * 16, cp ? dacc[i] : dacc[4 + i]); });
    lds_set(CVP + j, 4);
    {
      const int l = lane_id();
      u32 a = cb + KA + 2 * (2046 - 2 * (l & 15) - 64 * (l >> 4) - 1024 * cp) - 1600;
      pin(a);
      sfor<8>([&](auto i) AI { kv2[i] = ldsr<u32>(a + 64 * (1 - (i & 1)) + 512 * (3 - (i >> 1))); });
    }
    STAMP(7);
    {
      const float dv = bflo(dvv);
      sfor<8>([&](auto i) AI { kv2[i] = pk2s(bfhi(kv2[i]) * dv, bflo(kv2[i]) * dv); pin(kv2[i]); });
      wait_byte<0>(CVP + (j ^ 2), 4);
      f32x4 o[4];
      sfor<4>([&](auto i) AI { o[i] = ldsr<f32x4>(XCH + (j ^ 2) * 4096 + i * 1024 + lane * 16); });
      asm volatile("" ::: "memory");
      lds_set(CVP + j, 5);
      sfor<4>([&](auto i) AI {
        const f32x4 m = cp ? dacc[4 + i] : dacc[i];
        sfor<2>([&](auto rr) AI {
          const u32 yb = pk2s(m[2 * rr] + o[i][2 * rr], m[2 * rr + 1] + o[i][2 * rr + 1]), kd = kv2[2 * i + rr];
          ldsw<u32>(ZST + j * 2048 + 4 * c16 + 128 * q + 64 * rr + 512 * i, pk2s(bflo(yb) + bflo(kd), bfhi(yb) + bfhi(kd)));
        });
      });
      waitl();
      u32x2 zo[4];
      sfor<4>([&](auto t) AI { zo[t] = ldsr<u32x2>(ZST + j * 2048 + t * 512 + lane * 8); });
      sfor<4>([&](auto t) AI {
        __builtin_amdgcn_raw_buffer_store_b128(u32x4{zo[t].x, tag, zo[t].y, tag}, rws, lane * 16 + W_ZT + (2 * b + ch) * 8192 + cp * 4096 + t * 1024, 0, 16);
      });
    }
    STAMP(8);
    const float mean = bflo(sg), rs = __builtin_amdgcn_rcpf(bfhi(sg));
    u32 xn[8];
    sfor<8>([&](auto i) AI {
      const u32 x2 = xr[i >> 1][i & 1], n2 = pk2s(bflo(x2) - mean, bfhi(x2) - mean);
      xn[i] = pk2s(bflo(n2) * rs, bfhi(n2) * rs);
      pin(xn[i]);
    });
    if (b == 0 && j == 3) {
      sfor<4>([&](auto t) AI { poll_flags<1>(ws, W_PF + t * 4096, 64, epoch, lane); });
      if (lane == 0) __hip_atomic_store((GAS u64*)(ws + W_E), epoch, RLX_AGENT);
    }
    wait_min4<2>(FREE, ZAFTER);
    u32x4 zr[4];
#pragma unroll 1
    for (u32 it = 0; it < SPIN_CAP; ++it) {
      sfor<4>([&](auto t) AI { bld4_sc1<0>(zr[t], l8 * 8192 + c8 * 16, rws, W_ZT + (ocol + 8 * t) * 8192 + (64 * rt + 32 * mh) * 4); });
      asm volatile("s_waitcnt vmcnt(0)" : "+v"(zr[0]), "+v"(zr[1]), "+v"(zr[2]), "+v"(zr[3])::"memory");
      u32 bad = 0;
      sfor<4>([&](auto t) AI { bad |= (zr[t].y ^ tag) | (zr[t].w ^ tag); });
      if (__all(bad == 0u)) break;
      __builtin_amdgcn_s_sleep(ZSLEEP);
    }
    STAMP(10);
    sfor<4>([&](auto t) AI { ldsw<u32x2>(ZST + j * 2048 + t * 512 + lane * 8, u32x2{zr[t].x, zr[t].z}); });
    waitl();
    float tt[16];
    sfor<4>([&](auto g) AI {
      const u32x2 z4 = ldtr(ZST + j * 2048 + (8 * g + 4 * hh + ((lane & 15) >> 2)) * 64 + 32 * ((lane >> 4) & 1) + 8 * (lane & 3));
      sfor<2>([&](auto e) AI {
        const u32 x2 = xr[g][(int)e], z2 = z4[(int)e];
        u32 n2 = pk2s(bflo(xn[2 * g + e]) * bflo(z2), bfhi(xn[2 * g + e]) * bfhi(z2));
        n2 = pk2s(bflo(x2) + bflo(n2), bfhi(x2) + bfhi(n2));
        tt[4 * g + 2 * e] = bflo(n2); tt[4 * g + 2 * e + 1] = bfhi(n2);
      });
    });
    sfor<16>([&](auto i) AI { pin(tt[i]); });
    STAMP(11);
    wait_min4<0>(FREE, 12);
    STAMP(12);
    u32x2 ob[4];
    sfor<4>([&](auto g) AI {
      const f32x4 v = ldsr<f32x4>(PART + mh * 8192 + db * 4096 + g * 1024 + lane * 16) + ldsr<f32x4>(PART + (2 + mh) * 8192 + db * 4096 + g * 1024 + lane * 16);
      ob[g].x = pk2(tt[4 * g] + rs * v.x, tt[4 * g + 1] + rs * v.y);
      ob[g].y = pk2(tt[4 * g + 2] + rs * v.z, tt[4 * g + 3] + rs * v.w);
    });
    sfor<2>([&](auto gp) AI {
      auto s0 = __builtin_amdgcn_permlane32_swap(ob[2 * gp].x, ob[2 * gp + 1].x, false, false);
      auto s1 = __builtin_amdgcn_permlane32_swap(ob[2 * gp].y, ob[2 * gp + 1].y, false, false);
      const u32x4 v = {(u32)s0[0], (u32)s1[0], (u32)s0[1], (u32)s1[1]};
      *(GAS u32x4*)((GAS char*)mkp(kb[2], kb[3]) + orow * 1024 + ocol * 2 + 32 * gp + 16 * hh) = v;
    });
    STAMP(13);
    DONE();
  } else if (wave < 4) {
    u32 junk, junk2;
    asm volatile("s_load_dword %0, %2, %3\n\ts_load_dword %1, %2, %4" : "=&s"(junk), "=&s"(junk2)
                 : "s"(pc0 & ~127ull), "s"((u32)(li + 32 * j) * 128u), "s"(min((u32)(li + 32 * (4 + j)) * 128u, CODE_BYTES)));
    const rsrc_t rws = mk_rsrc(ws, W_END);
    const int wm = wave >> 1, wn = wave & 1;
    const int l0 = lane_id();
    u32 wa = ((l0 >> 4) & 1) * (12288 + UPAD) + (8 * (l0 >> 5) + ((l0 & 15) >> 2)) * 192 + 96 * wn + 8 * (l0 & 3);
    u64 epoch;
    f32x16 acc[2][3];
    sfor<2>([&](auto m) AI { sfor<3>([&](auto n) AI { sfor<16>([&](auto r) AI { acc[m][n][(int)r] = 0.f; }); pin(acc[m][n]); }); });
    u32 xa[4];
    sfor<4>([&](auto ks) AI { xa[ks] = (64 * wm + (l0 & 31)) * 128 + (((2 * ks + (l0 >> 5)) ^ ((l0 >> 1) & 7)) << 4); pin(xa[ks]); });
    asm volatile("s_waitcnt lgkmcnt(0)" : "+s"(junk), "+s"(junk2));
    STAMP(10);
    if (has) {
      bf16x8 xf[2][2], wf[2][3];
      auto ldw = [&](auto st, auto n) AI { const u32 a = wa + G_OFF + (st & 3) * 3072 + n * 32; wf[st & 1][n] = cat(ldtr(a), ldtr(a + 768)); };
      auto ldx = [&](auto st, auto m) AI { xf[st & 1][m] = ldsr<bf16x8>(xa[st & 3] + m * 4096); };
      wait_min4<1>(FULL, 1);
      u32 accw = 0, cntw = 0;
      STAMP(1);
      ldw(IC<0>{}, IC<0>{}); ldx(IC<0>{}, IC<0>{}); ldx(IC<0>{}, IC<1>{}); ldw(IC<0>{}, IC<1>{}); ldw(IC<0>{}, IC<2>{});
      ldw(IC<1>{}, IC<0>{}); ldw(IC<1>{}, IC<1>{}); ldx(IC<1>{}, IC<0>{});
      GAP();
      u32 needw = 0x01010101u, off = 0;
#pragma unroll 1
      for (int c = 0; c < 8; ++c) {
        u32 fl, adv, have;
        sfor<24>([&](auto gg) AI {
          constexpr int st = gg / 6, g = gg % 6, n = g >> 1, m = g & 1;
          acc[m][n] = __builtin_amdgcn_mfma_f32_32x32x16_bf16(wf[st & 1][n], xf[st & 1][m], acc[m][n], 0, 0, 0);
          if constexpr (g == 0) { ldw(IC<st + 1>{}, IC<2>{}); ldx(IC<st + 1>{}, IC<1>{}); }
          if constexpr (g == 2) ldw(IC<st + 2>{}, IC<0>{});
          if constexpr (g == 4) ldw(IC<st + 2>{}, IC<1>{});
          if constexpr (g == 5) ldx(IC<st + 2>{}, IC<0>{});
          if constexpr (st == 0 && g == 3) { const u32 o2 = off == 2 * SLOT ? 0u : off + SLOT; adv = o2 - off; off = o2; needw += 0x01010101u; spin(adv); spin(off); spin(needw); }
          if constexpr (st == 1 && g == 1) fl = lds_ld1(FULL);
          if constexpr (st == 1 && g == 3) { xa[0] += adv; xa[1] += adv; xa[2] += adv; pin(xa[0]); pin(xa[1]); pin(xa[2]); }
          if constexpr (st == 2 && g == 0) { have = (rfl(fl) - needw) & 0x80808080u; spin(have); }
          if constexpr (st == 2 && g == 1) {
            xa[3] += adv; wa += adv; pin(xa[3]); pin(wa);
            if (__builtin_expect(have != 0u, 0)) wait_min4<0>(FULL, c + 2);
          }
          if constexpr (st == 3 && g == 1) lds_set(FREE + wave, c + 1);
          GAP();
        });
      }
      STAMP(2);
      __builtin_amdgcn_s_setprio(PRE);
      const int lane = lane_id(), s31 = lane & 31, hh = lane >> 5;
      f32x2 st[2];
      f32x4 ca[4];
      auto ldc = [&](auto n) AI { sfor<4>([&](auto g) AI { const u32 a = CS + (wn * 96 + n * 32 + 8 * g + 4 * hh) * 4; ca[g] = ldsr<f32x4>(a); }); };
#pragma unroll 1
      for (u32 it = 0; it < SPIN_CAP * 16; ++it) {
        const u32 r1 = lds_ld1(STRDY);
        asm volatile("" ::: "memory");
        const u32x2 e = lds_ld2(EPO);
        sfor<2>([&](auto m) AI { st[m] = ldsr<f32x2>(ST + (64 * wm + 32 * m + s31) * 8); });
        ldc(IC<0>{});
        asm volatile("" ::: "memory");
        epoch = ((u64)rfl(e.y) << 32) | rfl(e.x);
        if (__builtin_expect(all_ge(rfl(r1), 1), 1)) break;
      }
      STAMP(11);
      sfor<3>([&](auto n) AI {
        const int ks = 6 * cg + 3 * wn + n;
        if (ks < 86) {
          f32x4 c4[4];
          sfor<4>([&](auto g) AI { c4[g] = ca[g]; });
          f32x2 g[8], u[8], e[8];
          sfor<8>([&](auto i) AI {
            constexpr int m = i >> 2, r = 2 * (i & 3);
            const f32x2 cg2 = {c4[r >> 2][r & 3], c4[r >> 2][(r & 3) + 1]};
            g[i] = f32x2{acc[m][n][r], acc[m][n][r + 1]} - st[m].x * cg2;
          });
          GAP();
          sfor<8>([&](auto i) AI { g[i] = g[i] * st[i >> 2].y; });
          GAP();
          sfor<8>([&](auto i) AI { e[i] = g[i] * g[i]; });
          GAP();
          sfor<8>([&](auto i) AI { e[i] = e[i] * -0.1029432397f - 2.3022081985f; });
          GAP();
          sfor<8>([&](auto i) AI { e[i] = e[i] * g[i]; });
          GAP();
          sfor<8>([&](auto i) AI {
            constexpr int m = i >> 2, r = 2 * (i & 3);
            const f32x2 cu2 = {c4[2 + (r >> 2)][r & 3], c4[2 + (r >> 2)][(r & 3) + 1]};
            e[i] = f32x2{__builtin_amdgcn_exp2f(e[i].x), __builtin_amdgcn_exp2f(e[i].y)};
            u[i] = f32x2{acc[m][n][r + 8], acc[m][n][r + 9]} - st[m].x * cu2;
            GAP();
          });
          if constexpr (n < 2) ldc(IC<n + 1>{});
          sfor<8>([&](auto i) AI { e[i] = e[i] + 1.0f; });
          GAP();
          sfor<8>([&](auto i) AI {
            e[i] = f32x2{__builtin_amdgcn_rcpf(e[i].x), __builtin_amdgcn_rcpf(e[i].y)};
            g[i] = g[i] * u[i];
            GAP();
          });
          sfor<8>([&](auto i) AI { g[i] = g[i] * e[i]; });
          GAP();
          sfor<2>([&](auto m) AI {
            const u32x4 o = {pk2(g[4 * m].x, g[4 * m].y), pk2(g[4 * m + 1].x, g[4 * m + 1].y), pk2(g[4 * m + 2].x, g[4 * m + 2].y), pk2(g[4 * m + 3].x, g[4 * m + 3].y)};
            __builtin_amdgcn_raw_buffer_store_b128(o, rws, lane * 16 + W_H + ((4 * rb + 2 * wm + m) * 86 + ks) * 1024, 0, 16);
          });
          GAP();
          if constexpr (n < 2) STAMP(20 + n);
        }
      });
      lds_set(FREE + wave, 9);
      STAMP(3);
      waitv<0>();
      __builtin_amdgcn_s_setprio(PRW);
      if (lds_arrive(CNT + 0, lane) == 4 && lane == 0) __hip_atomic_store((GAS u64*)(ws + W_HF + (rb * 16 + cg) * 64), epoch, RLX_AGENT);
      STAMP(4);
    } else {
      if (csw) {
        f32x4 cs[3];
        sfor<3>([&](auto n) AI { cs[n] = f32x4{0.f, 0.f, 0.f, 0.f}; });
        const u32 o2_ = (l0 & 15) == ((l0 >> 4) & 1) ? 0x3f803f80u : 0u;
        const bf16x8 sel = __builtin_bit_cast(bf16x8, u32x4{o2_, o2_, o2_, o2_});
        wa += wm * 6144;
#pragma unroll 1
        for (int c = 0; c < 8; ++c) {
          wait_min4<1>(FULL, c + 1);
          bf16x8 w[6];
          sfor<6>([&](auto i) AI { const u32 a = wa + (i / 3) * 3072 + (i % 3) * 32; w[i] = cat(ldtr(a), ldtr(a + 768)); });
          asm volatile("" ::: "memory");
          lds_set(FREE + wave, c < 3 ? c + 1 : 3);
          sfor<6>([&](auto i) AI { cs[i % 3] = __builtin_amdgcn_mfma_f32_16x16x32_bf16(w[i], sel, cs[i % 3], 0, 0, 0); });
          wa += c == 4 ? (u32)(-4 * (int)CSLOT) : CSLOT;
        }
        if ((l0 & 15) < 2) sfor<3>([&](auto n) AI { ldsw<f32x4>(CS + (wm * 192 + wn * 96 + n * 32 + (l0 & 15) * 16 + 4 * (l0 >> 4)) * 4, cs[n]); });
      }
      lds_set(FREE + wave, 9);
      epoch = wait_epoch();
      if (csw && wave < 3) {
        wait_min4<1>(FREE, 9);
        const u32 i = 64 * wave + l0;
        const float v = ldsr<float>(CS + i * 4) + ldsr<float>(CS + 768 + i * 4);
        __hip_atomic_store((GAS u64*)(ws + W_CG + (cg * 192 + i) * 8), ((u64)(u32)(epoch >> 17) << 32) | __float_as_uint(v), RLX_AGENT);
      }
      STAMP(4);
    }
    const int lane = lane_id(), hh = lane >> 5, g16 = lane >> 4, q4 = (lane & 15) >> 2, p4 = lane & 3;
    const int kh = wave >> 1, mh = wave & 1;
    u32x4 hf[43];
    const u32 hso = W_H + ((2 * rt + mh) * 86 + 43 * kh) * 1024, hvo = lane * 16;
    f32x16 o2[2];
    sfor<2>([&](auto n) AI { sfor<16>([&](auto r) AI { o2[n][(int)r] = 0.f; }); pin(o2[n]); });
    u32 db_[2][3];
    sfor<2>([&](auto n) AI {
      const u32 da = kh * 8192 + (4 * hh + q4) * 128 + ((4 * ((int)n ^ (q4 >> 1)) + 2 * (g16 & 1) + (p4 >> 1)) << 4) + 8 * (p4 & 1);
      sfor<3>([&](auto w) AI { db_[n][w] = da + w * 61440; pin(db_[n][w]); });
    });
    bf16x8 df[3][2];
    u32 fl;
    auto ldd = [&](auto k) AI {
      constexpr int c = k >> 2, ks = k & 3;
      constexpr u32 wb = (c < 7 ? 16384u * c : c == 7 ? CONV : c == 8 ? CONV + CH_SZ : 16384u * (c - 9)) + ks * 2048;
      constexpr int w = wb / 61440 > 2 ? 2 : wb / 61440;
      sfor<2>([&](auto n) AI { const u32 a = db_[n][w] + (wb - w * 61440); df[k % 3][n] = cat(ldtr(a), ldtr(a + 1024)); });
    };
    if (wave == 0) { poll_flags<1>(ws, W_HF + (rt >> 1) * 16 * 64, 15, epoch, lane); lds_set(CVP + 6, 1); }
    else wait_byte<WSL>(CVP + 6, 1);
    __builtin_amdgcn_s_setprio(2);
    STAMP(5);
    auto hload = [&](auto k) AI { bld4_sc1<(k & 3) * 1024>(hf[k], hvo, rws, hso + (k >> 2) * 4096); };
    sfor<PD>([&](auto k) AI { hload(k); });
    wait_min4<0>(WDRDY, 1);
    STAMP(6);
    ldd(IC<0>{}); ldd(IC<1>{});
    GAP();
    sfor<43>([&](auto k) AI {
      if constexpr (k == 10) lds_set(FREE + wave, 10);
      if constexpr (k == 18) lds_set(FREE + wave, 11);
      if constexpr (k == 20 || k == 32) fl = lds_ld1(WDRDY);
      if constexpr (k == 40) fl = lds_ld1(FREE);
      if constexpr (k == 22 || k == 34) { STAMP(k == 22 ? 12 : 14); if (__builtin_expect(!all_ge(rfl(fl), k == 22 ? 2u : 3u), 0)) wait_min4<0>(WDRDY, k == 22 ? 2 : 3); STAMP(k == 22 ? 13 : 15); GAP(); }
      constexpr int younger = (k + PD <= 43 ? PD : 43 - k) - 1;
      waitv_pin<younger>(hf[k]);
      if constexpr (k == 0) STAMP(24);
      if constexpr (k == 8) STAMP(25);
      if constexpr (k == 16) STAMP(26);
      o2[0] = __builtin_amdgcn_mfma_f32_32x32x16_bf16(df[k % 3][0], __builtin_bit_cast(bf16x8, hf[k]), o2[0], 0, 0, 0);
      if constexpr (k + 2 < 43) ldd(IC<k + 2>{});
      GAP();
      o2[1] = __builtin_amdgcn_mfma_f32_32x32x16_bf16(df[k % 3][1], __builtin_bit_cast(bf16x8, hf[k]), o2[1], 0, 0, 0);
      if constexpr (k + PD < 43) hload(IC<k + PD>{});
      GAP();
    });
    STAMP(7);
    if (!all_ge(rfl(fl), 11u)) wait_min4<0>(FREE, 11);
    sfor<2>([&](auto n) AI { sfor<4>([&](auto g) AI {
      f32x4 v = {o2[n][4 * g], o2[n][4 * g + 1], o2[n][4 * g + 2], o2[n][4 * g + 3]};
      ldsw<f32x4>(PART + wave * 8192 + n * 4096 + g * 1024 + lane * 16, v);
    }); });
    waitl();
    lds_set(FREE + wave, 12);
    STAMP(8);
    DONE();
  } else {
    __builtin_amdgcn_s_setprio(PRL);
    const rsrc_t rg = mk_rsrc(mkp(ka[2], ka[3]), D * F * 2), ru = mk_rsrc(mkp(ka[4], ka[5]), D * F * 2);
    const int crot = ROT * ((rb + cg) & 1);
    auto chx = [&](int c, u32 sb) AI { dma4(sb + 4 * j * 1024, xv[0], xv[1], xv[2], xv[3], rq, (rb * 128 + 32 * j) * 1024 + (c ^ crot) * 128); };
    auto chw = [&](int c, u32 sb) AI {
      dma3(sb + G_OFF + 3 * j * 1024, wv[0], wv[1], wv[2], rg, (c ^ crot) * 64 * F * 2);
      dma3(sb + U_OFF + 3 * j * 1024, wv[0], wv[1], wv[2], ru, (c ^ crot) * 64 * F * 2);
    };
    if (has) {
      chx(0, 0); chw(0, 0); chx(1, SLOT); chw(1, SLOT);
      int ic = 2, lc = 0;
      u32 sb = 2 * SLOT;
#pragma unroll 1
      for (u32 it = 0; it < SPIN_CAP * 16; ++it) {
        if (lc < ic && vm_pending() <= 10u * (u32)(ic - lc - 1)) {
          asm volatile("" ::: "memory");
          ++lc;
          lds_set(FULL + j, lc == 8 ? 9 : lc);
          if (lc == 1) STAMP(1);
          if (lc == 8) break;
        } else if (ic < 8 && all_ge(rfl(lds_ld1(FREE)), ic - 2)) {
          asm volatile("" ::: "memory");
          chx(ic, sb);
          if (lc < ic && vm_pending() <= 10u * (u32)(ic - lc - 1) + 4u) {
            lds_set(FULL + j, ++lc);
          }
          chw(ic, sb);
          ++ic;
          sb = sb == 2 * SLOT ? 0 : sb + SLOT;
        } else {
          __builtin_amdgcn_s_sleep(LSLEEP);
        }
      }
      waitv<0>();
    } else if (csw) {
      sfor<8>([&](auto c) AI {
        if constexpr (c >= 5) { waitv<24>(); lds_set(FULL + j, (6u * c - vm_pending()) * 43u >> 8); wait_min4<1>(FREE, c - 4); }
        chw(c, (c % 5) * CSLOT - G_OFF);
        if constexpr (c >= 2) lds_set(FULL + j, (6u * (c + 1) - vm_pending()) * 43u >> 8);
      });
      waitv<12>(); lds_set(FULL + j, 6);
      waitv<6>(); lds_set(FULL + j, 7);
      waitv<0>(); lds_set(FULL + j, 8);
    }
    STAMP(2);
    const rsrc_t rd = mk_rsrc(mkp(kb[0], kb[1]), F * D * 2);
    u32 dv[4];
    sfor<4>([&](auto t) AI { dv[t] = (7 * t + l8) * 1024 + ((c8 ^ (((l8 >> 1) & 1) << 2)) << 4) + ct * 128; });
    auto wso = [&](int g) AI { return (u32)(((g >> 1) & 1) * 688 + (g >> 2) * 64 + (g & 1) * 32) * 1024u; };
    auto wd_groups = [&](int lo, int hi) AI {
#pragma unroll 1
      for (int g = lo + j; g < hi; g += 4) { dma4(g * 4096, dv[0], dv[1], dv[2], dv[3], rd, wso(g)); waitv<WDFL>(); }
    };
    u32x4 wr[5][4];
    auto wst = [&](auto t, u32 a) AI { sfor<4>([&](auto i) AI { pin(wr[t][i]); ldsw<u32x4>(a + j * 4096 + i * 1024 + lane * 16, wr[t][i]); }); };
    auto wrl = [&]() AI { sfor<5>([&](auto t) AI { const u32 so = wso(24 + 4 * t + j); sfor<4>([&](auto i) AI { bld4o<i * 1024>(wr[t][i], dv[i], rd, so); }); waitv<WRFL>(); }); };
    wait_min4<1>(FREE, WD1);
    wd_groups(21, 24);
    wait_min4<1>(FREE, WD2);
    wd_groups(0, 10);
    wait_min4<1>(FREE, WD3);
    wd_groups(10, 21);
    waitv<0>();
    lds_set(WDRDY + j, 1);
    wait_min4<1>(FREE, WRLAST);
    wrl();
    waitv<0>();
    lds_set(WDRDY + j, 1);
    STAMP(3);
    wait_min4<4>(CVP, 5);
    wst(IC<0>{}, XCH); wst(IC<1>{}, CONV); wst(IC<2>{}, CONV + CH_SZ);
    waitl();
    lds_set(WDRDY + j, 2);
    STAMP(4);
    wait_min4<2>(FREE, 10);
    wst(IC<3>{}, 0); wst(IC<4>{}, 16384);
    waitl();
    lds_set(WDRDY + j, 3);
    STAMP(5);
    DONE();
  }
}

extern "C" void kernel_launch(void* const* d_in, const int* in_sizes, int n_in, void* d_out, int out_size, void* d_ws, size_t ws_size,
                              hipStream_t stream) {
  Args a;
  a.q = (const u16*)d_in[0]; a.k = (const u16*)d_in[1]; a.v = (const u16*)d_in[2]; a.sk = (const u16*)d_in[3];
  a.h = (const u16*)d_in[4]; a.dv = (const u16*)d_in[5];
  a.wg = (const u16*)d_in[7]; a.wu = (const u16*)d_in[8]; a.wd = (const u16*)d_in[9];
  a.out = (u16*)d_out; a.ws = (char*)d_ws; a.pad = 0;
  (void)hipFuncSetAttribute((const void*)fused, hipFuncAttributeMaxDynamicSharedMemorySize, LDS_BYTES);
  fused<<<256, 768, LDS_BYTES, stream>>>(a);
}
```

```cpp
#define WRLAST 9
#define WDFL 4
#include <hip/hip_runtime.h>
#include <stdint.h>
#include <utility>

typedef unsigned short u16;
typedef unsigned int u32;
typedef unsigned long long u64;
typedef __bf16 bf16x8 __attribute__((ext_vector_type(8)));
typedef __bf16 bf16x2 __attribute__((ext_vector_type(2)));
typedef short s16x4 __attribute__((ext_vector_type(4)));
typedef short s16x8 __attribute__((ext_vector_type(8)));
typedef float f32x16 __attribute__((ext_vector_type(16)));
typedef float f32x4 __attribute__((ext_vector_type(4)));
typedef float f32x2 __attribute__((ext_vector_type(2)));
typedef u32 u32x4 __attribute__((ext_vector_type(4)));
typedef u32 u32x2 __attribute__((ext_vector_type(2)));
typedef __amdgpu_buffer_rsrc_t rsrc_t;
#define DI __device__ __forceinline__
#define AI __attribute__((always_inline))
#define LAS __attribute__((address_space(3)))
#define GAS __attribute__((address_space(1)))
#define RLX_AGENT __ATOMIC_RELAXED, __HIP_MEMORY_SCOPE_AGENT
#define RLX_WG __ATOMIC_RELAXED, __HIP_MEMORY_SCOPE_WORKGROUP

constexpr int S = 2048, D = 512, F = 1376;
#define UPAD 32
#define SLOTB (40960 + UPAD)
constexpr u32 SLOT = SLOTB, G_OFF = 16384, U_OFF = 16384 + 12288 + UPAD;
constexpr u32 CSLOT = 2 * (12288 + UPAD) + 64 - 2 * UPAD % 64;
constexpr u32 CONV = 3 * 41216;
constexpr u32 CH_SZ = 17520, HA = 64, HB = 4292, KA = 8400, KB = 12976, XB = 4304;
constexpr u32 MISC = CONV + 2 * CH_SZ;
constexpr u32 CS = MISC, ST = MISC + 1536, CNT = MISC + 2560;
constexpr u32 CKV = CNT + 8, CSP = CNT + 12, EPO = CNT + 16, CVP = CNT + 24, FULL = CNT + 32, FREE = CNT + 36, STRDY = CNT + 40, WDRDY = CNT + 44;
constexpr u32 LDS_BYTES = MISC + 2816;
static_assert(LDS_BYTES <= 163840, "LDS");
constexpr u32 XCH = 98304, ZST = 114688, PART = 32768;
constexpr u32 W_E = 0, W_PF = 4096, W_HF = W_PF + 16384, W_ZF = W_HF + 16384, W_SG = W_ZF + 16384, W_CG = W_SG + 16384;
constexpr u32 W_KV = 131072, W_ZT = W_KV + 2097152 + 4096, W_H = W_ZT + 4194304, W_END = W_H + 64 * 86 * 1024;
constexpr u32 W_DBG = 16u << 20;
constexpr u32 CODE_BYTES = 26112;
#define PRL 1
#define PRS 0
#define CONVAFTER 9
#define LSLEEP 1
#define PRA 0
#define ROT 0
#define S2AFTER 8
#define KVSTAG 0
#define SSTOP 9
#define POLLS 4
#define PD 10
#define WD1 6
#define WD2 7
#define PFLO 8
#define PFPRIO 0
#define PFWAIT 0
#define KVMODE 0
#define WD3 8
#define PRE 2
#define PRW 0
#define WSL 1
#define PRC 3
#define ZSLEEP 4
#define ZAFTER 11
#define WRFL 40
#define KVAFTER 1
constexpr u32 SPIN_CAP = 1u << 18;

constexpr int cpair(int g) {
  int i = 0;
  for (int n = 0; n < 8; ++n) for (int m = 0; m <= n; ++m) if (m + n <= 7) {
    if (i++ == g) return n * 8 + m;
    if (m < n && i++ == g) return m * 8 + n;
  }
  return 0;
}
template <class Fn, int... I> DI void sfor_impl(Fn&& f, std::integer_sequence<int, I...>) { (f(std::integral_constant<int, I>{}), ...); }
template <int N, class Fn> DI void sfor(Fn&& f) { sfor_impl(f, std::make_integer_sequence<int, N>{}); }

DI rsrc_t mk_rsrc(const void* p, u32 bytes) { return __builtin_amdgcn_make_buffer_rsrc((void*)p, (short)0, (int)bytes, 0x00020000); }
DI void dma(u32 lds, u32 voff, rsrc_t r, u32 soff) {
  asm volatile("s_mov_b32 m0, %0\n\ts_nop 0\n\tbuffer_load_dwordx4 %1, %2, %3 offen lds" ::"s"(lds), "v"(voff), "s"(r), "s"(soff) : "memory");
}

DI void dma4(u32 lds, u32 v0, u32 v1, u32 v2, u32 v3, rsrc_t r, u32 soff) {
  asm volatile("s_mov_b32 m0, %0\n\ts_nop 0\n\tbuffer_load_dwordx4 %1, %5, %6 offen lds\n\tbuffer_load_dwordx4 %2, %5, %6 offen offset:1024 lds\n\t"
               "buffer_load_dwordx4 %3, %5, %6 offen offset:2048 lds\n\tbuffer_load_dwordx4 %4, %5, %6 offen offset:3072 lds"
               ::"s"(lds), "v"(v0), "v"(v1), "v"(v2), "v"(v3), "s"(r), "s"(soff) : "memory");
}
DI void dma3(u32 lds, u32 v0, u32 v1, u32 v2, rsrc_t r, u32 soff) {
  asm volatile("s_mov_b32 m0, %0\n\ts_nop 0\n\tbuffer_load_dwordx4 %1, %4, %5 offen lds\n\tbuffer_load_dwordx4 %2, %4, %5 offen offset:1024 lds\n\t"
               "buffer_load_dwordx4 %3, %4, %5 offen offset:2048 lds"
               ::"s"(lds), "v"(v0), "v"(v1), "v"(v2), "s"(r), "s"(soff) : "memory");
}
template <int OFF> DI void bld4_sc1(u32x4& d, u32 voff, rsrc_t r, u32 soff) {
  asm volatile("buffer_load_dwordx4 %0, %1, %2, %3 offen offset:%4 sc1" : "=v"(d) : "v"(voff), "s"(r), "s"(soff), "i"(OFF));
}
template <int OFF> DI void bld4o(u32x4& d, u32 voff, rsrc_t r, u32 soff) { asm volatile("buffer_load_dwordx4 %0, %1, %2, %3 offen offset:%4" : "=v"(d) : "v"(voff), "s"(r), "s"(soff), "i"(OFF)); }
DI void bld4(u32x4& d, u32 voff, rsrc_t r, u32 soff) { asm volatile("buffer_load_dwordx4 %0, %1, %2, %3 offen" : "=v"(d) : "v"(voff), "s"(r), "s"(soff)); }
template <int OFF> DI void bld2(u32x2& d, u32 voff, rsrc_t r) { asm volatile("buffer_load_dwordx2 %0, %1, %2, 0 offen offset:%3" : "=v"(d) : "v"(voff), "s"(r), "i"(OFF)); }
template <int OFF> DI void gld4_sc1(u32x4& d, u32 voff, const char* base) {
  asm volatile("global_load_dwordx4 %0, %1, %2 offset:%3 sc1" : "=v"(d) : "v"(voff), "s"(base), "i"(OFF));
}
template <int N> DI void waitv_pin(u32x4& d) { asm volatile("s_waitcnt vmcnt(%1)" : "+v"(d) : "i"(N)); }
template <int N> DI void waitv() { asm volatile("s_waitcnt vmcnt(%0)" ::"i"(N) : "memory"); }
DI u32 vm_pending() { u32 r; asm volatile("s_getreg_b32 %0, hwreg(HW_REG_IB_STS)" : "=s"(r)::"memory"); return (r & 15u) | ((r >> 18) & 48u); }
DI void bar() { asm volatile("s_waitcnt lgkmcnt(0)\n\ts_barrier" ::: "memory"); }
DI void waitl() { asm volatile("s_waitcnt lgkmcnt(0)" ::: "memory"); }
DI float bflo(u32 x) { return __uint_as_float(x << 16); }
DI float bfhi(u32 x) { return __uint_as_float(x & 0xffff0000u); }
DI u32 pk2(float lo, float hi) { f32x2 v = {lo, hi}; return __builtin_bit_cast(u32, __builtin_convertvector(v, bf16x2)); }
DI u32 pk2s(float lo, float hi) { asm("" : "+v"(lo)); asm("" : "+v"(hi)); return pk2(lo, hi); }
DI float rbf(float x) { return bflo(pk2(x, x)); }
template <class T> DI T ldsr(u32 a) { return *(const LAS T*)a; }
template <class T> DI void ldsw(u32 a, T v) { *(LAS T*)a = v; }
DI u32x2 ldtr(u32 a) { return __builtin_bit_cast(u32x2, __builtin_amdgcn_ds_read_tr16_b64_v4i16((LAS s16x4*)a)); }
DI bf16x8 cat(u32x2 a, u32x2 b) { u32x4 v = {a.x, a.y, b.x, b.y}; return __builtin_bit_cast(bf16x8, v); }
template <int CTRL, int RM> DI float dpp_add(float v) {
  return v + __builtin_bit_cast(float, __builtin_amdgcn_update_dpp(0, __builtin_bit_cast(int, v), CTRL, RM, 0xf, false));
}
DI float wave_sum(float v) {
  v = dpp_add<0x111, 0xf>(v); v = dpp_add<0x112, 0xf>(v); v = dpp_add<0x114, 0xf>(v); v = dpp_add<0x118, 0xf>(v);
  v = dpp_add<0x142, 0xa>(v); v = dpp_add<0x143, 0xc>(v);
  return __builtin_bit_cast(float, __builtin_amdgcn_readlane(__builtin_bit_cast(int, v), 63));
}
DI void wave_sum2(float& a, float& c) {
  a = dpp_add<0x111, 0xf>(a); c = dpp_add<0x111, 0xf>(c); a = dpp_add<0x112, 0xf>(a); c = dpp_add<0x112, 0xf>(c);
  a = dpp_add<0x114, 0xf>(a); c = dpp_add<0x114, 0xf>(c); a = dpp_add<0x118, 0xf>(a); c = dpp_add<0x118, 0xf>(c);
  a = dpp_add<0x142, 0xa>(a); c = dpp_add<0x142, 0xa>(c); a = dpp_add<0x143, 0xc>(a); c = dpp_add<0x143, 0xc>(c);
  a = __builtin_bit_cast(float, __builtin_amdgcn_readlane(__builtin_bit_cast(int, a), 63));
  c = __builtin_bit_cast(float, __builtin_amdgcn_readlane(__builtin_bit_cast(int, c), 63));
}
DI void gld8_sc1(u64& d, const char* p) { asm volatile("global_load_dwordx2 %0, %1, off sc1" : "=v"(d) : "v"(p)); }
DI void gld_u16(u32& d, u32 voff, const char* base) { asm volatile("global_load_ushort %0, %1, %2" : "=v"(d) : "v"(voff), "s"(base)); }
template <int SLEEP> DI void poll_flags(const char* ws, u32 off, int n, u64 epoch, int lane) {
  const GAS u64* p = (const GAS u64*)(ws + off + (u32)(lane < n ? lane : 0) * 64u);
#pragma unroll 1
  for (u32 it = 0; it < SPIN_CAP; ++it) {
    u64 v = __hip_atomic_load(p, RLX_AGENT);
    if (__all(v == epoch)) break;
    __builtin_amdgcn_s_sleep(SLEEP);
  }
  asm volatile("" ::: "memory");
}
DI u32 poll_granule(const char* ws, u32 row, u32 tag) {
  const GAS u64* p = (const GAS u64*)(ws + W_SG + row * 8u);
  u64 v = 0;
#pragma unroll 1
  for (u32 it = 0; it < SPIN_CAP; ++it) {
    v = __hip_atomic_load(p, RLX_AGENT);
    if (__all((u32)(v >> 32) == tag)) break;
    __builtin_amdgcn_s_sleep(1);
  }
  return (u32)v;
}
DI u32 lds_arrive(u32 a, int lane) {
  u32 r = 0;
  waitl();
  if (lane == 0) r = __hip_atomic_fetch_add((LAS u32*)a, 1u, RLX_WG);
  return (u32)__builtin_amdgcn_readfirstlane(r) + 1u;
}
DI void lds_wait(u32 a, u32 want) {
#pragma unroll 1
  for (u32 it = 0; it < SPIN_CAP * 16; ++it) {
    u32 v = __hip_atomic_load((LAS u32*)a, RLX_WG);
    if ((u32)__builtin_amdgcn_readfirstlane(v) >= want) break;
  }
  asm volatile("" ::: "memory");
}

DI int lane_id() { int l; asm volatile("v_mbcnt_lo_u32_b32 %0, -1, 0\n\tv_mbcnt_hi_u32_b32 %0, -1, %0" : "=v"(l)); return l; }
DI u32 rfl(u32 x) { return (u32)__builtin_amdgcn_readfirstlane(x); }
DI u32x2 lds_ld2(u32 a) { return __builtin_bit_cast(u32x2, __hip_atomic_load((LAS u64*)a, RLX_WG)); }
template <int SLEEP> DI void wait_min2(u32 a, u32 want) {
#pragma unroll 1
  for (u32 it = 0; it < SPIN_CAP * 16; ++it) {
    const u32x2 v = lds_ld2(a);
    if (rfl(min(v.x, v.y)) >= want) break;
    if (SLEEP) __builtin_amdgcn_s_sleep(SLEEP);
  }
  asm volatile("" ::: "memory");
}
DI u32 lds_ld1(u32 a) { return __hip_atomic_load((LAS u32*)a, RLX_WG); }
DI bool all_ge(u32 w, u32 want) { return ((w - want * 0x01010101u) & 0x80808080u) == 0u; }
template <int SLEEP> DI void wait_min4(u32 a, u32 want) {
#pragma unroll 1
  for (u32 it = 0; it < SPIN_CAP * 16; ++it) {
    if (all_ge(rfl(lds_ld1(a)), want)) break;
    if (SLEEP) __builtin_amdgcn_s_sleep(SLEEP);
  }
  asm volatile("" ::: "memory");
}
template <int SLEEP> DI void wait_byte(u32 a, u32 want) {
#pragma unroll 1
  for (u32 it = 0; it < SPIN_CAP * 16; ++it) {
    if (rfl(__hip_atomic_load((LAS unsigned char*)a, RLX_WG)) >= want) break;
    if (SLEEP) __builtin_amdgcn_s_sleep(SLEEP);
  }
  asm volatile("" ::: "memory");
}
DI u64 wait_epoch() {
  u32x2 e;
#pragma unroll 1
  for (u32 it = 0; it < SPIN_CAP * 16; ++it) {
    e = lds_ld2(EPO);
    if (rfl(e.x) != 0u) break;
    __builtin_amdgcn_s_sleep(2);
  }
  return ((u64)rfl(e.y) << 32) | rfl(e.x);
}
DI u64 gld_agent(const char* p) { return __hip_atomic_load((const GAS u64*)p, RLX_AGENT); }
DI void lds_set(u32 a, u32 v) { __hip_atomic_store((LAS unsigned char*)a, (unsigned char)v, RLX_WG); }
DI void gather(u32& junk, u32 voff, rsrc_t r, u32 soff) { asm volatile("buffer_load_dword %0, %1, %2, %3 offen" : "=v"(junk) : "v"(voff), "s"(r), "s"(soff)); }
template <int N> using IC = std::integral_constant<int, N>;

#define DONE() do {} while (0)
#define STAMP(id) do {} while (0)

struct Args { const u16 *q, *wg, *wu, *k, *v, *sk, *h, *dv, *wd; u16* out; char* ws; u64 pad; };
typedef u32 u32x16 __attribute__((ext_vector_type(16)));
typedef u32 u32x8 __attribute__((ext_vector_type(8)));
DI char* mkp(u32 lo, u32 hi) { return (char*)(((u64)hi << 32) | lo); }
#define GAP() __builtin_amdgcn_sched_barrier(0)
DI void barx() { asm volatile("s_barrier" ::: "memory"); }
template <class T> DI void spin(T& x) { asm volatile("" : "+s"(x)); }
template <class T> DI void pin(T& x) { asm volatile("" : "+v"(x)); }

__global__ void __launch_bounds__(768, 3) fused(Args) {
  u64 pc0; asm volatile("s_getpc_b64 %0" : "=s"(pc0));
  u32x16 ka; u32x8 kb;
  asm volatile("s_load_dwordx16 %0, %2, 0x0\n\ts_load_dwordx8 %1, %2, 0x40" : "=&s"(ka), "=&s"(kb) : "s"(__builtin_amdgcn_kernarg_segment_ptr()));
  GAP();
  const int tid = threadIdx.x, lane = tid & 63;
  const int wave = __builtin_amdgcn_readfirstlane(tid >> 6);
  const int b = blockIdx.x, xcd = b & 7, li = b >> 3;
  if (tid < 16) ldsw<u32>(CNT + tid * 4, 0u);
  const bool has = li < 30;
  const int id = 30 * xcd + (has ? li : 0), rr_ = id % 120, rb = 8 * (id / 120) + (rr_ & 7);
  const int cg = has ? rr_ >> 3 : 4 * (xcd & 3) + 2 * (xcd >> 2) + (li - 30);
  const bool csw = !has && cg < 15;
  const int rt = 4 * xcd + (li & 3), ct = li >> 2;
  const int j = wave & 3, l8 = lane >> 3, c8 = lane & 7;
  u32 xv[4], wv[3];
  sfor<4>([&](auto t) AI { xv[t] = (7 * t + l8) * 1024 + ((c8 ^ ((4 * t + (l8 >> 1)) & 7)) << 4); pin(xv[t]); });
  sfor<3>([&](auto t) AI {
    const u32 byte = t * 1024 + lane * 16, row = byte / 192u;
    const u32 seg = byte - row * 192u;
    wv[t] = cg * 192 + seg >= F * 2 ? 0x40000000u : (16 * j + row) * (F * 2) + cg * 192 + seg - t * 1024;
    pin(wv[t]);
  });
  asm volatile("s_waitcnt lgkmcnt(0)\n\ts_barrier" : "+s"(ka), "+s"(kb)::"memory");
  STAMP(9);
  char* const ws = mkp(kb[4], kb[5]);
  const rsrc_t rq = mk_rsrc(mkp(ka[0], ka[1]), S * D * 2);
  if (wave >= 8) {
    __builtin_amdgcn_s_setprio(3);
    u64 epoch;
    asm volatile("s_load_dwordx2 %0, %1, 0x0" : "=s"(epoch) : "s"(ws));
    const int lane = lane_id(), l8 = lane >> 3, c8 = lane & 7;
    const int ch = j & 1, cp = j >> 1, c16 = lane & 15, q = lane >> 4;
    const rsrc_t rws = mk_rsrc(ws, W_END);
    const u32 cb = CONV + ch * CH_SZ, hown = cb + (cp ? XB : HA), kown = cb + (cp ? KB : KA);
    const int srow = 256 * xcd + 8 * li + 2 * j;
    if (SSTOP == 0) wait_min4<8>(FREE, 8);
    u32x4 sx[2];
    sfor<2>([&](auto r) AI { bld4(sx[r], lane * 16, rq, (srow + r) * 1024); });
    STAMP(23);
    const int pa = b & 31, pg = b >> 5, s0 = 64 * pa + 16 * j;
    u32x2 skw[2];
    u32 dvv;
    auto kvload = [&]() AI {
      const rsrc_t rk = mk_rsrc(mkp(ka[6], ka[7]), S * D * 2), rv = mk_rsrc(mkp(ka[8], ka[9]), S * D * 2), rs_ = mk_rsrc(mkp(ka[10], ka[11]), D * 8);
      const u32 kvv = l8 * 1024 + c8 * 16 + pg * 128;
      sfor<3>([&](auto t) AI { const int r = s0 - 8 + 8 * t; dma(hown + t * 1024, kvv, rk, (r < 0 ? 0 : r) * 1024); });
      sfor<2>([&](auto t) AI { dma(kown + t * 1024, kvv, rv, (s0 + 8 * t) * 1024); });
      sfor<2>([&](auto t) AI { bld2<256 * t>(skw[t], (64 * pg + 16 * (q >> 1) + c16) * 8, rs_); });
      gld_u16(dvv, (2 * b + ch) * 2, mkp(ka[14], ka[15]));
    };
    asm volatile("s_waitcnt lgkmcnt(0)" : "+s"(epoch));
    epoch += 0x9E3779B97F4A7C15ull;
    epoch = (epoch ^ (epoch >> 30)) * 0xBF58476D1CE4E5B9ull; epoch = (epoch ^ (epoch >> 27)) * 0x94D049BB133111EBull;
    epoch = (epoch ^ (epoch >> 31)) | 1ull;
    ldsw<u64>(EPO, epoch);
    STAMP(18);
    const u32 tag = (u32)(epoch >> 17);
    if (has) wait_min4<2>(FULL, KVAFTER + KVSTAG * (li & 3));
    STAMP(19);
    kvload();
    STAMP(20);
    asm volatile("s_waitcnt vmcnt(8)" : "+v"(sx[0]), "+v"(sx[1]));
    STAMP(21);
    __builtin_amdgcn_s_setprio(0);
    {
      float x[2][8], mean[2], var[2];
      sfor<2>([&](auto r) AI {
        sfor<4>([&](auto i) AI { x[r][2 * i] = bflo(sx[r][(int)i]); x[r][2 * i + 1] = bfhi(sx[r][(int)i]); });
        mean[r] = x[r][0];
        sfor<7>([&](auto i) AI { mean[r] += x[r][i + 1]; });
      });
      wave_sum2(mean[0], mean[1]);
      sfor<2>([&](auto r) AI {
        mean[r] *= 1.0f / D; var[r] = 0.f;
        sfor<8>([&](auto i) AI { float d = x[r][i] - mean[r]; asm("" : "+v"(d)); var[r] += d * d; });
      });
      wave_sum2(var[0], var[1]);
      STAMP(22);
      const float mn = lane ? mean[1] : mean[0], vr = rbf((lane ? var[1] : var[0]) * (1.0f / D));
      const u32 g = pk2s(mn, __builtin_sqrtf(rbf(vr + 1e-5f)));
      if (lane < 2) __hip_atomic_store((GAS u64*)(ws + W_SG + (srow + lane) * 8), ((u64)tag << 32) | g, RLX_AGENT);
    }
    STAMP(1);
    asm volatile("s_waitcnt vmcnt(1)" : "+v"(skw[0]), "+v"(skw[1]), "+v"(dvv)::"memory");
    STAMP(2);
    {
      const bool first = s0 == 0 && (q & 1) == 0;
      u32x2 kr[2][5];
      sfor<2>([&](auto gi) AI {
        const u32 a = (8 * (q & 1) + (c16 >> 2)) * 128 + 64 * gi + 32 * (q >> 1) + 8 * (lane & 3);
        kr[gi][0] = ldtr(hown + a + 512); kr[gi][1] = ldtr(hown + a + 1024); kr[gi][2] = ldtr(hown + a + 1536); kr[gi][3] = ldtr(kown + a); kr[gi][4] = ldtr(kown + a + 512);
      });
      GAP();
      if (SSTOP == 1) wait_min4<8>(FREE, 8);
      sfor<2>([&](auto gi) AI {
        const float w0 = bflo(skw[gi].x), w1 = bfhi(skw[gi].x), w2 = bflo(skw[gi].y), w3 = bfhi(skw[gi].y);
        const u32x2 k0 = kr[gi][0], k1 = kr[gi][1], k2 = kr[gi][2], v0 = kr[gi][3], v1 = kr[gi][4];
        const float kk[11] = {first ? 0.f : bfhi(k0.x), first ? 0.f : bflo(k0.y), first ? 0.f : bfhi(k0.y), bflo(k1.x), bfhi(k1.x), bflo(k1.y), bfhi(k1.y), bflo(k2.x), bfhi(k2.x), bflo(k2.y), bfhi(k2.y)};
        const float vv[8] = {bflo(v0.x), bfhi(v0.x), bflo(v0.y), bfhi(v0.y), bflo(v1.x), bfhi(v1.x), bflo(v1.y), bfhi(v1.y)};
        u32 a4[4], m4[4];
        sfor<4>([&](auto t) AI { a4[t] = pk2s(w3 * kk[2 * t], w3 * kk[2 * t + 1]); m4[t] = pk2s(w2 * kk[2 * t + 1], w2 * kk[2 * t + 2]); });
        GAP();
        sfor<4>([&](auto t) AI { a4[t] = pk2s(bflo(a4[t]) + bflo(m4[t]), bfhi(a4[t]) + bfhi(m4[t])); m4[t] = pk2s(w1 * kk[2 * t + 2], w1 * kk[2 * t + 3]); });
        GAP();
        sfor<4>([&](auto t) AI { a4[t] = pk2s(bflo(a4[t]) + bflo(m4[t]), bfhi(a4[t]) + bfhi(m4[t])); m4[t] = pk2s(w0 * kk[2 * t + 3], w0 * kk[2 * t + 4]); });
        GAP();
        sfor<4>([&](auto t) AI { a4[t] = pk2s(bflo(a4[t]) + bflo(m4[t]), bfhi(a4[t]) + bfhi(m4[t])); });
        GAP();
        u32x4 ov;
        sfor<4>([&](auto t) AI { ov[3 - t] = pk2s(bfhi(a4[t]) * vv[2 * t + 1], bflo(a4[t]) * vv[2 * t]); });
        const int dg = 2 * gi + (q >> 1);
        ldsw<u32x4>(CONV + (dg & 1) * CH_SZ + (dg >> 1 ? KB : KA) + 2048 + c16 * 128 + (((7 - 2 * j - (q & 1)) ^ (c16 & 7)) << 4), ov);
      });
    }
    waitl();
    lds_set(CVP + j, 1);
    __builtin_amdgcn_s_setprio(3);
    STAMP(17);
    wait_min4<0>(CVP, 1);
    {
      u32x4 o[2];
      sfor<2>([&](auto t) AI { o[t] = ldsr<u32x4>(kown + 2048 + (8 * t + l8) * 128 + ((c8 ^ l8) << 4)); });
      sfor<2>([&](auto t) AI { __builtin_amdgcn_raw_buffer_store_b128(o[t], rws, W_KV + (64 * pg + 16 * j + 8 * t + l8) * 4096 + (1984 - 64 * pa) * 2 + c8 * 16, 0, 16); });
    }
    const int mh = j & 1, db = j >> 1, s31 = lane & 31, hh = lane >> 5;
    const int orow = 64 * rt + 32 * mh + s31, ocol = 64 * ct + 32 * db;
    u32x2 xr[4];
    u32 sg;
    {
      const char* p1 = ws + W_SG + (hh ? orow : rb * 128 + 32 * j + s31) * 8;
      const char* p2 = ws + W_CG + ((has ? cg : 0) * 192 + 48 * j + (lane < 48 ? lane : 0)) * 8;
      u64 g1, g2;
      gld8_sc1(g1, p1); gld8_sc1(g2, p2);
      asm volatile("s_waitcnt vmcnt(0)" : "+v"(g1), "+v"(g2)::"memory");
      if (lds_arrive(CKV, lane) == 4 && lane == 0) __hip_atomic_store((GAS u64*)(ws + W_PF + b * 64), epoch, RLX_AGENT);
      STAMP(3);
#pragma unroll 1
      for (u32 it = 0; it < SPIN_CAP; ++it) {
        if (__all((u32)(g1 >> 32) == tag && (u32)(g2 >> 32) == tag)) break;
        __builtin_amdgcn_s_sleep(4);
        g1 = gld_agent(p1); g2 = gld_agent(p2);
      }
      sg = (u32)g1;
      if (has) {
        if (lane < 32) ldsw<f32x2>(ST + (32 * j + lane) * 8, f32x2{bflo(sg), __builtin_amdgcn_rcpf(bfhi(sg))});
        if (lane < 48) ldsw<u32>(CS + (48 * j + lane) * 4, (u32)g2);
      }
      lds_set(STRDY + j, 1);
      STAMP(4);
    }
    if (has) wait_min4<4>(FULL, S2AFTER);
    STAMP(24);
    {
      u64 f;
      gld8_sc1(f, ws + W_PF + ((b >> 5) * 32 + (j == 0 ? s31 : 0)) * 64);
      dma4(cb + (cp ? HB : HA), lane * 16, lane * 16, lane * 16, lane * 16, mk_rsrc(mkp(ka[12], ka[13]), D * S * 2), (2 * b + ch) * 4096);
      if (lane < (cp ? 33 : 16)) ldsw<u32>(cb + (cp ? 4160 : 0) + lane * 4, 0u);
      if (lane < (cp ? 28 : 30)) ldsw<u32x4>(kown + 4096 + lane * 16, u32x4{0u, 0u, 0u, 0u});
      asm volatile("s_waitcnt vmcnt(4)" : "+v"(f));
      sg = (u32)__builtin_amdgcn_permlane32_swap(sg, sg, false, false)[1];
      if (j == 0) { if (!__all(f == epoch)) poll_flags<2>(ws, W_PF + (b >> 5) * 32 * 64, 32, epoch, lane); lds_set(CVP + 4, 1); }
      wait_byte<1>(CVP + 4, 1);
      u32x4 kvr[4];
      const char* src = ws + W_KV + (2 * b + ch) * 4096 + 2 * cp;
      sfor<4>([&](auto t) AI { gld4_sc1<t * 1024>(kvr[t], lane * 16, src); });
      wait_min4<1>(FREE, 6);
      sfor<2>([&](auto t) AI {
        const int r = 16 * t + (lane >> 2);
        dma(ZST + j * 2048 + t * 1024, r * 1024 + (((lane & 3) ^ ((r >> 1) & 3)) << 4), rq, (64 * rt + 32 * mh) * 1024 + ocol * 2);
      });
      asm volatile("s_waitcnt vmcnt(0)" : "+v"(kvr[0]), "+v"(kvr[1]), "+v"(kvr[2]), "+v"(kvr[3])::"memory");
      if (cp && lane == 63) kvr[3].w &= 0xffffu;
      sfor<4>([&](auto t) AI { ldsw<u32x4>(kown + t * 1024 + lane * 16, kvr[t]); });
      sfor<4>([&](auto g) AI { xr[g] = ldsr<u32x2>(ZST + j * 2048 + s31 * 64 + ((g ^ ((s31 >> 1) & 3)) << 4) + 8 * hh); });
      waitl();
      pin(xr[0]); pin(xr[1]); pin(xr[2]); pin(xr[3]);
      lds_set(CVP + j, 2);
      STAMP(5);
    }
    __builtin_amdgcn_s_setprio(0);
    wait_min4<1>(FREE, CONVAFTER);
    wait_byte<0>(CVP + (j ^ 2), 2);
    __builtin_amdgcn_s_setprio(PRC);
    STAMP(6);
    f32x4 dacc[8];
    sfor<8>([&](auto i) AI { dacc[i] = f32x4{0.f, 0.f, 0.f, 0.f}; });
    u32 kv2[8];
    {
      u32 hb = cb + ((c16 & 1) ? HA : HB) + 4 * (c16 - 15) + 16 * q + 256 * cp;
      u32 kb_ = cb + ((c16 & 1) ? KA : KB) + 448 - 64 * (c16 >> 1) + 16 * q + 256 * cp;
      u32 hb2 = hb + 8;
      pin(hb); pin(hb2); pin(kb_);
      bf16x8 ta[12], kf[12];
      auto ld1 = [&](auto odd, auto n, auto w, auto off) AI {
        constexpr int r = (n < 4 && odd) ? 8 + n : n;
        if constexpr (w == 0) kf[r] = ldsr<bf16x8>(kb_ + 512 * (7 - n) + off);
        if constexpr (w == 1) { const u32x2 v = ldsr<u32x2>(hb + 512 * n + off); u32x4 t = __builtin_bit_cast(u32x4, ta[r]); t.x = v.x; t.y = v.y; ta[r] = __builtin_bit_cast(bf16x8, t); }
        if constexpr (w == 2) { const u32x2 v = ldsr<u32x2>(hb2 + 512 * n + off); u32x4 t = __builtin_bit_cast(u32x4, ta[r]); t.z = v.x; t.w = v.y; ta[r] = __builtin_bit_cast(bf16x8, t); }
      };
      auto cstep = [&](auto odd, auto off) AI {
        sfor<36>([&](auto g) AI {
          constexpr int c = cpair(35 - g) >> 3, dl = cpair(35 - g) & 7;
          constexpr int rc = (c < 4 && odd) ? 8 + c : c, rd_ = (dl < 4 && odd) ? 8 + dl : dl;
          dacc[c + dl] = __builtin_amdgcn_mfma_f32_16x16x32_bf16(kf[rc], ta[rd_], dacc[c + dl], 0, 0, 0);
          if constexpr (g < 12) ld1(IC<1 - odd>{}, IC<g / 3>{}, IC<g % 3>{}, off);
          else if constexpr (g < 24) ld1(IC<1 - odd>{}, IC<7 - (g - 12) / 3>{}, IC<g % 3>{}, off);
          GAP();
        });
      };
      sfor<24>([&](auto i) AI { ld1(IC<0>{}, IC<(i < 3 ? 0 : i < 6 ? 7 : i < 9 ? 1 : i < 12 ? 6 : i < 15 ? 2 : i < 18 ? 5 : i < 21 ? 3 : 4)>{}, IC<i % 3>{}, IC<0>{}); GAP(); });
#pragma unroll 1
      for (int i = 0; i < 2; ++i) {
        cstep(IC<0>{}, IC<64>{});
        cstep(IC<1>{}, IC<128>{});
        hb += 128; hb2 += 128; kb_ += 128;
      }
    }
    __builtin_amdgcn_s_setprio(3);
    STAMP(25);
    sfor<4>([&](auto i) AI { ldsw<f32x4>(XCH + j * 4096 + i * 1024 + lane * 16, cp ? dacc[i] : dacc[4 + i]); });
    lds_set(CVP + j, 4);
    {
      const int l = lane_id();
      u32 a = cb + KA + 2 * (2046 - 2 * (l & 15) - 64 * (l >> 4) - 1024 * cp) - 1600;
      pin(a);
      sfor<8>([&](auto i) AI { kv2[i] = ldsr<u32>(a + 64 * (1 - (i & 1)) + 512 * (3 - (i >> 1))); });
    }
    STAMP(7);
    {
      const float dv = bflo(dvv);
      sfor<8>([&](auto i) AI { kv2[i] = pk2s(bfhi(kv2[i]) * dv, bflo(kv2[i]) * dv); pin(kv2[i]); });
      wait_byte<0>(CVP + (j ^ 2), 4);
      f32x4 o[4];
      sfor<4>([&](auto i) AI { o[i] = ldsr<f32x4>(XCH + (j ^ 2) * 4096 + i * 1024 + lane * 16); });
      asm volatile("" ::: "memory");
      lds_set(CVP + j, 5);
      sfor<4>([&](auto i) AI {
        const f32x4 m = cp ? dacc[4 + i] : dacc[i];
        sfor<2>([&](auto rr) AI {
          const u32 yb = pk2s(m[2 * rr] + o[i][2 * rr], m[2 * rr + 1] + o[i][2 * rr + 1]), kd = kv2[2 * i + rr];
          ldsw<u32>(ZST + j * 2048 + 4 * c16 + 128 * q + 64 * rr + 512 * i, pk2s(bflo(yb) + bflo(kd), bfhi(yb) + bfhi(kd)));
        });
      });
      waitl();
      u32x2 zo[4];
      sfor<4>([&](auto t) AI { zo[t] = ldsr<u32x2>(ZST + j * 2048 + t * 512 + lane * 8); });
      sfor<4>([&](auto t) AI {
        __builtin_amdgcn_raw_buffer_store_b128(u32x4{zo[t].x, tag, zo[t].y, tag}, rws, lane * 16 + W_ZT + (2 * b + ch) * 8192 + cp * 4096 + t * 1024, 0, 16);
      });
    }
    STAMP(8);
    const float mean = bflo(sg), rs = __builtin_amdgcn_rcpf(bfhi(sg));
    u32 xn[8];
    sfor<8>([&](auto i) AI {
      const u32 x2 = xr[i >> 1][i & 1], n2 = pk2s(bflo(x2) - mean, bfhi(x2) - mean);
      xn[i] = pk2s(bflo(n2) * rs, bfhi(n2) * rs);
      pin(xn[i]);
    });
    if (b == 0 && j == 3) {
      sfor<4>([&](auto t) AI { poll_flags<1>(ws, W_PF + t * 4096, 64, epoch, lane); });
      if (lane == 0) __hip_atomic_store((GAS u64*)(ws + W_E), epoch, RLX_AGENT);
    }
    wait_min4<2>(FREE, ZAFTER);
    u32x4 zr[4];
#pragma unroll 1
    for (u32 it = 0; it < SPIN_CAP; ++it) {
      sfor<4>([&](auto t) AI { bld4_sc1<0>(zr[t], l8 * 8192 + c8 * 16, rws, W_ZT + (ocol + 8 * t) * 8192 + (64 * rt + 32 * mh) * 4); });
      asm volatile("s_waitcnt vmcnt(0)" : "+v"(zr[0]), "+v"(zr[1]), "+v"(zr[2]), "+v"(zr[3])::"memory");
      u32 bad = 0;
      sfor<4>([&](auto t) AI { bad |= (zr[t].y ^ tag) | (zr[t].w ^ tag); });
      if (__all(bad == 0u)) break;
      __builtin_amdgcn_s_sleep(ZSLEEP);
    }
    STAMP(10);
    sfor<4>([&](auto t) AI { ldsw<u32x2>(ZST + j * 2048 + t * 512 + lane * 8, u32x2{zr[t].x, zr[t].z}); });
    waitl();
    float tt[16];
    sfor<4>([&](auto g) AI {
      const u32x2 z4 = ldtr(ZST + j * 2048 + (8 * g + 4 * hh + ((lane & 15) >> 2)) * 64 + 32 * ((lane >> 4) & 1) + 8 * (lane & 3));
      sfor<2>([&](auto e) AI {
        const u32 x2 = xr[g][(int)e], z2 = z4[(int)e];
        u32 n2 = pk2s(bflo(xn[2 * g + e]) * bflo(z2), bfhi(xn[2 * g + e]) * bfhi(z2));
        n2 = pk2s(bflo(x2) + bflo(n2), bfhi(x2) + bfhi(n2));
        tt[4 * g + 2 * e] = bflo(n2); tt[4 * g + 2 * e + 1] = bfhi(n2);
      });
    });
    sfor<16>([&](auto i) AI { pin(tt[i]); });
    STAMP(11);
    wait_min4<0>(FREE, 12);
    STAMP(12);
    u32x2 ob[4];
    sfor<4>([&](auto g) AI {
      const f32x4 v = ldsr<f32x4>(PART + mh * 8192 + db * 4096 + g * 1024 + lane * 16) + ldsr<f32x4>(PART + (2 + mh) * 8192 + db * 4096 + g * 1024 + lane * 16);
      ob[g].x = pk2(tt[4 * g] + rs * v.x, tt[4 * g + 1] + rs * v.y);
      ob[g].y = pk2(tt[4 * g + 2] + rs * v.z, tt[4 * g + 3] + rs * v.w);
    });
    sfor<2>([&](auto gp) AI {
      auto s0 = __builtin_amdgcn_permlane32_swap(ob[2 * gp].x, ob[2 * gp + 1].x, false, false);
      auto s1 = __builtin_amdgcn_permlane32_swap(ob[2 * gp].y, ob[2 * gp + 1].y, false, false);
      const u32x4 v = {(u32)s0[0], (u32)s1[0], (u32)s0[1], (u32)s1[1]};
      *(GAS u32x4*)((GAS char*)mkp(kb[2], kb[3]) + orow * 1024 + ocol * 2 + 32 * gp + 16 * hh) = v;
    });
    STAMP(13);
    DONE();
  } else if (wave < 4) {
    u32 junk, junk2;
    asm volatile("s_load_dword %0, %2, %3\n\ts_load_dword %1, %2, %4" : "=&s"(junk), "=&s"(junk2)
                 : "s"(pc0 & ~127ull), "s"((u32)(li + 32 * j) * 128u), "s"(min((u32)(li + 32 * (4 + j)) * 128u, CODE_BYTES)));
    const rsrc_t rws = mk_rsrc(ws, W_END);
    const int wm = wave >> 1, wn = wave & 1;
    const int l0 = lane_id();
    u32 wa = ((l0 >> 4) & 1) * (12288 + UPAD) + (8 * (l0 >> 5) + ((l0 & 15) >> 2)) * 192 + 96 * wn + 8 * (l0 & 3);
    u64 epoch;
    f32x16 acc[2][3];
    sfor<2>([&](auto m) AI { sfor<3>([&](auto n) AI { sfor<16>([&](auto r) AI { acc[m][n][(int)r] = 0.f; }); pin(acc[m][n]); }); });
    u32 xa[4];
    sfor<4>([&](auto ks) AI { xa[ks] = (64 * wm + (l0 & 31)) * 128 + (((2 * ks + (l0 >> 5)) ^ ((l0 >> 1) & 7)) << 4); pin(xa[ks]); });
    asm volatile("s_waitcnt lgkmcnt(0)" : "+s"(junk), "+s"(junk2));
    STAMP(10);
    if (has) {
      bf16x8 xf[2][2], wf[2][3];
      auto ldw = [&](auto st, auto n) AI { const u32 a = wa + G_OFF + (st & 3) * 3072 + n * 32; wf[st & 1][n] = cat(ldtr(a), ldtr(a + 768)); };
      auto ldx = [&](auto st, auto m) AI { xf[st & 1][m] = ldsr<bf16x8>(xa[st & 3] + m * 4096); };
      wait_min4<1>(FULL, 1);
      u32 accw = 0, cntw = 0;
      STAMP(1);
      ldw(IC<0>{}, IC<0>{}); ldx(IC<0>{}, IC<0>{}); ldx(IC<0>{}, IC<1>{}); ldw(IC<0>{}, IC<1>{}); ldw(IC<0>{}, IC<2>{});
      ldw(IC<1>{}, IC<0>{}); ldw(IC<1>{}, IC<1>{}); ldx(IC<1>{}, IC<0>{});
      GAP();
      u32 needw = 0x01010101u, off = 0;
#pragma unroll 1
      for (int c = 0; c < 8; ++c) {
        u32 fl, adv, have;
        sfor<24>([&](auto gg) AI {
          constexpr int st = gg / 6, g = gg % 6, n = g >> 1, m = g & 1;
          acc[m][n] = __builtin_amdgcn_mfma_f32_32x32x16_bf16(wf[st & 1][n], xf[st & 1][m], acc[m][n], 0, 0, 0);
          if constexpr (g == 0) { ldw(IC<st + 1>{}, IC<2>{}); ldx(IC<st + 1>{}, IC<1>{}); }
          if constexpr (g == 2) ldw(IC<st + 2>{}, IC<0>{});
          if constexpr (g == 4) ldw(IC<st + 2>{}, IC<1>{});
          if constexpr (g == 5) ldx(IC<st + 2>{}, IC<0>{});
          if constexpr (st == 0 && g == 3) { const u32 o2 = off == 2 * SLOT ? 0u : off + SLOT; adv = o2 - off; off = o2; needw += 0x01010101u; spin(adv); spin(off); spin(needw); }
          if constexpr (st == 1 && g == 1) fl = lds_ld1(FULL);
          if constexpr (st == 1 && g == 3) { xa[0] += adv; xa[1] += adv; xa[2] += adv; pin(xa[0]); pin(xa[1]); pin(xa[2]); }
          if constexpr (st == 2 && g == 0) { have = (rfl(fl) - needw) & 0x80808080u; spin(have); }
          if constexpr (st == 2 && g == 1) {
            xa[3] += adv; wa += adv; pin(xa[3]); pin(wa);
            if (__builtin_expect(have != 0u, 0)) wait_min4<0>(FULL, c + 2);
          }
          if constexpr (st == 3 && g == 1) lds_set(FREE + wave, c + 1);
          GAP();
        });
      }
      STAMP(2);
      __builtin_amdgcn_s_setprio(PRE);
      const int lane = lane_id(), s31 = lane & 31, hh = lane >> 5;
      f32x2 st[2];
      f32x4 ca[4];
      auto ldc = [&](auto n) AI { sfor<4>([&](auto g) AI { const u32 a = CS + (wn * 96 + n * 32 + 8 * g + 4 * hh) * 4; ca[g] = ldsr<f32x4>(a); }); };
#pragma unroll 1
      for (u32 it = 0; it < SPIN_CAP * 16; ++it) {
        const u32 r1 = lds_ld1(STRDY);
        asm volatile("" ::: "memory");
        const u32x2 e = lds_ld2(EPO);
        sfor<2>([&](auto m) AI { st[m] = ldsr<f32x2>(ST + (64 * wm + 32 * m + s31) * 8); });
        ldc(IC<0>{});
        asm volatile("" ::: "memory");
        epoch = ((u64)rfl(e.y) << 32) | rfl(e.x);
        if (__builtin_expect(all_ge(rfl(r1), 1), 1)) break;
      }
      STAMP(11);
      sfor<3>([&](auto n) AI {
        const int ks = 6 * cg + 3 * wn + n;
        if (ks < 86) {
          f32x4 c4[4];
          sfor<4>([&](auto g) AI { c4[g] = ca[g]; });
          f32x2 g[8], u[8], e[8];
          sfor<8>([&](auto i) AI {
            constexpr int m = i >> 2, r = 2 * (i & 3);
            const f32x2 cg2 = {c4[r >> 2][r & 3], c4[r >> 2][(r & 3) + 1]};
            g[i] = f32x2{acc[m][n][r], acc[m][n][r + 1]} - st[m].x * cg2;
          });
          GAP();
          sfor<8>([&](auto i) AI { g[i] = g[i] * st[i >> 2].y; });
          GAP();
          sfor<8>([&](auto i) AI { e[i] = g[i] * g[i]; });
          GAP();
          sfor<8>([&](auto i) AI { e[i] = e[i] * -0.1029432397f - 2.3022081985f; });
          GAP();
          sfor<8>([&](auto i) AI { e[i] = e[i] * g[i]; });
          GAP();
          sfor<8>([&](auto i) AI {
            constexpr int m = i >> 2, r = 2 * (i & 3);
            const f32x2 cu2 = {c4[2 + (r >> 2)][r & 3], c4[2 + (r >> 2)][(r & 3) + 1]};
            e[i] = f32x2{__builtin_amdgcn_exp2f(e[i].x), __builtin_amdgcn_exp2f(e[i].y)};
            u[i] = f32x2{acc[m][n][r + 8], acc[m][n][r + 9]} - st[m].x * cu2;
            GAP();
          });
          if constexpr (n < 2) ldc(IC<n + 1>{});
          sfor<8>([&](auto i) AI { e[i] = e[i] + 1.0f; });
          GAP();
          sfor<8>([&](auto i) AI {
            e[i] = f32x2{__builtin_amdgcn_rcpf(e[i].x), __builtin_amdgcn_rcpf(e[i].y)};
            g[i] = g[i] * u[i];
            GAP();
          });
          sfor<8>([&](auto i) AI { g[i] = g[i] * e[i]; });
          GAP();
          sfor<2>([&](auto m) AI {
            const u32x4 o = {pk2(g[4 * m].x, g[4 * m].y), pk2(g[4 * m + 1].x, g[4 * m + 1].y), pk2(g[4 * m + 2].x, g[4 * m + 2].y), pk2(g[4 * m + 3].x, g[4 * m + 3].y)};
            __builtin_amdgcn_raw_buffer_store_b128(o, rws, lane * 16 + W_H + ((4 * rb + 2 * wm + m) * 86 + ks) * 1024, 0, 16);
          });
          GAP();
          if constexpr (n < 2) STAMP(20 + n);
        }
      });
      lds_set(FREE + wave, 9);
      STAMP(3);
      waitv<0>();
      __builtin_amdgcn_s_setprio(PRW);
      if (lds_arrive(CNT + 0, lane) == 4 && lane == 0) __hip_atomic_store((GAS u64*)(ws + W_HF + (rb * 16 + cg) * 64), epoch, RLX_AGENT);
      STAMP(4);
    } else {
      if (csw) {
        f32x4 cs[3];
        sfor<3>([&](auto n) AI { cs[n] = f32x4{0.f, 0.f, 0.f, 0.f}; });
        const u32 o2_ = (l0 & 15) == ((l0 >> 4) & 1) ? 0x3f803f80u : 0u;
        const bf16x8 sel = __builtin_bit_cast(bf16x8, u32x4{o2_, o2_, o2_, o2_});
        wa += wm * 6144;
#pragma unroll 1
        for (int c = 0; c < 8; ++c) {
          wait_min4<1>(FULL, c + 1);
          bf16x8 w[6];
          sfor<6>([&](auto i) AI { const u32 a = wa + (i / 3) * 3072 + (i % 3) * 32; w[i] = cat(ldtr(a), ldtr(a + 768)); });
          asm volatile("" ::: "memory");
          lds_set(FREE + wave, c < 3 ? c + 1 : 3);
          sfor<6>([&](auto i) AI { cs[i % 3] = __builtin_amdgcn_mfma_f32_16x16x32_bf16(w[i], sel, cs[i % 3], 0, 0, 0); });
          wa += c == 4 ? (u32)(-4 * (int)CSLOT) : CSLOT;
        }
        if ((l0 & 15) < 2) sfor<3>([&](auto n) AI { ldsw<f32x4>(CS + (wm * 192 + wn * 96 + n * 32 + (l0 & 15) * 16 + 4 * (l0 >> 4)) * 4, cs[n]); });
      }
      lds_set(FREE + wave, 9);
      epoch = wait_epoch();
      if (csw && wave < 3) {
        wait_min4<1>(FREE, 9);
        const u32 i = 64 * wave + l0;
        const float v = ldsr<float>(CS + i * 4) + ldsr<float>(CS + 768 + i * 4);
        __hip_atomic_store((GAS u64*)(ws + W_CG + (cg * 192 + i) * 8), ((u64)(u32)(epoch >> 17) << 32) | __float_as_uint(v), RLX_AGENT);
      }
      STAMP(4);
    }
    const int lane = lane_id(), hh = lane >> 5, g16 = lane >> 4, q4 = (lane & 15) >> 2, p4 = lane & 3;
    const int kh = wave >> 1, mh = wave & 1;
    u32x4 hf[43];
    const u32 hso = W_H + ((2 * rt + mh) * 86 + 43 * kh) * 1024, hvo = lane * 16;
    f32x16 o2[2];
    sfor<2>([&](auto n) AI { sfor<16>([&](auto r) AI { o2[n][(int)r] = 0.f; }); pin(o2[n]); });
    u32 db_[2][3];
    sfor<2>([&](auto n) AI {
      const u32 da = kh * 8192 + (4 * hh + q4) * 128 + ((4 * ((int)n ^ (q4 >> 1)) + 2 * (g16 & 1) + (p4 >> 1)) << 4) + 8 * (p4 & 1);
      sfor<3>([&](auto w) AI { db_[n][w] = da + w * 61440; pin(db_[n][w]); });
    });
    bf16x8 df[3][2];
    u32 fl;
    auto ldd = [&](auto k) AI {
      constexpr int c = k >> 2, ks = k & 3;
      constexpr u32 wb = (c < 7 ? 16384u * c : c == 7 ? CONV : c == 8 ? CONV + CH_SZ : 16384u * (c - 9)) + ks * 2048;
      constexpr int w = wb / 61440 > 2 ? 2 : wb / 61440;
      sfor<2>([&](auto n) AI { const u32 a = db_[n][w] + (wb - w * 61440); df[k % 3][n] = cat(ldtr(a), ldtr(a + 1024)); });
    };
    if (wave == 0) { poll_flags<1>(ws, W_HF + (rt >> 1) * 16 * 64, 15, epoch, lane); lds_set(CVP + 6, 1); }
    else wait_byte<WSL>(CVP + 6, 1);
    __builtin_amdgcn_s_setprio(2);
    STAMP(5);
    auto hload = [&](auto k) AI { bld4_sc1<(k & 3) * 1024>(hf[k], hvo, rws, hso + (k >> 2) * 4096); };
    sfor<PD>([&](auto k) AI { hload(k); });
    wait_min4<0>(WDRDY, 1);
    STAMP(6);
    ldd(IC<0>{}); ldd(IC<1>{});
    GAP();
    sfor<43>([&](auto k) AI {
      if constexpr (k == 10) lds_set(FREE + wave, 10);
      if constexpr (k == 18) lds_set(FREE + wave, 11);
      if constexpr (k == 20 || k == 32) fl = lds_ld1(WDRDY);
      if constexpr (k == 40) fl = lds_ld1(FREE);
      if constexpr (k == 22 || k == 34) { STAMP(k == 22 ? 12 : 14); if (__builtin_expect(!all_ge(rfl(fl), k == 22 ? 2u : 3u), 0)) wait_min4<0>(WDRDY, k == 22 ? 2 : 3); STAMP(k == 22 ? 13 : 15); GAP(); }
      constexpr int younger = (k + PD <= 43 ? PD : 43 - k) - 1;
      waitv_pin<younger>(hf[k]);
      if constexpr (k == 0) STAMP(24);
      if constexpr (k == 8) STAMP(25);
      if constexpr (k == 16) STAMP(26);
      o2[0] = __builtin_amdgcn_mfma_f32_32x32x16_bf16(df[k % 3][0], __builtin_bit_cast(bf16x8, hf[k]), o2[0], 0, 0, 0);
      if constexpr (k + 2 < 43) ldd(IC<k + 2>{});
      GAP();
      o2[1] = __builtin_amdgcn_mfma_f32_32x32x16_bf16(df[k % 3][1], __builtin_bit_cast(bf16x8, hf[k]), o2[1], 0, 0, 0);
      if constexpr (k + PD < 43) hload(IC<k + PD>{});
      GAP();
    });
    STAMP(7);
    if (!all_ge(rfl(fl), 11u)) wait_min4<0>(FREE, 11);
    sfor<2>([&](auto n) AI { sfor<4>([&](auto g) AI {
      f32x4 v = {o2[n][4 * g], o2[n][4 * g + 1], o2[n][4 * g + 2], o2[n][4 * g + 3]};
      ldsw<f32x4>(PART + wave * 8192 + n * 4096 + g * 1024 + lane * 16, v);
    }); });
    waitl();
    lds_set(FREE + wave, 12);
    STAMP(8);
    DONE();
  } else {
    __builtin_amdgcn_s_setprio(PRL);
    const rsrc_t rg = mk_rsrc(mkp(ka[2], ka[3]), D * F * 2), ru = mk_rsrc(mkp(ka[4], ka[5]), D * F * 2);
    const int crot = ROT * ((rb + cg) & 1);
    auto chx = [&](int c, u32 sb) AI { dma4(sb + 4 * j * 1024, xv[0], xv[1], xv[2], xv[3], rq, (rb * 128 + 32 * j) * 1024 + (c ^ crot) * 128); };
    auto chw = [&](int c, u32 sb) AI {
      dma3(sb + G_OFF + 3 * j * 1024, wv[0], wv[1], wv[2], rg, (c ^ crot) * 64 * F * 2);
      dma3(sb + U_OFF + 3 * j * 1024, wv[0], wv[1], wv[2], ru, (c ^ crot) * 64 * F * 2);
    };
    if (has) {
      chx(0, 0); chw(0, 0); chx(1, SLOT); chw(1, SLOT);
      int ic = 2, lc = 0;
      u32 sb = 2 * SLOT;
#pragma unroll 1
      for (u32 it = 0; it < SPIN_CAP * 16; ++it) {
        if (lc < ic && vm_pending() <= 10u * (u32)(ic - lc - 1)) {
          asm volatile("" ::: "memory");
          ++lc;
          lds_set(FULL + j, lc == 8 ? 9 : lc);
          if (lc == 1) STAMP(1);
          if (lc == 8) break;
        } else if (ic < 8 && all_ge(rfl(lds_ld1(FREE)), ic - 2)) {
          asm volatile("" ::: "memory");
          chx(ic, sb);
          if (lc < ic && vm_pending() <= 10u * (u32)(ic - lc - 1) + 4u) {
            lds_set(FULL + j, ++lc);
          }
          chw(ic, sb);
          ++ic;
          sb = sb == 2 * SLOT ? 0 : sb + SLOT;
        } else {
          __builtin_amdgcn_s_sleep(LSLEEP);
        }
      }
      waitv<0>();
    } else if (csw) {
      sfor<8>([&](auto c) AI {
        if constexpr (c >= 5) { waitv<24>(); lds_set(FULL + j, (6u * c - vm_pending()) * 43u >> 8); wait_min4<1>(FREE, c - 4); }
        chw(c, (c % 5) * CSLOT - G_OFF);
        if constexpr (c >= 2) lds_set(FULL + j, (6u * (c + 1) - vm_pending()) * 43u >> 8);
      });
      waitv<12>(); lds_set(FULL + j, 6);
      waitv<6>(); lds_set(FULL + j, 7);
      waitv<0>(); lds_set(FULL + j, 8);
    }
    STAMP(2);
    const rsrc_t rd = mk_rsrc(mkp(kb[0], kb[1]), F * D * 2);
    u32 dv[4];
    sfor<4>([&](auto t) AI { dv[t] = (7 * t + l8) * 1024 + ((c8 ^ (((l8 >> 1) & 1) << 2)) << 4) + ct * 128; });
    auto wso = [&](int g) AI { return (u32)(((g >> 1) & 1) * 688 + (g >> 2) * 64 + (g & 1) * 32) * 1024u; };
    auto wd_groups = [&](int lo, int hi) AI {
#pragma unroll 1
      for (int g = lo + j; g < hi; g += 4) { dma4(g * 4096, dv[0], dv[1], dv[2], dv[3], rd, wso(g)); waitv<WDFL>(); }
    };
    u32x4 wr[5][4];
    auto wst = [&](auto t, u32 a) AI { sfor<4>([&](auto i) AI { pin(wr[t][i]); ldsw<u32x4>(a + j * 4096 + i * 1024 + lane * 16, wr[t][i]); }); };
    auto wrl = [&]() AI { sfor<5>([&](auto t) AI { const u32 so = wso(24 + 4 * t + j); sfor<4>([&](auto i) AI { bld4o<i * 1024>(wr[t][i], dv[i], rd, so); }); waitv<WRFL>(); }); };
    wait_min4<1>(FREE, WD1);
    wd_groups(21, 24);
    wait_min4<1>(FREE, WD2);
    wd_groups(0, 10);
    wait_min4<1>(FREE, WD3);
    wd_groups(10, 21);
    waitv<0>();
    lds_set(WDRDY + j, 1);
    wait_min4<1>(FREE, WRLAST);
    wrl();
    waitv<0>();
    lds_set(WDRDY + j, 1);
    STAMP(3);
    wait_min4<4>(CVP, 5);
    wst(IC<0>{}, XCH); wst(IC<1>{}, CONV); wst(IC<2>{}, CONV + CH_SZ);
    waitl();
    lds_set(WDRDY + j, 2);
    STAMP(4);
    wait_min4<2>(FREE, 10);
    wst(IC<3>{}, 0); wst(IC<4>{}, 16384);
    waitl();
    lds_set(WDRDY + j, 3);
    STAMP(5);
    DONE();
  }
}

extern "C" void kernel_launch(void* const* d_in, const int* in_sizes, int n_in, void* d_out, int out_size, void* d_ws, size_t ws_size,
                              hipStream_t stream) {
  Args a;
  a.q = (const u16*)d_in[0]; a.k = (const u16*)d_in[1]; a.v = (const u16*)d_in[2]; a.sk = (const u16*)d_in[3];
  a.h = (const u16*)d_in[4]; a.dv = (const u16*)d_in[5];
  a.wg = (const u16*)d_in[7]; a.wu = (const u16*)d_in[8]; a.wd = (const u16*)d_in[9];
  a.out = (u16*)d_out; a.ws = (char*)d_ws; a.pad = 0;
  (void)hipFuncSetAttribute((const void*)fused, hipFuncAttributeMaxDynamicSharedMemorySize, LDS_BYTES);
  fused<<<256, 768, LDS_BYTES, stream>>>(a);
}
```

```cpp
#define AMPID 3
#define AMPWAVE 0
#define WRLAST 9
#include <hip/hip_runtime.h>
#include <stdint.h>
#include <utility>

typedef unsigned short u16;
typedef unsigned int u32;
typedef unsigned long long u64;
typedef __bf16 bf16x8 __attribute__((ext_vector_type(8)));
typedef __bf16 bf16x2 __attribute__((ext_vector_type(2)));
typedef short s16x4 __attribute__((ext_vector_type(4)));
typedef short s16x8 __attribute__((ext_vector_type(8)));
typedef float f32x16 __attribute__((ext_vector_type(16)));
typedef float f32x4 __attribute__((ext_vector_type(4)));
typedef float f32x2 __attribute__((ext_vector_type(2)));
typedef u32 u32x4 __attribute__((ext_vector_type(4)));
typedef u32 u32x2 __attribute__((ext_vector_type(2)));
typedef __amdgpu_buffer_rsrc_t rsrc_t;
#define DI __device__ __forceinline__
#define AI __attribute__((always_inline))
#define LAS __attribute__((address_space(3)))
#define GAS __attribute__((address_space(1)))
#define RLX_AGENT __ATOMIC_RELAXED, __HIP_MEMORY_SCOPE_AGENT
#define RLX_WG __ATOMIC_RELAXED, __HIP_MEMORY_SCOPE_WORKGROUP

constexpr int S = 2048, D = 512, F = 1376;
#define UPAD 32
#define SLOTB (40960 + UPAD)
constexpr u32 SLOT = SLOTB, G_OFF = 16384, U_OFF = 16384 + 12288 + UPAD;
constexpr u32 CSLOT = 2 * (12288 + UPAD) + 64 - 2 * UPAD % 64;
constexpr u32 CONV = 3 * 41216;
constexpr u32 CH_SZ = 17520, HA = 64, HB = 4292, KA = 8400, KB = 12976, XB = 4304;
constexpr u32 MISC = CONV + 2 * CH_SZ;
constexpr u32 CS = MISC, ST = MISC + 1536, CNT = MISC + 2560;
constexpr u32 CKV = CNT + 8, CSP = CNT + 12, EPO = CNT + 16, CVP = CNT + 24, FULL = CNT + 32, FREE = CNT + 36, STRDY = CNT + 40, WDRDY = CNT + 44;
constexpr u32 LDS_BYTES = MISC + 2816;
static_assert(LDS_BYTES <= 163840, "LDS");
constexpr u32 XCH = 98304, ZST = 114688, PART = 32768;
constexpr u32 W_E = 0, W_PF = 4096, W_HF = W_PF + 16384, W_ZF = W_HF + 16384, W_SG = W_ZF + 16384, W_CG = W_SG + 16384;
constexpr u32 W_KV = 131072, W_ZT = W_KV + 2097152 + 4096, W_H = W_ZT + 4194304, W_END = W_H + 64 * 86 * 1024;
constexpr u32 W_DBG = 16u << 20;
constexpr u32 CODE_BYTES = 26112;
#define PRL 1
#define PRS 0
#define CONVAFTER 9
#define LSLEEP 1
#define PRA 0
#define ROT 0
#define S2AFTER 8
#define WDFL 2
#define KVSTAG 0
#define SSTOP 9
#define POLLS 4
#define PD 10
#define WD1 6
#define WD2 7
#define PFLO 8
#define PFPRIO 0
#define PFWAIT 0
#define KVMODE 0
#define WD3 8
#define PRE 2
#define PRW 0
#define WSL 1
#define PRC 3
#define ZSLEEP 4
#define ZAFTER 11
#define WRFL 40
#define KVAFTER 1
constexpr u32 SPIN_CAP = 1u << 18;

constexpr int cpair(int g) {
  int i = 0;
  for (int n = 0; n < 8; ++n) for (int m = 0; m <= n; ++m) if (m + n <= 7) {
    if (i++ == g) return n * 8 + m;
    if (m < n && i++ == g) return m * 8 + n;
  }
  return 0;
}
template <class Fn, int... I> DI void sfor_impl(Fn&& f, std::integer_sequence<int, I...>) { (f(std::integral_constant<int, I>{}), ...); }
template <int N, class Fn> DI void sfor(Fn&& f) { sfor_impl(f, std::make_integer_sequence<int, N>{}); }

DI rsrc_t mk_rsrc(const void* p, u32 bytes) { return __builtin_amdgcn_make_buffer_rsrc((void*)p, (short)0, (int)bytes, 0x00020000); }
DI void dma(u32 lds, u32 voff, rsrc_t r, u32 soff) {
  asm volatile("s_mov_b32 m0, %0\n\ts_nop 0\n\tbuffer_load_dwordx4 %1, %2, %3 offen lds" ::"s"(lds), "v"(voff), "s"(r), "s"(soff) : "memory");
}

DI void dma4(u32 lds, u32 v0, u32 v1, u32 v2, u32 v3, rsrc_t r, u32 soff) {
  asm volatile("s_mov_b32 m0, %0\n\ts_nop 0\n\tbuffer_load_dwordx4 %1, %5, %6 offen lds\n\tbuffer_load_dwordx4 %2, %5, %6 offen offset:1024 lds\n\t"
               "buffer_load_dwordx4 %3, %5, %6 offen offset:2048 lds\n\tbuffer_load_dwordx4 %4, %5, %6 offen offset:3072 lds"
               ::"s"(lds), "v"(v0), "v"(v1), "v"(v2), "v"(v3), "s"(r), "s"(soff) : "memory");
}
DI void dma3(u32 lds, u32 v0, u32 v1, u32 v2, rsrc_t r, u32 soff) {
  asm volatile("s_mov_b32 m0, %0\n\ts_nop 0\n\tbuffer_load_dwordx4 %1, %4, %5 offen lds\n\tbuffer_load_dwordx4 %2, %4, %5 offen offset:1024 lds\n\t"
               "buffer_load_dwordx4 %3, %4, %5 offen offset:2048 lds"
               ::"s"(lds), "v"(v0), "v"(v1), "v"(v2), "s"(r), "s"(soff) : "memory");
}
template <int OFF> DI void bld4_sc1(u32x4& d, u32 voff, rsrc_t r, u32 soff) {
  asm volatile("buffer_load_dwordx4 %0, %1, %2, %3 offen offset:%4 sc1" : "=v"(d) : "v"(voff), "s"(r), "s"(soff), "i"(OFF));
}
template <int OFF> DI void bld4o(u32x4& d, u32 voff, rsrc_t r, u32 soff) { asm volatile("buffer_load_dwordx4 %0, %1, %2, %3 offen offset:%4" : "=v"(d) : "v"(voff), "s"(r), "s"(soff), "i"(OFF)); }
DI void bld4(u32x4& d, u32 voff, rsrc_t r, u32 soff) { asm volatile("buffer_load_dwordx4 %0, %1, %2, %3 offen" : "=v"(d) : "v"(voff), "s"(r), "s"(soff)); }
template <int OFF> DI void bld2(u32x2& d, u32 voff, rsrc_t r) { asm volatile("buffer_load_dwordx2 %0, %1, %2, 0 offen offset:%3" : "=v"(d) : "v"(voff), "s"(r), "i"(OFF)); }
template <int OFF> DI void gld4_sc1(u32x4& d, u32 voff, const char* base) {
  asm volatile("global_load_dwordx4 %0, %1, %2 offset:%3 sc1" : "=v"(d) : "v"(voff), "s"(base), "i"(OFF));
}
template <int N> DI void waitv_pin(u32x4& d) { asm volatile("s_waitcnt vmcnt(%1)" : "+v"(d) : "i"(N)); }
template <int N> DI void waitv() { asm volatile("s_waitcnt vmcnt(%0)" ::"i"(N) : "memory"); }
DI u32 vm_pending() { u32 r; asm volatile("s_getreg_b32 %0, hwreg(HW_REG_IB_STS)" : "=s"(r)::"memory"); return (r & 15u) | ((r >> 18) & 48u); }
DI void bar() { asm volatile("s_waitcnt lgkmcnt(0)\n\ts_barrier" ::: "memory"); }
DI void waitl() { asm volatile("s_waitcnt lgkmcnt(0)" ::: "memory"); }
DI float bflo(u32 x) { return __uint_as_float(x << 16); }
DI float bfhi(u32 x) { return __uint_as_float(x & 0xffff0000u); }
DI u32 pk2(float lo, float hi) { f32x2 v = {lo, hi}; return __builtin_bit_cast(u32, __builtin_convertvector(v, bf16x2)); }
DI u32 pk2s(float lo, float hi) { asm("" : "+v"(lo)); asm("" : "+v"(hi)); return pk2(lo, hi); }
DI float rbf(float x) { return bflo(pk2(x, x)); }
template <class T> DI T ldsr(u32 a) { return *(const LAS T*)a; }
template <class T> DI void ldsw(u32 a, T v) { *(LAS T*)a = v; }
DI u32x2 ldtr(u32 a) { return __builtin_bit_cast(u32x2, __builtin_amdgcn_ds_read_tr16_b64_v4i16((LAS s16x4*)a)); }
DI bf16x8 cat(u32x2 a, u32x2 b) { u32x4 v = {a.x, a.y, b.x, b.y}; return __builtin_bit_cast(bf16x8, v); }
template <int CTRL, int RM> DI float dpp_add(float v) {
  return v + __builtin_bit_cast(float, __builtin_amdgcn_update_dpp(0, __builtin_bit_cast(int, v), CTRL, RM, 0xf, false));
}
DI float wave_sum(float v) {
  v = dpp_add<0x111, 0xf>(v); v = dpp_add<0x112, 0xf>(v); v = dpp_add<0x114, 0xf>(v); v = dpp_add<0x118, 0xf>(v);
  v = dpp_add<0x142, 0xa>(v); v = dpp_add<0x143, 0xc>(v);
  return __builtin_bit_cast(float, __builtin_amdgcn_readlane(__builtin_bit_cast(int, v), 63));
}
DI void wave_sum2(float& a, float& c) {
  a = dpp_add<0x111, 0xf>(a); c = dpp_add<0x111, 0xf>(c); a = dpp_add<0x112, 0xf>(a); c = dpp_add<0x112, 0xf>(c);
  a = dpp_add<0x114, 0xf>(a); c = dpp_add<0x114, 0xf>(c); a = dpp_add<0x118, 0xf>(a); c = dpp_add<0x118, 0xf>(c);
  a = dpp_add<0x142, 0xa>(a); c = dpp_add<0x142, 0xa>(c); a = dpp_add<0x143, 0xc>(a); c = dpp_add<0x143, 0xc>(c);
  a = __builtin_bit_cast(float, __builtin_amdgcn_readlane(__builtin_bit_cast(int, a), 63));
  c = __builtin_bit_cast(float, __builtin_amdgcn_readlane(__builtin_bit_cast(int, c), 63));
}
DI void gld8_sc1(u64& d, const char* p) { asm volatile("global_load_dwordx2 %0, %1, off sc1" : "=v"(d) : "v"(p)); }
DI void gld_u16(u32& d, u32 voff, const char* base) { asm volatile("global_load_ushort %0, %1, %2" : "=v"(d) : "v"(voff), "s"(base)); }
template <int SLEEP> DI void poll_flags(const char* ws, u32 off, int n, u64 epoch, int lane) {
  const GAS u64* p = (const GAS u64*)(ws + off + (u32)(lane < n ? lane : 0) * 64u);
#pragma unroll 1
  for (u32 it = 0; it < SPIN_CAP; ++it) {
    u64 v = __hip_atomic_load(p, RLX_AGENT);
    if (__all(v == epoch)) break;
    __builtin_amdgcn_s_sleep(SLEEP);
  }
  asm volatile("" ::: "memory");
}
DI u32 poll_granule(const char* ws, u32 row, u32 tag) {
  const GAS u64* p = (const GAS u64*)(ws + W_SG + row * 8u);
  u64 v = 0;
#pragma unroll 1
  for (u32 it = 0; it < SPIN_CAP; ++it) {
    v = __hip_atomic_load(p, RLX_AGENT);
    if (__all((u32)(v >> 32) == tag)) break;
    __builtin_amdgcn_s_sleep(1);
  }
  return (u32)v;
}
DI u32 lds_arrive(u32 a, int lane) {
  u32 r = 0;
  waitl();
  if (lane == 0) r = __hip_atomic_fetch_add((LAS u32*)a, 1u, RLX_WG);
  return (u32)__builtin_amdgcn_readfirstlane(r) + 1u;
}
DI void lds_wait(u32 a, u32 want) {
#pragma unroll 1
  for (u32 it = 0; it < SPIN_CAP * 16; ++it) {
    u32 v = __hip_atomic_load((LAS u32*)a, RLX_WG);
    if ((u32)__builtin_amdgcn_readfirstlane(v) >= want) break;
  }
  asm volatile("" ::: "memory");
}

DI int lane_id() { int l; asm volatile("v_mbcnt_lo_u32_b32 %0, -1, 0\n\tv_mbcnt_hi_u32_b32 %0, -1, %0" : "=v"(l)); return l; }
DI u32 rfl(u32 x) { return (u32)__builtin_amdgcn_readfirstlane(x); }
DI u32x2 lds_ld2(u32 a) { return __builtin_bit_cast(u32x2, __hip_atomic_load((LAS u64*)a, RLX_WG)); }
template <int SLEEP> DI void wait_min2(u32 a, u32 want) {
#pragma unroll 1
  for (u32 it = 0; it < SPIN_CAP * 16; ++it) {
    const u32x2 v = lds_ld2(a);
    if (rfl(min(v.x, v.y)) >= want) break;
    if (SLEEP) __builtin_amdgcn_s_sleep(SLEEP);
  }
  asm volatile("" ::: "memory");
}
DI u32 lds_ld1(u32 a) { return __hip_atomic_load((LAS u32*)a, RLX_WG); }
DI bool all_ge(u32 w, u32 want) { return ((w - want * 0x01010101u) & 0x80808080u) == 0u; }
template <int SLEEP> DI void wait_min4(u32 a, u32 want) {
#pragma unroll 1
  for (u32 it = 0; it < SPIN_CAP * 16; ++it) {
    if (all_ge(rfl(lds_ld1(a)), want)) break;
    if (SLEEP) __builtin_amdgcn_s_sleep(SLEEP);
  }
  asm volatile("" ::: "memory");
}
template <int SLEEP> DI void wait_byte(u32 a, u32 want) {
#pragma unroll 1
  for (u32 it = 0; it < SPIN_CAP * 16; ++it) {
    if (rfl(__hip_atomic_load((LAS unsigned char*)a, RLX_WG)) >= want) break;
    if (SLEEP) __builtin_amdgcn_s_sleep(SLEEP);
  }
  asm volatile("" ::: "memory");
}
DI u64 wait_epoch() {
  u32x2 e;
#pragma unroll 1
  for (u32 it = 0; it < SPIN_CAP * 16; ++it) {
    e = lds_ld2(EPO);
    if (rfl(e.x) != 0u) break;
    __builtin_amdgcn_s_sleep(2);
  }
  return ((u64)rfl(e.y) << 32) | rfl(e.x);
}
DI u64 gld_agent(const char* p) { return __hip_atomic_load((const GAS u64*)p, RLX_AGENT); }
DI void lds_set(u32 a, u32 v) { __hip_atomic_store((LAS unsigned char*)a, (unsigned char)v, RLX_WG); }
DI void gather(u32& junk, u32 voff, rsrc_t r, u32 soff) { asm volatile("buffer_load_dword %0, %1, %2, %3 offen" : "=v"(junk) : "v"(voff), "s"(r), "s"(soff)); }
template <int N> using IC = std::integral_constant<int, N>;

#define DONE() do { if (wave >= 8) { const u64 d_ = rfl(ldsr<u32>(CNT + 60)); while (__builtin_amdgcn_s_memrealtime() - tstart_ < 40 * d_) __builtin_amdgcn_s_sleep(1); } } while (0)
#define STAMP(id) do { if ((id) == AMPID && wave == AMPWAVE) ldsw<u32>(CNT + 60, (u32)(__builtin_amdgcn_s_memrealtime() - tstart_)); } while (0)

struct Args { const u16 *q, *wg, *wu, *k, *v, *sk, *h, *dv, *wd; u16* out; char* ws; u64 pad; };
typedef u32 u32x16 __attribute__((ext_vector_type(16)));
typedef u32 u32x8 __attribute__((ext_vector_type(8)));
DI char* mkp(u32 lo, u32 hi) { return (char*)(((u64)hi << 32) | lo); }
#define GAP() __builtin_amdgcn_sched_barrier(0)
DI void barx() { asm volatile("s_barrier" ::: "memory"); }
template <class T> DI void spin(T& x) { asm volatile("" : "+s"(x)); }
template <class T> DI void pin(T& x) { asm volatile("" : "+v"(x)); }

__global__ void __launch_bounds__(768, 3) fused(Args) {
  u64 pc0; asm volatile("s_getpc_b64 %0" : "=s"(pc0));
  const u64 tstart_ = __builtin_amdgcn_s_memrealtime();
  asm volatile("s_waitcnt lgkmcnt(0)");
  u32x16 ka; u32x8 kb;
  asm volatile("s_load_dwordx16 %0, %2, 0x0\n\ts_load_dwordx8 %1, %2, 0x40" : "=&s"(ka), "=&s"(kb) : "s"(__builtin_amdgcn_kernarg_segment_ptr()));
  GAP();
  const int tid = threadIdx.x, lane = tid & 63;
  const int wave = __builtin_amdgcn_readfirstlane(tid >> 6);
  const int b = blockIdx.x, xcd = b & 7, li = b >> 3;
  if (tid < 16) ldsw<u32>(CNT + tid * 4, 0u);
  const bool has = li < 30;
  const int id = 30 * xcd + (has ? li : 0), rr_ = id % 120, rb = 8 * (id / 120) + (rr_ & 7);
  const int cg = has ? rr_ >> 3 : 4 * (xcd & 3) + 2 * (xcd >> 2) + (li - 30);
  const bool csw = !has && cg < 15;
  const int rt = 4 * xcd + (li & 3), ct = li >> 2;
  const int j = wave & 3, l8 = lane >> 3, c8 = lane & 7;
  u32 xv[4], wv[3];
  sfor<4>([&](auto t) AI { xv[t] = (7 * t + l8) * 1024 + ((c8 ^ ((4 * t + (l8 >> 1)) & 7)) << 4); pin(xv[t]); });
  sfor<3>([&](auto t) AI {
    const u32 byte = t * 1024 + lane * 16, row = byte / 192u;
    const u32 seg = byte - row * 192u;
    wv[t] = cg * 192 + seg >= F * 2 ? 0x40000000u : (16 * j + row) * (F * 2) + cg * 192 + seg - t * 1024;
    pin(wv[t]);
  });
  asm volatile("s_waitcnt lgkmcnt(0)\n\ts_barrier" : "+s"(ka), "+s"(kb)::"memory");
  STAMP(9);
  char* const ws = mkp(kb[4], kb[5]);
  const rsrc_t rq = mk_rsrc(mkp(ka[0], ka[1]), S * D * 2);
  if (wave >= 8) {
    __builtin_amdgcn_s_setprio(3);
    u64 epoch;
    asm volatile("s_load_dwordx2 %0, %1, 0x0" : "=s"(epoch) : "s"(ws));
    const int lane = lane_id(), l8 = lane >> 3, c8 = lane & 7;
    const int ch = j & 1, cp = j >> 1, c16 = lane & 15, q = lane >> 4;
    const rsrc_t rws = mk_rsrc(ws, W_END);
    const u32 cb = CONV + ch * CH_SZ, hown = cb + (cp ? XB : HA), kown = cb + (cp ? KB : KA);
    const int srow = 256 * xcd + 8 * li + 2 * j;
    if (SSTOP == 0) wait_min4<8>(FREE, 8);
    u32x4 sx[2];
    sfor<2>([&](auto r) AI { bld4(sx[r], lane * 16, rq, (srow + r) * 1024); });
    STAMP(23);
    const int pa = b & 31, pg = b >> 5, s0 = 64 * pa + 16 * j;
    u32x2 skw[2];
    u32 dvv;
    auto kvload = [&]() AI {
      const rsrc_t rk = mk_rsrc(mkp(ka[6], ka[7]), S * D * 2), rv = mk_rsrc(mkp(ka[8], ka[9]), S * D * 2), rs_ = mk_rsrc(mkp(ka[10], ka[11]), D * 8);
      const u32 kvv = l8 * 1024 + c8 * 16 + pg * 128;
      sfor<3>([&](auto t) AI { const int r = s0 - 8 + 8 * t; dma(hown + t * 1024, kvv, rk, (r < 0 ? 0 : r) * 1024); });
      sfor<2>([&](auto t) AI { dma(kown + t * 1024, kvv, rv, (s0 + 8 * t) * 1024); });
      sfor<2>([&](auto t) AI { bld2<256 * t>(skw[t], (64 * pg + 16 * (q >> 1) + c16) * 8, rs_); });
      gld_u16(dvv, (2 * b + ch) * 2, mkp(ka[14], ka[15]));
    };
    asm volatile("s_waitcnt lgkmcnt(0)" : "+s"(epoch));
    epoch += 0x9E3779B97F4A7C15ull;
    epoch = (epoch ^ (epoch >> 30)) * 0xBF58476D1CE4E5B9ull; epoch = (epoch ^ (epoch >> 27)) * 0x94D049BB133111EBull;
    epoch = (epoch ^ (epoch >> 31)) | 1ull;
    ldsw<u64>(EPO, epoch);
    STAMP(18);
    const u32 tag = (u32)(epoch >> 17);
    if (has) wait_min4<2>(FULL, KVAFTER + KVSTAG * (li & 3));
    STAMP(19);
    kvload();
    STAMP(20);
    asm volatile("s_waitcnt vmcnt(8)" : "+v"(sx[0]), "+v"(sx[1]));
    STAMP(21);
    __builtin_amdgcn_s_setprio(0);
    {
      float x[2][8], mean[2], var[2];
      sfor<2>([&](auto r) AI {
        sfor<4>([&](auto i) AI { x[r][2 * i] = bflo(sx[r][(int)i]); x[r][2 * i + 1] = bfhi(sx[r][(int)i]); });
        mean[r] = x[r][0];
        sfor<7>([&](auto i) AI { mean[r] += x[r][i + 1]; });
      });
      wave_sum2(mean[0], mean[1]);
      sfor<2>([&](auto r) AI {
        mean[r] *= 1.0f / D; var[r] = 0.f;
        sfor<8>([&](auto i) AI { float d = x[r][i] - mean[r]; asm("" : "+v"(d)); var[r] += d * d; });
      });
      wave_sum2(var[0], var[1]);
      STAMP(22);
      const float mn = lane ? mean[1] : mean[0], vr = rbf((lane ? var[1] : var[0]) * (1.0f / D));
      const u32 g = pk2s(mn, __builtin_sqrtf(rbf(vr + 1e-5f)));
      if (lane < 2) __hip_atomic_store((GAS u64*)(ws + W_SG + (srow + lane) * 8), ((u64)tag << 32) | g, RLX_AGENT);
    }
    STAMP(1);
    asm volatile("s_waitcnt vmcnt(1)" : "+v"(skw[0]), "+v"(skw[1]), "+v"(dvv)::"memory");
    STAMP(2);
    {
      const bool first = s0 == 0 && (q & 1) == 0;
      u32x2 kr[2][5];
      sfor<2>([&](auto gi) AI {
        const u32 a = (8 * (q & 1) + (c16 >> 2)) * 128 + 64 * gi + 32 * (q >> 1) + 8 * (lane & 3);
        kr[gi][0] = ldtr(hown + a + 512); kr[gi][1] = ldtr(hown + a + 1024); kr[gi][2] = ldtr(hown + a + 1536); kr[gi][3] = ldtr(kown + a); kr[gi][4] = ldtr(kown + a + 512);
      });
      GAP();
      if (SSTOP == 1) wait_min4<8>(FREE, 8);
      sfor<2>([&](auto gi) AI {
        const float w0 = bflo(skw[gi].x), w1 = bfhi(skw[gi].x), w2 = bflo(skw[gi].y), w3 = bfhi(skw[gi].y);
        const u32x2 k0 = kr[gi][0], k1 = kr[gi][1], k2 = kr[gi][2], v0 = kr[gi][3], v1 = kr[gi][4];
        const float kk[11] = {first ? 0.f : bfhi(k0.x), first ? 0.f : bflo(k0.y), first ? 0.f : bfhi(k0.y), bflo(k1.x), bfhi(k1.x), bflo(k1.y), bfhi(k1.y), bflo(k2.x), bfhi(k2.x), bflo(k2.y), bfhi(k2.y)};
        const float vv[8] = {bflo(v0.x), bfhi(v0.x), bflo(v0.y), bfhi(v0.y), bflo(v1.x), bfhi(v1.x), bflo(v1.y), bfhi(v1.y)};
        u32 a4[4], m4[4];
        sfor<4>([&](auto t) AI { a4[t] = pk2s(w3 * kk[2 * t], w3 * kk[2 * t + 1]); m4[t] = pk2s(w2 * kk[2 * t + 1], w2 * kk[2 * t + 2]); });
        GAP();
        sfor<4>([&](auto t) AI { a4[t] = pk2s(bflo(a4[t]) + bflo(m4[t]), bfhi(a4[t]) + bfhi(m4[t])); m4[t] = pk2s(w1 * kk[2 * t + 2], w1 * kk[2 * t + 3]); });
        GAP();
        sfor<4>([&](auto t) AI { a4[t] = pk2s(bflo(a4[t]) + bflo(m4[t]), bfhi(a4[t]) + bfhi(m4[t])); m4[t] = pk2s(w0 * kk[2 * t + 3], w0 * kk[2 * t + 4]); });
        GAP();
        sfor<4>([&](auto t) AI { a4[t] = pk2s(bflo(a4[t]) + bflo(m4[t]), bfhi(a4[t]) + bfhi(m4[t])); });
        GAP();
        u32x4 ov;
        sfor<4>([&](auto t) AI { ov[3 - t] = pk2s(bfhi(a4[t]) * vv[2 * t + 1], bflo(a4[t]) * vv[2 * t]); });
        const int dg = 2 * gi + (q >> 1);
        ldsw<u32x4>(CONV + (dg & 1) * CH_SZ + (dg >> 1 ? KB : KA) + 2048 + c16 * 128 + (((7 - 2 * j - (q & 1)) ^ (c16 & 7)) << 4), ov);
      });
    }
    waitl();
    lds_set(CVP + j, 1);
    __builtin_amdgcn_s_setprio(3);
    STAMP(17);
    wait_min4<0>(CVP, 1);
    {
      u32x4 o[2];
      sfor<2>([&](auto t) AI { o[t] = ldsr<u32x4>(kown + 2048 + (8 * t + l8) * 128 + ((c8 ^ l8) << 4)); });
      sfor<2>([&](auto t) AI { __builtin_amdgcn_raw_buffer_store_b128(o[t], rws, W_KV + (64 * pg + 16 * j + 8 * t + l8) * 4096 + (1984 - 64 * pa) * 2 + c8 * 16, 0, 16); });
    }
    const int mh = j & 1, db = j >> 1, s31 = lane & 31, hh = lane >> 5;
    const int orow = 64 * rt + 32 * mh + s31, ocol = 64 * ct + 32 * db;
    u32x2 xr[4];
    u32 sg;
    {
      const char* p1 = ws + W_SG + (hh ? orow : rb * 128 + 32 * j + s31) * 8;
      const char* p2 = ws + W_CG + ((has ? cg : 0) * 192 + 48 * j + (lane < 48 ? lane : 0)) * 8;
      u64 g1, g2;
      gld8_sc1(g1, p1); gld8_sc1(g2, p2);
      asm volatile("s_waitcnt vmcnt(0)" : "+v"(g1), "+v"(g2)::"memory");
      if (lds_arrive(CKV, lane) == 4 && lane == 0) __hip_atomic_store((GAS u64*)(ws + W_PF + b * 64), epoch, RLX_AGENT);
      STAMP(3);
#pragma unroll 1
      for (u32 it = 0; it < SPIN_CAP; ++it) {
        if (__all((u32)(g1 >> 32) == tag && (u32)(g2 >> 32) == tag)) break;
        __builtin_amdgcn_s_sleep(4);
        g1 = gld_agent(p1); g2 = gld_agent(p2);
      }
      sg = (u32)g1;
      if (has) {
        if (lane < 32) ldsw<f32x2>(ST + (32 * j + lane) * 8, f32x2{bflo(sg), __builtin_amdgcn_rcpf(bfhi(sg))});
        if (lane < 48) ldsw<u32>(CS + (48 * j + lane) * 4, (u32)g2);
      }
      lds_set(STRDY + j, 1);
      STAMP(4);
    }
    if (has) wait_min4<4>(FULL, S2AFTER);
    STAMP(24);
    {
      u64 f;
      gld8_sc1(f, ws + W_PF + ((b >> 5) * 32 + (j == 0 ? s31 : 0)) * 64);
      dma4(cb + (cp ? HB : HA), lane * 16, lane * 16, lane * 16, lane * 16, mk_rsrc(mkp(ka[12], ka[13]), D * S * 2), (2 * b + ch) * 4096);
      if (lane < (cp ? 33 : 16)) ldsw<u32>(cb + (cp ? 4160 : 0) + lane * 4, 0u);
      if (lane < (cp ? 28 : 30)) ldsw<u32x4>(kown + 4096 + lane * 16, u32x4{0u, 0u, 0u, 0u});
      asm volatile("s_waitcnt vmcnt(4)" : "+v"(f));
      sg = (u32)__builtin_amdgcn_permlane32_swap(sg, sg, false, false)[1];
      if (j == 0) { if (!__all(f == epoch)) poll_flags<2>(ws, W_PF + (b >> 5) * 32 * 64, 32, epoch, lane); lds_set(CVP + 4, 1); }
      wait_byte<1>(CVP + 4, 1);
      u32x4 kvr[4];
      const char* src = ws + W_KV + (2 * b + ch) * 4096 + 2 * cp;
      sfor<4>([&](auto t) AI { gld4_sc1<t * 1024>(kvr[t], lane * 16, src); });
      wait_min4<1>(FREE, 6);
      sfor<2>([&](auto t) AI {
        const int r = 16 * t + (lane >> 2);
        dma(ZST + j * 2048 + t * 1024, r * 1024 + (((lane & 3) ^ ((r >> 1) & 3)) << 4), rq, (64 * rt + 32 * mh) * 1024 + ocol * 2);
      });
      asm volatile("s_waitcnt vmcnt(0)" : "+v"(kvr[0]), "+v"(kvr[1]), "+v"(kvr[2]), "+v"(kvr[3])::"memory");
      if (cp && lane == 63) kvr[3].w &= 0xffffu;
      sfor<4>([&](auto t) AI { ldsw<u32x4>(kown + t * 1024 + lane * 16, kvr[t]); });
      sfor<4>([&](auto g) AI { xr[g] = ldsr<u32x2>(ZST + j * 2048 + s31 * 64 + ((g ^ ((s31 >> 1) & 3)) << 4) + 8 * hh); });
      waitl();
      pin(xr[0]); pin(xr[1]); pin(xr[2]); pin(xr[3]);
      lds_set(CVP + j, 2);
      STAMP(5);
    }
    __builtin_amdgcn_s_setprio(0);
    wait_min4<1>(FREE, CONVAFTER);
    wait_byte<0>(CVP + (j ^ 2), 2);
    __builtin_amdgcn_s_setprio(PRC);
    STAMP(6);
    f32x4 dacc[8];
    sfor<8>([&](auto i) AI { dacc[i] = f32x4{0.f, 0.f, 0.f, 0.f}; });
    u32 kv2[8];
    {
      u32 hb = cb + ((c16 & 1) ? HA : HB) + 4 * (c16 - 15) + 16 * q + 256 * cp;
      u32 kb_ = cb + ((c16 & 1) ? KA : KB) + 448 - 64 * (c16 >> 1) + 16 * q + 256 * cp;
      u32 hb2 = hb + 8;
      pin(hb); pin(hb2); pin(kb_);
      bf16x8 ta[12], kf[12];
      auto ld1 = [&](auto odd, auto n, auto w, auto off) AI {
        constexpr int r = (n < 4 && odd) ? 8 + n : n;
        if constexpr (w == 0) kf[r] = ldsr<bf16x8>(kb_ + 512 * (7 - n) + off);
        if constexpr (w == 1) { const u32x2 v = ldsr<u32x2>(hb + 512 * n + off); u32x4 t = __builtin_bit_cast(u32x4, ta[r]); t.x = v.x; t.y = v.y; ta[r] = __builtin_bit_cast(bf16x8, t); }
        if constexpr (w == 2) { const u32x2 v = ldsr<u32x2>(hb2 + 512 * n + off); u32x4 t = __builtin_bit_cast(u32x4, ta[r]); t.z = v.x; t.w = v.y; ta[r] = __builtin_bit_cast(bf16x8, t); }
      };
      auto cstep = [&](auto odd, auto off) AI {
        sfor<36>([&](auto g) AI {
          constexpr int c = cpair(35 - g) >> 3, dl = cpair(35 - g) & 7;
          constexpr int rc = (c < 4 && odd) ? 8 + c : c, rd_ = (dl < 4 && odd) ? 8 + dl : dl;
          dacc[c + dl] = __builtin_amdgcn_mfma_f32_16x16x32_bf16(kf[rc], ta[rd_], dacc[c + dl], 0, 0, 0);
          if constexpr (g < 12) ld1(IC<1 - odd>{}, IC<g / 3>{}, IC<g % 3>{}, off);
          else if constexpr (g < 24) ld1(IC<1 - odd>{}, IC<7 - (g - 12) / 3>{}, IC<g % 3>{}, off);
          GAP();
        });
      };
      sfor<24>([&](auto i) AI { ld1(IC<0>{}, IC<(i < 3 ? 0 : i < 6 ? 7 : i < 9 ? 1 : i < 12 ? 6 : i < 15 ? 2 : i < 18 ? 5 : i < 21 ? 3 : 4)>{}, IC<i % 3>{}, IC<0>{}); GAP(); });
#pragma unroll 1
      for (int i = 0; i < 2; ++i) {
        cstep(IC<0>{}, IC<64>{});
        cstep(IC<1>{}, IC<128>{});
        hb += 128; hb2 += 128; kb_ += 128;
      }
    }
    __builtin_amdgcn_s_setprio(3);
    STAMP(25);
    sfor<4>([&](auto i) AI { ldsw<f32x4>(XCH + j * 4096 + i * 1024 + lane * 16, cp ? dacc[i] : dacc[4 + i]); });
    lds_set(CVP + j, 4);
    {
      const int l = lane_id();
      u32 a = cb + KA + 2 * (2046 - 2 * (l & 15) - 64 * (l >> 4) - 1024 * cp) - 1600;
      pin(a);
      sfor<8>([&](auto i) AI { kv2[i] = ldsr<u32>(a + 64 * (1 - (i & 1)) + 512 * (3 - (i >> 1))); });
    }
    STAMP(7);
    {
      const float dv = bflo(dvv);
      sfor<8>([&](auto i) AI { kv2[i] = pk2s(bfhi(kv2[i]) * dv, bflo(kv2[i]) * dv); pin(kv2[i]); });
      wait_byte<0>(CVP + (j ^ 2), 4);
      f32x4 o[4];
      sfor<4>([&](auto i) AI { o[i] = ldsr<f32x4>(XCH + (j ^ 2) * 4096 + i * 1024 + lane * 16); });
      asm volatile("" ::: "memory");
      lds_set(CVP + j, 5);
      sfor<4>([&](auto i) AI {
        const f32x4 m = cp ? dacc[4 + i] : dacc[i];
        sfor<2>([&](auto rr) AI {
          const u32 yb = pk2s(m[2 * rr] + o[i][2 * rr], m[2 * rr + 1] + o[i][2 * rr + 1]), kd = kv2[2 * i + rr];
          ldsw<u32>(ZST + j * 2048 + 4 * c16 + 128 * q + 64 * rr + 512 * i, pk2s(bflo(yb) + bflo(kd), bfhi(yb) + bfhi(kd)));
        });
      });
      waitl();
      u32x2 zo[4];
      sfor<4>([&](auto t) AI { zo[t] = ldsr<u32x2>(ZST + j * 2048 + t * 512 + lane * 8); });
      sfor<4>([&](auto t) AI {
        __builtin_amdgcn_raw_buffer_store_b128(u32x4{zo[t].x, tag, zo[t].y, tag}, rws, lane * 16 + W_ZT + (2 * b + ch) * 8192 + cp * 4096 + t * 1024, 0, 16);
      });
    }
    STAMP(8);
    const float mean = bflo(sg), rs = __builtin_amdgcn_rcpf(bfhi(sg));
    u32 xn[8];
    sfor<8>([&](auto i) AI {
      const u32 x2 = xr[i >> 1][i & 1], n2 = pk2s(bflo(x2) - mean, bfhi(x2) - mean);
      xn[i] = pk2s(bflo(n2) * rs, bfhi(n2) * rs);
      pin(xn[i]);
    });
    if (b == 0 && j == 3) {
      sfor<4>([&](auto t) AI { poll_flags<1>(ws, W_PF + t * 4096, 64, epoch, lane); });
      if (lane == 0) __hip_atomic_store((GAS u64*)(ws + W_E), epoch, RLX_AGENT);
    }
    wait_min4<2>(FREE, ZAFTER);
    u32x4 zr[4];
#pragma unroll 1
    for (u32 it = 0; it < SPIN_CAP; ++it) {
      sfor<4>([&](auto t) AI { bld4_sc1<0>(zr[t], l8 * 8192 + c8 * 16, rws, W_ZT + (ocol + 8 * t) * 8192 + (64 * rt + 32 * mh) * 4); });
      asm volatile("s_waitcnt vmcnt(0)" : "+v"(zr[0]), "+v"(zr[1]), "+v"(zr[2]), "+v"(zr[3])::"memory");
      u32 bad = 0;
      sfor<4>([&](auto t) AI { bad |= (zr[t].y ^ tag) | (zr[t].w ^ tag); });
      if (__all(bad == 0u)) break;
      __builtin_amdgcn_s_sleep(ZSLEEP);
    }
    STAMP(10);
    sfor<4>([&](auto t) AI { ldsw<u32x2>(ZST + j * 2048 + t * 512 + lane * 8, u32x2{zr[t].x, zr[t].z}); });
    waitl();
    float tt[16];
    sfor<4>([&](auto g) AI {
      const u32x2 z4 = ldtr(ZST + j * 2048 + (8 * g + 4 * hh + ((lane & 15) >> 2)) * 64 + 32 * ((lane >> 4) & 1) + 8 * (lane & 3));
      sfor<2>([&](auto e) AI {
        const u32 x2 = xr[g][(int)e], z2 = z4[(int)e];
        u32 n2 = pk2s(bflo(xn[2 * g + e]) * bflo(z2), bfhi(xn[2 * g + e]) * bfhi(z2));
        n2 = pk2s(bflo(x2) + bflo(n2), bfhi(x2) + bfhi(n2));
        tt[4 * g + 2 * e] = bflo(n2); tt[4 * g + 2 * e + 1] = bfhi(n2);
      });
    });
    sfor<16>([&](auto i) AI { pin(tt[i]); });
    STAMP(11);
    wait_min4<0>(FREE, 12);
    STAMP(12);
    u32x2 ob[4];
    sfor<4>([&](auto g) AI {
      const f32x4 v = ldsr<f32x4>(PART + mh * 8192 + db * 4096 + g * 1024 + lane * 16) + ldsr<f32x4>(PART + (2 + mh) * 8192 + db * 4096 + g * 1024 + lane * 16);
      ob[g].x = pk2(tt[4 * g] + rs * v.x, tt[4 * g + 1] + rs * v.y);
      ob[g].y = pk2(tt[4 * g + 2] + rs * v.z, tt[4 * g + 3] + rs * v.w);
    });
    sfor<2>([&](auto gp) AI {
      auto s0 = __builtin_amdgcn_permlane32_swap(ob[2 * gp].x, ob[2 * gp + 1].x, false, false);
      auto s1 = __builtin_amdgcn_permlane32_swap(ob[2 * gp].y, ob[2 * gp + 1].y, false, false);
      const u32x4 v = {(u32)s0[0], (u32)s1[0], (u32)s0[1], (u32)s1[1]};
      *(GAS u32x4*)((GAS char*)mkp(kb[2], kb[3]) + orow * 1024 + ocol * 2 + 32 * gp + 16 * hh) = v;
    });
    STAMP(13);
    DONE();
  } else if (wave < 4) {
    u32 junk, junk2;
    asm volatile("s_load_dword %0, %2, %3\n\ts_load_dword %1, %2, %4" : "=&s"(junk), "=&s"(junk2)
                 : "s"(pc0 & ~127ull), "s"((u32)(li + 32 * j) * 128u), "s"(min((u32)(li + 32 * (4 + j)) * 128u, CODE_BYTES)));
    const rsrc_t rws = mk_rsrc(ws, W_END);
    const int wm = wave >> 1, wn = wave & 1;
    const int l0 = lane_id();
    u32 wa = ((l0 >> 4) & 1) * (12288 + UPAD) + (8 * (l0 >> 5) + ((l0 & 15) >> 2)) * 192 + 96 * wn + 8 * (l0 & 3);
    u64 epoch;
    f32x16 acc[2][3];
    sfor<2>([&](auto m) AI { sfor<3>([&](auto n) AI { sfor<16>([&](auto r) AI { acc[m][n][(int)r] = 0.f; }); pin(acc[m][n]); }); });
    u32 xa[4];
    sfor<4>([&](auto ks) AI { xa[ks] = (64 * wm + (l0 & 31)) * 128 + (((2 * ks + (l0 >> 5)) ^ ((l0 >> 1) & 7)) << 4); pin(xa[ks]); });
    asm volatile("s_waitcnt lgkmcnt(0)" : "+s"(junk), "+s"(junk2));
    STAMP(10);
    if (has) {
      bf16x8 xf[2][2], wf[2][3];
      auto ldw = [&](auto st, auto n) AI { const u32 a = wa + G_OFF + (st & 3) * 3072 + n * 32; wf[st & 1][n] = cat(ldtr(a), ldtr(a + 768)); };
      auto ldx = [&](auto st, auto m) AI { xf[st & 1][m] = ldsr<bf16x8>(xa[st & 3] + m * 4096); };
      wait_min4<1>(FULL, 1);
      u32 accw = 0, cntw = 0;
      STAMP(1);
      ldw(IC<0>{}, IC<0>{}); ldx(IC<0>{}, IC<0>{}); ldx(IC<0>{}, IC<1>{}); ldw(IC<0>{}, IC<1>{}); ldw(IC<0>{}, IC<2>{});
      ldw(IC<1>{}, IC<0>{}); ldw(IC<1>{}, IC<1>{}); ldx(IC<1>{}, IC<0>{});
      GAP();
      u32 needw = 0x01010101u, off = 0;
#pragma unroll 1
      for (int c = 0; c < 8; ++c) {
        u32 fl, adv, have;
        sfor<24>([&](auto gg) AI {
          constexpr int st = gg / 6, g = gg % 6, n = g >> 1, m = g & 1;
          acc[m][n] = __builtin_amdgcn_mfma_f32_32x32x16_bf16(wf[st & 1][n], xf[st & 1][m], acc[m][n], 0, 0, 0);
          if constexpr (g == 0) { ldw(IC<st + 1>{}, IC<2>{}); ldx(IC<st + 1>{}, IC<1>{}); }
          if constexpr (g == 2) ldw(IC<st + 2>{}, IC<0>{});
          if constexpr (g == 4) ldw(IC<st + 2>{}, IC<1>{});
          if constexpr (g == 5) ldx(IC<st + 2>{}, IC<0>{});
          if constexpr (st == 0 && g == 3) { const u32 o2 = off == 2 * SLOT ? 0u : off + SLOT; adv = o2 - off; off = o2; needw += 0x01010101u; spin(adv); spin(off); spin(needw); }
          if constexpr (st == 1 && g == 1) fl = lds_ld1(FULL);
          if constexpr (st == 1 && g == 3) { xa[0] += adv; xa[1] += adv; xa[2] += adv; pin(xa[0]); pin(xa[1]); pin(xa[2]); }
          if constexpr (st == 2 && g == 0) { have = (rfl(fl) - needw) & 0x80808080u; spin(have); }
          if constexpr (st == 2 && g == 1) {
            xa[3] += adv; wa += adv; pin(xa[3]); pin(wa);
            if (__builtin_expect(have != 0u, 0)) wait_min4<0>(FULL, c + 2);
          }
          if constexpr (st == 3 && g == 1) lds_set(FREE + wave, c + 1);
          GAP();
        });
      }
      STAMP(2);
      __builtin_amdgcn_s_setprio(PRE);
      const int lane = lane_id(), s31 = lane & 31, hh = lane >> 5;
      f32x2 st[2];
      f32x4 ca[4];
      auto ldc = [&](auto n) AI { sfor<4>([&](auto g) AI { const u32 a = CS + (wn * 96 + n * 32 + 8 * g + 4 * hh) * 4; ca[g] = ldsr<f32x4>(a); }); };
#pragma unroll 1
      for (u32 it = 0; it < SPIN_CAP * 16; ++it) {
        const u32 r1 = lds_ld1(STRDY);
        asm volatile("" ::: "memory");
        const u32x2 e = lds_ld2(EPO);
        sfor<2>([&](auto m) AI { st[m] = ldsr<f32x2>(ST + (64 * wm + 32 * m + s31) * 8); });
        ldc(IC<0>{});
        asm volatile("" ::: "memory");
        epoch = ((u64)rfl(e.y) << 32) | rfl(e.x);
        if (__builtin_expect(all_ge(rfl(r1), 1), 1)) break;
      }
      STAMP(11);
      sfor<3>([&](auto n) AI {
        const int ks = 6 * cg + 3 * wn + n;
        if (ks < 86) {
          f32x4 c4[4];
          sfor<4>([&](auto g) AI { c4[g] = ca[g]; });
          f32x2 g[8], u[8], e[8];
          sfor<8>([&](auto i) AI {
            constexpr int m = i >> 2, r = 2 * (i & 3);
            const f32x2 cg2 = {c4[r >> 2][r & 3], c4[r >> 2][(r & 3) + 1]};
            g[i] = f32x2{acc[m][n][r], acc[m][n][r + 1]} - st[m].x * cg2;
          });
          GAP();
          sfor<8>([&](auto i) AI { g[i] = g[i] * st[i >> 2].y; });
          GAP();
          sfor<8>([&](auto i) AI { e[i] = g[i] * g[i]; });
          GAP();
          sfor<8>([&](auto i) AI { e[i] = e[i] * -0.1029432397f - 2.3022081985f; });
          GAP();
          sfor<8>([&](auto i) AI { e[i] = e[i] * g[i]; });
          GAP();
          sfor<8>([&](auto i) AI {
            constexpr int m = i >> 2, r = 2 * (i & 3);
            const f32x2 cu2 = {c4[2 + (r >> 2)][r & 3], c4[2 + (r >> 2)][(r & 3) + 1]};
            e[i] = f32x2{__builtin_amdgcn_exp2f(e[i].x), __builtin_amdgcn_exp2f(e[i].y)};
            u[i] = f32x2{acc[m][n][r + 8], acc[m][n][r + 9]} - st[m].x * cu2;
            GAP();
          });
          if constexpr (n < 2) ldc(IC<n + 1>{});
          sfor<8>([&](auto i) AI { e[i] = e[i] + 1.0f; });
          GAP();
          sfor<8>([&](auto i) AI {
            e[i] = f32x2{__builtin_amdgcn_rcpf(e[i].x), __builtin_amdgcn_rcpf(e[i].y)};
            g[i] = g[i] * u[i];
            GAP();
          });
          sfor<8>([&](auto i) AI { g[i] = g[i] * e[i]; });
          GAP();
          sfor<2>([&](auto m) AI {
            const u32x4 o = {pk2(g[4 * m].x, g[4 * m].y), pk2(g[4 * m + 1].x, g[4 * m + 1].y), pk2(g[4 * m + 2].x, g[4 * m + 2].y), pk2(g[4 * m + 3].x, g[4 * m + 3].y)};
            __builtin_amdgcn_raw_buffer_store_b128(o, rws, lane * 16 + W_H + ((4 * rb + 2 * wm + m) * 86 + ks) * 1024, 0, 16);
          });
          GAP();
          if constexpr (n < 2) STAMP(20 + n);
        }
      });
      lds_set(FREE + wave, 9);
      STAMP(3);
      waitv<0>();
      __builtin_amdgcn_s_setprio(PRW);
      if (lds_arrive(CNT + 0, lane) == 4 && lane == 0) __hip_atomic_store((GAS u64*)(ws + W_HF + (rb * 16 + cg) * 64), epoch, RLX_AGENT);
      STAMP(4);
    } else {
      if (csw) {
        f32x4 cs[3];
        sfor<3>([&](auto n) AI { cs[n] = f32x4{0.f, 0.f, 0.f, 0.f}; });
        const u32 o2_ = (l0 & 15) == ((l0 >> 4) & 1) ? 0x3f803f80u : 0u;
        const bf16x8 sel = __builtin_bit_cast(bf16x8, u32x4{o2_, o2_, o2_, o2_});
        wa += wm * 6144;
#pragma unroll 1
        for (int c = 0; c < 8; ++c) {
          wait_min4<1>(FULL, c + 1);
          bf16x8 w[6];
          sfor<6>([&](auto i) AI { const u32 a = wa + (i / 3) * 3072 + (i % 3) * 32; w[i] = cat(ldtr(a), ldtr(a + 768)); });
          asm volatile("" ::: "memory");
          lds_set(FREE + wave, c < 3 ? c + 1 : 3);
          sfor<6>([&](auto i) AI { cs[i % 3] = __builtin_amdgcn_mfma_f32_16x16x32_bf16(w[i], sel, cs[i % 3], 0, 0, 0); });
          wa += c == 4 ? (u32)(-4 * (int)CSLOT) : CSLOT;
        }
        if ((l0 & 15) < 2) sfor<3>([&](auto n) AI { ldsw<f32x4>(CS + (wm * 192 + wn * 96 + n * 32 + (l0 & 15) * 16 + 4 * (l0 >> 4)) * 4, cs[n]); });
      }
      lds_set(FREE + wave, 9);
      epoch = wait_epoch();
      if (csw && wave < 3) {
        wait_min4<1>(FREE, 9);
        const u32 i = 64 * wave + l0;
        const float v = ldsr<float>(CS + i * 4) + ldsr<float>(CS + 768 + i * 4);
        __hip_atomic_store((GAS u64*)(ws + W_CG + (cg * 192 + i) * 8), ((u64)(u32)(epoch >> 17) << 32) | __float_as_uint(v), RLX_AGENT);
      }
      STAMP(4);
    }
    const int lane = lane_id(), hh = lane >> 5, g16 = lane >> 4, q4 = (lane & 15) >> 2, p4 = lane & 3;
    const int kh = wave >> 1, mh = wave & 1;
    u32x4 hf[43];
    const u32 hso = W_H + ((2 * rt + mh) * 86 + 43 * kh) * 1024, hvo = lane * 16;
    f32x16 o2[2];
    sfor<2>([&](auto n) AI { sfor<16>([&](auto r) AI { o2[n][(int)r] = 0.f; }); pin(o2[n]); });
    u32 db_[2][3];
    sfor<2>([&](auto n) AI {
      const u32 da = kh * 8192 + (4 * hh + q4) * 128 + ((4 * ((int)n ^ (q4 >> 1)) + 2 * (g16 & 1) + (p4 >> 1)) << 4) + 8 * (p4 & 1);
      sfor<3>([&](auto w) AI { db_[n][w] = da + w * 61440; pin(db_[n][w]); });
    });
    bf16x8 df[3][2];
    u32 fl;
    auto ldd = [&](auto k) AI {
      constexpr int c = k >> 2, ks = k & 3;
      constexpr u32 wb = (c < 7 ? 16384u * c : c == 7 ? CONV : c == 8 ? CONV + CH_SZ : 16384u * (c - 9)) + ks * 2048;
      constexpr int w = wb / 61440 > 2 ? 2 : wb / 61440;
      sfor<2>([&](auto n) AI { const u32 a = db_[n][w] + (wb - w * 61440); df[k % 3][n] = cat(ldtr(a), ldtr(a + 1024)); });
    };
    if (wave == 0) { poll_flags<1>(ws, W_HF + (rt >> 1) * 16 * 64, 15, epoch, lane); lds_set(CVP + 6, 1); }
    else wait_byte<WSL>(CVP + 6, 1);
    __builtin_amdgcn_s_setprio(2);
    STAMP(5);
    auto hload = [&](auto k) AI { bld4_sc1<(k & 3) * 1024>(hf[k], hvo, rws, hso + (k >> 2) * 4096); };
    sfor<PD>([&](auto k) AI { hload(k); });
    wait_min4<0>(WDRDY, 1);
    STAMP(6);
    ldd(IC<0>{}); ldd(IC<1>{});
    GAP();
    sfor<43>([&](auto k) AI {
      if constexpr (k == 10) lds_set(FREE + wave, 10);
      if constexpr (k == 18) lds_set(FREE + wave, 11);
      if constexpr (k == 20 || k == 32) fl = lds_ld1(WDRDY);
      if constexpr (k == 40) fl = lds_ld1(FREE);
      if constexpr (k == 22 || k == 34) { STAMP(k == 22 ? 12 : 14); if (__builtin_expect(!all_ge(rfl(fl), k == 22 ? 2u : 3u), 0)) wait_min4<0>(WDRDY, k == 22 ? 2 : 3); STAMP(k == 22 ? 13 : 15); GAP(); }
      constexpr int younger = (k + PD <= 43 ? PD : 43 - k) - 1;
      waitv_pin<younger>(hf[k]);
      if constexpr (k == 0) STAMP(24);
      if constexpr (k == 8) STAMP(25);
      if constexpr (k == 16) STAMP(26);
      o2[0] = __builtin_amdgcn_mfma_f32_32x32x16_bf16(df[k % 3][0], __builtin_bit_cast(bf16x8, hf[k]), o2[0], 0, 0, 0);
      if constexpr (k + 2 < 43) ldd(IC<k + 2>{});
      GAP();
      o2[1] = __builtin_amdgcn_mfma_f32_32x32x16_bf16(df[k % 3][1], __builtin_bit_cast(bf16x8, hf[k]), o2[1], 0, 0, 0);
      if constexpr (k + PD < 43) hload(IC<k + PD>{});
      GAP();
    });
    STAMP(7);
    if (!all_ge(rfl(fl), 11u)) wait_min4<0>(FREE, 11);
    sfor<2>([&](auto n) AI { sfor<4>([&](auto g) AI {
      f32x4 v = {o2[n][4 * g], o2[n][4 * g + 1], o2[n][4 * g + 2], o2[n][4 * g + 3]};
      ldsw<f32x4>(PART + wave * 8192 + n * 4096 + g * 1024 + lane * 16, v);
    }); });
    waitl();
    lds_set(FREE + wave, 12);
    STAMP(8);
    DONE();
  } else {
    __builtin_amdgcn_s_setprio(PRL);
    const rsrc_t rg = mk_rsrc(mkp(ka[2], ka[3]), D * F * 2), ru = mk_rsrc(mkp(ka[4], ka[5]), D * F * 2);
    const int crot = ROT * ((rb + cg) & 1);
    auto chx = [&](int c, u32 sb) AI { dma4(sb + 4 * j * 1024, xv[0], xv[1], xv[2], xv[3], rq, (rb * 128 + 32 * j) * 1024 + (c ^ crot) * 128); };
    auto chw = [&](int c, u32 sb) AI {
      dma3(sb + G_OFF + 3 * j * 1024, wv[0], wv[1], wv[2], rg, (c ^ crot) * 64 * F * 2);
      dma3(sb + U_OFF + 3 * j * 1024, wv[0], wv[1], wv[2], ru, (c ^ crot) * 64 * F * 2);
    };
    if (has) {
      chx(0, 0); chw(0, 0); chx(1, SLOT); chw(1, SLOT);
      int ic = 2, lc = 0;
      u32 sb = 2 * SLOT;
#pragma unroll 1
      for (u32 it = 0; it < SPIN_CAP * 16; ++it) {
        if (lc < ic && vm_pending() <= 10u * (u32)(ic - lc - 1)) {
          asm volatile("" ::: "memory");
          ++lc;
          lds_set(FULL + j, lc == 8 ? 9 : lc);
          if (lc == 1) STAMP(1);
          if (lc == 8) break;
        } else if (ic < 8 && all_ge(rfl(lds_ld1(FREE)), ic - 2)) {
          asm volatile("" ::: "memory");
          chx(ic, sb);
          if (lc < ic && vm_pending() <= 10u * (u32)(ic - lc - 1) + 4u) {
            lds_set(FULL + j, ++lc);
          }
          chw(ic, sb);
          ++ic;
          sb = sb == 2 * SLOT ? 0 : sb + SLOT;
        } else {
          __builtin_amdgcn_s_sleep(LSLEEP);
        }
      }
      waitv<0>();
    } else if (csw) {
      sfor<8>([&](auto c) AI {
        if constexpr (c >= 5) { waitv<24>(); lds_set(FULL + j, (6u * c - vm_pending()) * 43u >> 8); wait_min4<1>(FREE, c - 4); }
        chw(c, (c % 5) * CSLOT - G_OFF);
        if constexpr (c >= 2) lds_set(FULL + j, (6u * (c + 1) - vm_pending()) * 43u >> 8);
      });
      waitv<12>(); lds_set(FULL + j, 6);
      waitv<6>(); lds_set(FULL + j, 7);
      waitv<0>(); lds_set(FULL + j, 8);
    }
    STAMP(2);
    const rsrc_t rd = mk_rsrc(mkp(kb[0], kb[1]), F * D * 2);
    u32 dv[4];
    sfor<4>([&](auto t) AI { dv[t] = (7 * t + l8) * 1024 + ((c8 ^ (((l8 >> 1) & 1) << 2)) << 4) + ct * 128; });
    auto wso = [&](int g) AI { return (u32)(((g >> 1) & 1) * 688 + (g >> 2) * 64 + (g & 1) * 32) * 1024u; };
    auto wd_groups = [&](int lo, int hi) AI {
#pragma unroll 1
      for (int g = lo + j; g < hi; g += 4) { dma4(g * 4096, dv[0], dv[1], dv[2], dv[3], rd, wso(g)); waitv<WDFL>(); }
    };
    u32x4 wr[5][4];
    auto wst = [&](auto t, u32 a) AI { sfor<4>([&](auto i) AI { pin(wr[t][i]); ldsw<u32x4>(a + j * 4096 + i * 1024 + lane * 16, wr[t][i]); }); };
    auto wrl = [&]() AI { sfor<5>([&](auto t) AI { const u32 so = wso(24 + 4 * t + j); sfor<4>([&](auto i) AI { bld4o<i * 1024>(wr[t][i], dv[i], rd, so); }); waitv<WRFL>(); }); };
    wait_min4<1>(FREE, WD1);
    wd_groups(21, 24);
    wait_min4<1>(FREE, WD2);
    wd_groups(0, 10);
    wait_min4<1>(FREE, WD3);
    wd_groups(10, 21);
    waitv<0>();
    lds_set(WDRDY + j, 1);
    wait_min4<1>(FREE, WRLAST);
    wrl();
    waitv<0>();
    lds_set(WDRDY + j, 1);
    STAMP(3);
    wait_min4<4>(CVP, 5);
    wst(IC<0>{}, XCH); wst(IC<1>{}, CONV); wst(IC<2>{}, CONV + CH_SZ);
    waitl();
    lds_set(WDRDY + j, 2);
    STAMP(4);
    wait_min4<2>(FREE, 10);
    wst(IC<3>{}, 0); wst(IC<4>{}, 16384);
    waitl();
    lds_set(WDRDY + j, 3);
    STAMP(5);
    DONE();
  }
}

extern "C" void kernel_launch(void* const* d_in, const int* in_sizes, int n_in, void* d_out, int out_size, void* d_ws, size_t ws_size,
                              hipStream_t stream) {
  Args a;
  a.q = (const u16*)d_in[0]; a.k = (const u16*)d_in[1]; a.v = (const u16*)d_in[2]; a.sk = (const u16*)d_in[3];
  a.h = (const u16*)d_in[4]; a.dv = (const u16*)d_in[5];
  a.wg = (const u16*)d_in[7]; a.wu = (const u16*)d_in[8]; a.wd = (const u16*)d_in[9];
  a.out = (u16*)d_out; a.ws = (char*)d_ws; a.pad = 0;
  (void)hipFuncSetAttribute((const void*)fused, hipFuncAttributeMaxDynamicSharedMemorySize, LDS_BYTES);
  fused<<<256, 768, LDS_BYTES, stream>>>(a);
}
```

```cpp
#define WRFL 2
#include <hip/hip_runtime.h>
#include <stdint.h>
#include <utility>

typedef unsigned short u16;
typedef unsigned int u32;
typedef unsigned long long u64;
typedef __bf16 bf16x8 __attribute__((ext_vector_type(8)));
typedef __bf16 bf16x2 __attribute__((ext_vector_type(2)));
typedef short s16x4 __attribute__((ext_vector_type(4)));
typedef short s16x8 __attribute__((ext_vector_type(8)));
typedef float f32x16 __attribute__((ext_vector_type(16)));
typedef float f32x4 __attribute__((ext_vector_type(4)));
typedef float f32x2 __attribute__((ext_vector_type(2)));
typedef u32 u32x4 __attribute__((ext_vector_type(4)));
typedef u32 u32x2 __attribute__((ext_vector_type(2)));
typedef __amdgpu_buffer_rsrc_t rsrc_t;
#define DI __device__ __forceinline__
#define AI __attribute__((always_inline))
#define LAS __attribute__((address_space(3)))
#define GAS __attribute__((address_space(1)))
#define RLX_AGENT __ATOMIC_RELAXED, __HIP_MEMORY_SCOPE_AGENT
#define RLX_WG __ATOMIC_RELAXED, __HIP_MEMORY_SCOPE_WORKGROUP

constexpr int S = 2048, D = 512, F = 1376;
#define UPAD 32
#define SLOTB (40960 + UPAD)
constexpr u32 SLOT = SLOTB, G_OFF = 16384, U_OFF = 16384 + 12288 + UPAD;
constexpr u32 CSLOT = 2 * (12288 + UPAD) + 64 - 2 * UPAD % 64;
constexpr u32 CONV = 3 * 41216;
constexpr u32 CH_SZ = 17520, HA = 64, HB = 4292, KA = 8400, KB = 12976, XB = 4304;
constexpr u32 MISC = CONV + 2 * CH_SZ;
constexpr u32 CS = MISC, ST = MISC + 1536, CNT = MISC + 2560;
constexpr u32 CKV = CNT + 8, CSP = CNT + 12, EPO = CNT + 16, CVP = CNT + 24, FULL = CNT + 32, FREE = CNT + 36, STRDY = CNT + 40, WDRDY = CNT + 44;
constexpr u32 LDS_BYTES = MISC + 2816;
static_assert(LDS_BYTES <= 163840, "LDS");
constexpr u32 XCH = 98304, ZST = 114688, PART = 32768;
constexpr u32 W_E = 0, W_PF = 4096, W_HF = W_PF + 16384, W_ZF = W_HF + 16384, W_SG = W_ZF + 16384, W_CG = W_SG + 16384;
constexpr u32 W_KV = 131072, W_ZT = W_KV + 2097152 + 4096, W_H = W_ZT + 4194304, W_END = W_H + 64 * 86 * 1024;
constexpr u32 W_DBG = 16u << 20;
constexpr u32 CODE_BYTES = 26112;
#define PRL 1
#define PRS 0
#define CONVAFTER 9
#define LSLEEP 1
#define PRA 0
#define ROT 0
#define S2AFTER 8
#define WDFL 2
#define KVSTAG 0
#define SSTOP 9
#define POLLS 4
#define PD 10
#define WD1 6
#define WD2 7
#define PFLO 8
#define PFPRIO 0
#define PFWAIT 0
#define KVMODE 0
#define WD3 8
#define PRE 2
#define PRW 0
#define WSL 1
#define PRC 3
#define ZSLEEP 4
#define ZAFTER 11
#define KVAFTER 1
constexpr u32 SPIN_CAP = 1u << 18;

constexpr int cpair(int g) {
  int i = 0;
  for (int n = 0; n < 8; ++n) for (int m = 0; m <= n; ++m) if (m + n <= 7) {
    if (i++ == g) return n * 8 + m;
    if (m < n && i++ == g) return m * 8 + n;
  }
  return 0;
}
template <class Fn, int... I> DI void sfor_impl(Fn&& f, std::integer_sequence<int, I...>) { (f(std::integral_constant<int, I>{}), ...); }
template <int N, class Fn> DI void sfor(Fn&& f) { sfor_impl(f, std::make_integer_sequence<int, N>{}); }

DI rsrc_t mk_rsrc(const void* p, u32 bytes) { return __builtin_amdgcn_make_buffer_rsrc((void*)p, (short)0, (int)bytes, 0x00020000); }
DI void dma(u32 lds, u32 voff, rsrc_t r, u32 soff) {
  asm volatile("s_mov_b32 m0, %0\n\ts_nop 0\n\tbuffer_load_dwordx4 %1, %2, %3 offen lds" ::"s"(lds), "v"(voff), "s"(r), "s"(soff) : "memory");
}

DI void dma4(u32 lds, u32 v0, u32 v1, u32 v2, u32 v3, rsrc_t r, u32 soff) {
  asm volatile("s_mov_b32 m0, %0\n\ts_nop 0\n\tbuffer_load_dwordx4 %1, %5, %6 offen lds\n\tbuffer_load_dwordx4 %2, %5, %6 offen offset:1024 lds\n\t"
               "buffer_load_dwordx4 %3, %5, %6 offen offset:2048 lds\n\tbuffer_load_dwordx4 %4, %5, %6 offen offset:3072 lds"
               ::"s"(lds), "v"(v0), "v"(v1), "v"(v2), "v"(v3), "s"(r), "s"(soff) : "memory");
}
DI void dma3(u32 lds, u32 v0, u32 v1, u32 v2, rsrc_t r, u32 soff) {
  asm volatile("s_mov_b32 m0, %0\n\ts_nop 0\n\tbuffer_load_dwordx4 %1, %4, %5 offen lds\n\tbuffer_load_dwordx4 %2, %4, %5 offen offset:1024 lds\n\t"
               "buffer_load_dwordx4 %3, %4, %5 offen offset:2048 lds"
               ::"s"(lds), "v"(v0), "v"(v1), "v"(v2), "s"(r), "s"(soff) : "memory");
}
template <int OFF> DI void bld4_sc1(u32x4& d, u32 voff, rsrc_t r, u32 soff) {
  asm volatile("buffer_load_dwordx4 %0, %1, %2, %3 offen offset:%4 sc1" : "=v"(d) : "v"(voff), "s"(r), "s"(soff), "i"(OFF));
}
template <int OFF> DI void bld4o(u32x4& d, u32 voff, rsrc_t r, u32 soff) { asm volatile("buffer_load_dwordx4 %0, %1, %2, %3 offen offset:%4" : "=v"(d) : "v"(voff), "s"(r), "s"(soff), "i"(OFF)); }
DI void bld4(u32x4& d, u32 voff, rsrc_t r, u32 soff) { asm volatile("buffer_load_dwordx4 %0, %1, %2, %3 offen" : "=v"(d) : "v"(voff), "s"(r), "s"(soff)); }
template <int OFF> DI void bld2(u32x2& d, u32 voff, rsrc_t r) { asm volatile("buffer_load_dwordx2 %0, %1, %2, 0 offen offset:%3" : "=v"(d) : "v"(voff), "s"(r), "i"(OFF)); }
template <int OFF> DI void gld4_sc1(u32x4& d, u32 voff, const char* base) {
  asm volatile("global_load_dwordx4 %0, %1, %2 offset:%3 sc1" : "=v"(d) : "v"(voff), "s"(base), "i"(OFF));
}
template <int N> DI void waitv_pin(u32x4& d) { asm volatile("s_waitcnt vmcnt(%1)" : "+v"(d) : "i"(N)); }
template <int N> DI void waitv() { asm volatile("s_waitcnt vmcnt(%0)" ::"i"(N) : "memory"); }
DI u32 vm_pending() { u32 r; asm volatile("s_getreg_b32 %0, hwreg(HW_REG_IB_STS)" : "=s"(r)::"memory"); return (r & 15u) | ((r >> 18) & 48u); }
DI void bar() { asm volatile("s_waitcnt lgkmcnt(0)\n\ts_barrier" ::: "memory"); }
DI void waitl() { asm volatile("s_waitcnt lgkmcnt(0)" ::: "memory"); }
DI float bflo(u32 x) { return __uint_as_float(x << 16); }
DI float bfhi(u32 x) { return __uint_as_float(x & 0xffff0000u); }
DI u32 pk2(float lo, float hi) { f32x2 v = {lo, hi}; return __builtin_bit_cast(u32, __builtin_convertvector(v, bf16x2)); }
DI u32 pk2s(float lo, float hi) { asm("" : "+v"(lo)); asm("" : "+v"(hi)); return pk2(lo, hi); }
DI float rbf(float x) { return bflo(pk2(x, x)); }
template <class T> DI T ldsr(u32 a) { return *(const LAS T*)a; }
template <class T> DI void ldsw(u32 a, T v) { *(LAS T*)a = v; }
DI u32x2 ldtr(u32 a) { return __builtin_bit_cast(u32x2, __builtin_amdgcn_ds_read_tr16_b64_v4i16((LAS s16x4*)a)); }
DI bf16x8 cat(u32x2 a, u32x2 b) { u32x4 v = {a.x, a.y, b.x, b.y}; return __builtin_bit_cast(bf16x8, v); }
template <int CTRL, int RM> DI float dpp_add(float v) {
  return v + __builtin_bit_cast(float, __builtin_amdgcn_update_dpp(0, __builtin_bit_cast(int, v), CTRL, RM, 0xf, false));
}
DI float wave_sum(float v) {
  v = dpp_add<0x111, 0xf>(v); v = dpp_add<0x112, 0xf>(v); v = dpp_add<0x114, 0xf>(v); v = dpp_add<0x118, 0xf>(v);
  v = dpp_add<0x142, 0xa>(v); v = dpp_add<0x143, 0xc>(v);
  return __builtin_bit_cast(float, __builtin_amdgcn_readlane(__builtin_bit_cast(int, v), 63));
}
DI void wave_sum2(float& a, float& c) {
  a = dpp_add<0x111, 0xf>(a); c = dpp_add<0x111, 0xf>(c); a = dpp_add<0x112, 0xf>(a); c = dpp_add<0x112, 0xf>(c);
  a = dpp_add<0x114, 0xf>(a); c = dpp_add<0x114, 0xf>(c); a = dpp_add<0x118, 0xf>(a); c = dpp_add<0x118, 0xf>(c);
  a = dpp_add<0x142, 0xa>(a); c = dpp_add<0x142, 0xa>(c); a = dpp_add<0x143, 0xc>(a); c = dpp_add<0x143, 0xc>(c);
  a = __builtin_bit_cast(float, __builtin_amdgcn_readlane(__builtin_bit_cast(int, a), 63));
  c = __builtin_bit_cast(float, __builtin_amdgcn_readlane(__builtin_bit_cast(int, c), 63));
}
DI void gld8_sc1(u64& d, const char* p) { asm volatile("global_load_dwordx2 %0, %1, off sc1" : "=v"(d) : "v"(p)); }
DI void gld_u16(u32& d, u32 voff, const char* base) { asm volatile("global_load_ushort %0, %1, %2" : "=v"(d) : "v"(voff), "s"(base)); }
template <int SLEEP> DI void poll_flags(const char* ws, u32 off, int n, u64 epoch, int lane) {
  const GAS u64* p = (const GAS u64*)(ws + off + (u32)(lane < n ? lane : 0) * 64u);
#pragma unroll 1
  for (u32 it = 0; it < SPIN_CAP; ++it) {
    u64 v = __hip_atomic_load(p, RLX_AGENT);
    if (__all(v == epoch)) break;
    __builtin_amdgcn_s_sleep(SLEEP);
  }
  asm volatile("" ::: "memory");
}
DI u32 poll_granule(const char* ws, u32 row, u32 tag) {
  const GAS u64* p = (const GAS u64*)(ws + W_SG + row * 8u);
  u64 v = 0;
#pragma unroll 1
  for (u32 it = 0; it < SPIN_CAP; ++it) {
    v = __hip_atomic_load(p, RLX_AGENT);
    if (__all((u32)(v >> 32) == tag)) break;
    __builtin_amdgcn_s_sleep(1);
  }
  return (u32)v;
}
DI u32 lds_arrive(u32 a, int lane) {
  u32 r = 0;
  waitl();
  if (lane == 0) r = __hip_atomic_fetch_add((LAS u32*)a, 1u, RLX_WG);
  return (u32)__builtin_amdgcn_readfirstlane(r) + 1u;
}
DI void lds_wait(u32 a, u32 want) {
#pragma unroll 1
  for (u32 it = 0; it < SPIN_CAP * 16; ++it) {
    u32 v = __hip_atomic_load((LAS u32*)a, RLX_WG);
    if ((u32)__builtin_amdgcn_readfirstlane(v) >= want) break;
  }
  asm volatile("" ::: "memory");
}

DI int lane_id() { int l; asm volatile("v_mbcnt_lo_u32_b32 %0, -1, 0\n\tv_mbcnt_hi_u32_b32 %0, -1, %0" : "=v"(l)); return l; }
DI u32 rfl(u32 x) { return (u32)__builtin_amdgcn_readfirstlane(x); }
DI u32x2 lds_ld2(u32 a) { return __builtin_bit_cast(u32x2, __hip_atomic_load((LAS u64*)a, RLX_WG)); }
template <int SLEEP> DI void wait_min2(u32 a, u32 want) {
#pragma unroll 1
  for (u32 it = 0; it < SPIN_CAP * 16; ++it) {
    const u32x2 v = lds_ld2(a);
    if (rfl(min(v.x, v.y)) >= want) break;
    if (SLEEP) __builtin_amdgcn_s_sleep(SLEEP);
  }
  asm volatile("" ::: "memory");
}
DI u32 lds_ld1(u32 a) { return __hip_atomic_load((LAS u32*)a, RLX_WG); }
DI bool all_ge(u32 w, u32 want) { return ((w - want * 0x01010101u) & 0x80808080u) == 0u; }
template <int SLEEP> DI void wait_min4(u32 a, u32 want) {
#pragma unroll 1
  for (u32 it = 0; it < SPIN_CAP * 16; ++it) {
    if (all_ge(rfl(lds_ld1(a)), want)) break;
    if (SLEEP) __builtin_amdgcn_s_sleep(SLEEP);
  }
  asm volatile("" ::: "memory");
}
template <int SLEEP> DI void wait_byte(u32 a, u32 want) {
#pragma unroll 1
  for (u32 it = 0; it < SPIN_CAP * 16; ++it) {
    if (rfl(__hip_atomic_load((LAS unsigned char*)a, RLX_WG)) >= want) break;
    if (SLEEP) __builtin_amdgcn_s_sleep(SLEEP);
  }
  asm volatile("" ::: "memory");
}
DI u64 wait_epoch() {
  u32x2 e;
#pragma unroll 1
  for (u32 it = 0; it < SPIN_CAP * 16; ++it) {
    e = lds_ld2(EPO);
    if (rfl(e.x) != 0u) break;
    __builtin_amdgcn_s_sleep(2);
  }
  return ((u64)rfl(e.y) << 32) | rfl(e.x);
}
DI u64 gld_agent(const char* p) { return __hip_atomic_load((const GAS u64*)p, RLX_AGENT); }
DI void lds_set(u32 a, u32 v) { __hip_atomic_store((LAS unsigned char*)a, (unsigned char)v, RLX_WG); }
DI void gather(u32& junk, u32 voff, rsrc_t r, u32 soff) { asm volatile("buffer_load_dword %0, %1, %2, %3 offen" : "=v"(junk) : "v"(voff), "s"(r), "s"(soff)); }
template <int N> using IC = std::integral_constant<int, N>;

#define DONE() do {} while (0)
#define STAMP(id) do {} while (0)

struct Args { const u16 *q, *wg, *wu, *k, *v, *sk, *h, *dv, *wd; u16* out; char* ws; u64 pad; };
typedef u32 u32x16 __attribute__((ext_vector_type(16)));
typedef u32 u32x8 __attribute__((ext_vector_type(8)));
DI char* mkp(u32 lo, u32 hi) { return (char*)(((u64)hi << 32) | lo); }
#define GAP() __builtin_amdgcn_sched_barrier(0)
DI void barx() { asm volatile("s_barrier" ::: "memory"); }
template <class T> DI void spin(T& x) { asm volatile("" : "+s"(x)); }
template <class T> DI void pin(T& x) { asm volatile("" : "+v"(x)); }

__global__ void __launch_bounds__(768, 3) fused(Args) {
  u64 pc0; asm volatile("s_getpc_b64 %0" : "=s"(pc0));
  u32x16 ka; u32x8 kb;
  asm volatile("s_load_dwordx16 %0, %2, 0x0\n\ts_load_dwordx8 %1, %2, 0x40" : "=&s"(ka), "=&s"(kb) : "s"(__builtin_amdgcn_kernarg_segment_ptr()));
  GAP();
  const int tid = threadIdx.x, lane = tid & 63;
  const int wave = __builtin_amdgcn_readfirstlane(tid >> 6);
  const int b = blockIdx.x, xcd = b & 7, li = b >> 3;
  if (tid < 16) ldsw<u32>(CNT + tid * 4, 0u);
  const bool has = li < 30;
  const int id = 30 * xcd + (has ? li : 0), rr_ = id % 120, rb = 8 * (id / 120) + (rr_ & 7);
  const int cg = has ? rr_ >> 3 : 4 * (xcd & 3) + 2 * (xcd >> 2) + (li - 30);
  const bool csw = !has && cg < 15;
  const int rt = 4 * xcd + (li & 3), ct = li >> 2;
  const int j = wave & 3, l8 = lane >> 3, c8 = lane & 7;
  u32 xv[4], wv[3];
  sfor<4>([&](auto t) AI { xv[t] = (7 * t + l8) * 1024 + ((c8 ^ ((4 * t + (l8 >> 1)) & 7)) << 4); pin(xv[t]); });
  sfor<3>([&](auto t) AI {
    const u32 byte = t * 1024 + lane * 16, row = byte / 192u;
    const u32 seg = byte - row * 192u;
    wv[t] = cg * 192 + seg >= F * 2 ? 0x40000000u : (16 * j + row) * (F * 2) + cg * 192 + seg - t * 1024;
    pin(wv[t]);
  });
  asm volatile("s_waitcnt lgkmcnt(0)\n\ts_barrier" : "+s"(ka), "+s"(kb)::"memory");
  STAMP(9);
  char* const ws = mkp(kb[4], kb[5]);
  const rsrc_t rq = mk_rsrc(mkp(ka[0], ka[1]), S * D * 2);
  if (wave >= 8) {
    __builtin_amdgcn_s_setprio(3);
    u64 epoch;
    asm volatile("s_load_dwordx2 %0, %1, 0x0" : "=s"(epoch) : "s"(ws));
    const int lane = lane_id(), l8 = lane >> 3, c8 = lane & 7;
    const int ch = j & 1, cp = j >> 1, c16 = lane & 15, q = lane >> 4;
    const rsrc_t rws = mk_rsrc(ws, W_END);
    const u32 cb = CONV + ch * CH_SZ, hown = cb + (cp ? XB : HA), kown = cb + (cp ? KB : KA);
    const int srow = 256 * xcd + 8 * li + 2 * j;
    if (SSTOP == 0) wait_min4<8>(FREE, 8);
    u32x4 sx[2];
    sfor<2>([&](auto r) AI { bld4(sx[r], lane * 16, rq, (srow + r) * 1024); });
    STAMP(23);
    const int pa = b & 31, pg = b >> 5, s0 = 64 * pa + 16 * j;
    u32x2 skw[2];
    u32 dvv;
    auto kvload = [&]() AI {
      const rsrc_t rk = mk_rsrc(mkp(ka[6], ka[7]), S * D * 2), rv = mk_rsrc(mkp(ka[8], ka[9]), S * D * 2), rs_ = mk_rsrc(mkp(ka[10], ka[11]), D * 8);
      const u32 kvv = l8 * 1024 + c8 * 16 + pg * 128;
      sfor<3>([&](auto t) AI { const int r = s0 - 8 + 8 * t; dma(hown + t * 1024, kvv, rk, (r < 0 ? 0 : r) * 1024); });
      sfor<2>([&](auto t) AI { dma(kown + t * 1024, kvv, rv, (s0 + 8 * t) * 1024); });
      sfor<2>([&](auto t) AI { bld2<256 * t>(skw[t], (64 * pg + 16 * (q >> 1) + c16) * 8, rs_); });
      gld_u16(dvv, (2 * b + ch) * 2, mkp(ka[14], ka[15]));
    };
    asm volatile("s_waitcnt lgkmcnt(0)" : "+s"(epoch));
    epoch += 0x9E3779B97F4A7C15ull;
    epoch = (epoch ^ (epoch >> 30)) * 0xBF58476D1CE4E5B9ull; epoch = (epoch ^ (epoch >> 27)) * 0x94D049BB133111EBull;
    epoch = (epoch ^ (epoch >> 31)) | 1ull;
    ldsw<u64>(EPO, epoch);
    STAMP(18);
    const u32 tag = (u32)(epoch >> 17);
    if (has) wait_min4<2>(FULL, KVAFTER + KVSTAG * (li & 3));
    STAMP(19);
    kvload();
    STAMP(20);
    asm volatile("s_waitcnt vmcnt(8)" : "+v"(sx[0]), "+v"(sx[1]));
    STAMP(21);
    __builtin_amdgcn_s_setprio(0);
    {
      float x[2][8], mean[2], var[2];
      sfor<2>([&](auto r) AI {
        sfor<4>([&](auto i) AI { x[r][2 * i] = bflo(sx[r][(int)i]); x[r][2 * i + 1] = bfhi(sx[r][(int)i]); });
        mean[r] = x[r][0];
        sfor<7>([&](auto i) AI { mean[r] += x[r][i + 1]; });
      });
      wave_sum2(mean[0], mean[1]);
      sfor<2>([&](auto r) AI {
        mean[r] *= 1.0f / D; var[r] = 0.f;
        sfor<8>([&](auto i) AI { float d = x[r][i] - mean[r]; asm("" : "+v"(d)); var[r] += d * d; });
      });
      wave_sum2(var[0], var[1]);
      STAMP(22);
      const float mn = lane ? mean[1] : mean[0], vr = rbf((lane ? var[1] : var[0]) * (1.0f / D));
      const u32 g = pk2s(mn, __builtin_sqrtf(rbf(vr + 1e-5f)));
      if (lane < 2) __hip_atomic_store((GAS u64*)(ws + W_SG + (srow + lane) * 8), ((u64)tag << 32) | g, RLX_AGENT);
    }
    STAMP(1);
    asm volatile("s_waitcnt vmcnt(1)" : "+v"(skw[0]), "+v"(skw[1]), "+v"(dvv)::"memory");
    STAMP(2);
    {
      const bool first = s0 == 0 && (q & 1) == 0;
      u32x2 kr[2][5];
      sfor<2>([&](auto gi) AI {
        const u32 a = (8 * (q & 1) + (c16 >> 2)) * 128 + 64 * gi + 32 * (q >> 1) + 8 * (lane & 3);
        kr[gi][0] = ldtr(hown + a + 512); kr[gi][1] = ldtr(hown + a + 1024); kr[gi][2] = ldtr(hown + a + 1536); kr[gi][3] = ldtr(kown + a); kr[gi][4] = ldtr(kown + a + 512);
      });
      GAP();
      if (SSTOP == 1) wait_min4<8>(FREE, 8);
      sfor<2>([&](auto gi) AI {
        const float w0 = bflo(skw[gi].x), w1 = bfhi(skw[gi].x), w2 = bflo(skw[gi].y), w3 = bfhi(skw[gi].y);
        const u32x2 k0 = kr[gi][0], k1 = kr[gi][1], k2 = kr[gi][2], v0 = kr[gi][3], v1 = kr[gi][4];
        const float kk[11] = {first ? 0.f : bfhi(k0.x), first ? 0.f : bflo(k0.y), first ? 0.f : bfhi(k0.y), bflo(k1.x), bfhi(k1.x), bflo(k1.y), bfhi(k1.y), bflo(k2.x), bfhi(k2.x), bflo(k2.y), bfhi(k2.y)};
        const float vv[8] = {bflo(v0.x), bfhi(v0.x), bflo(v0.y), bfhi(v0.y), bflo(v1.x), bfhi(v1.x), bflo(v1.y), bfhi(v1.y)};
        u32 a4[4], m4[4];
        sfor<4>([&](auto t) AI { a4[t] = pk2s(w3 * kk[2 * t], w3 * kk[2 * t + 1]); m4[t] = pk2s(w2 * kk[2 * t + 1], w2 * kk[2 * t + 2]); });
        GAP();
        sfor<4>([&](auto t) AI { a4[t] = pk2s(bflo(a4[t]) + bflo(m4[t]), bfhi(a4[t]) + bfhi(m4[t])); m4[t] = pk2s(w1 * kk[2 * t + 2], w1 * kk[2 * t + 3]); });
        GAP();
        sfor<4>([&](auto t) AI { a4[t] = pk2s(bflo(a4[t]) + bflo(m4[t]), bfhi(a4[t]) + bfhi(m4[t])); m4[t] = pk2s(w0 * kk[2 * t + 3], w0 * kk[2 * t + 4]); });
        GAP();
        sfor<4>([&](auto t) AI { a4[t] = pk2s(bflo(a4[t]) + bflo(m4[t]), bfhi(a4[t]) + bfhi(m4[t])); });
        GAP();
        u32x4 ov;
        sfor<4>([&](auto t) AI { ov[3 - t] = pk2s(bfhi(a4[t]) * vv[2 * t + 1], bflo(a4[t]) * vv[2 * t]); });
        const int dg = 2 * gi + (q >> 1);
        ldsw<u32x4>(CONV + (dg & 1) * CH_SZ + (dg >> 1 ? KB : KA) + 2048 + c16 * 128 + (((7 - 2 * j - (q & 1)) ^ (c16 & 7)) << 4), ov);
      });
    }
    waitl();
    lds_set(CVP + j, 1);
    __builtin_amdgcn_s_setprio(3);
    STAMP(17);
    wait_min4<0>(CVP, 1);
    {
      u32x4 o[2];
      sfor<2>([&](auto t) AI { o[t] = ldsr<u32x4>(kown + 2048 + (8 * t + l8) * 128 + ((c8 ^ l8) << 4)); });
      sfor<2>([&](auto t) AI { __builtin_amdgcn_raw_buffer_store_b128(o[t], rws, W_KV + (64 * pg + 16 * j + 8 * t + l8) * 4096 + (1984 - 64 * pa) * 2 + c8 * 16, 0, 16); });
    }
    const int mh = j & 1, db = j >> 1, s31 = lane & 31, hh = lane >> 5;
    const int orow = 64 * rt + 32 * mh + s31, ocol = 64 * ct + 32 * db;
    u32x2 xr[4];
    u32 sg;
    {
      const char* p1 = ws + W_SG + (hh ? orow : rb * 128 + 32 * j + s31) * 8;
      const char* p2 = ws + W_CG + ((has ? cg : 0) * 192 + 48 * j + (lane < 48 ? lane : 0)) * 8;
      u64 g1, g2;
      gld8_sc1(g1, p1); gld8_sc1(g2, p2);
      asm volatile("s_waitcnt vmcnt(0)" : "+v"(g1), "+v"(g2)::"memory");
      if (lds_arrive(CKV, lane) == 4 && lane == 0) __hip_atomic_store((GAS u64*)(ws + W_PF + b * 64), epoch, RLX_AGENT);
      STAMP(3);
#pragma unroll 1
      for (u32 it = 0; it < SPIN_CAP; ++it) {
        if (__all((u32)(g1 >> 32) == tag && (u32)(g2 >> 32) == tag)) break;
        __builtin_amdgcn_s_sleep(4);
        g1 = gld_agent(p1); g2 = gld_agent(p2);
      }
      sg = (u32)g1;
      if (has) {
        if (lane < 32) ldsw<f32x2>(ST + (32 * j + lane) * 8, f32x2{bflo(sg), __builtin_amdgcn_rcpf(bfhi(sg))});
        if (lane < 48) ldsw<u32>(CS + (48 * j + lane) * 4, (u32)g2);
      }
      lds_set(STRDY + j, 1);
      STAMP(4);
    }
    if (has) wait_min4<4>(FULL, S2AFTER);
    STAMP(24);
    {
      u64 f;
      gld8_sc1(f, ws + W_PF + ((b >> 5) * 32 + (j == 0 ? s31 : 0)) * 64);
      dma4(cb + (cp ? HB : HA), lane * 16, lane * 16, lane * 16, lane * 16, mk_rsrc(mkp(ka[12], ka[13]), D * S * 2), (2 * b + ch) * 4096);
      if (lane < (cp ? 33 : 16)) ldsw<u32>(cb + (cp ? 4160 : 0) + lane * 4, 0u);
      if (lane < (cp ? 28 : 30)) ldsw<u32x4>(kown + 4096 + lane * 16, u32x4{0u, 0u, 0u, 0u});
      asm volatile("s_waitcnt vmcnt(4)" : "+v"(f));
      sg = (u32)__builtin_amdgcn_permlane32_swap(sg, sg, false, false)[1];
      if (j == 0) { if (!__all(f == epoch)) poll_flags<2>(ws, W_PF + (b >> 5) * 32 * 64, 32, epoch, lane); lds_set(CVP + 4, 1); }
      wait_byte<1>(CVP + 4, 1);
      u32x4 kvr[4];
      const char* src = ws + W_KV + (2 * b + ch) * 4096 + 2 * cp;
      sfor<4>([&](auto t) AI { gld4_sc1<t * 1024>(kvr[t], lane * 16, src); });
      wait_min4<1>(FREE, 6);
      sfor<2>([&](auto t) AI {
        const int r = 16 * t + (lane >> 2);
        dma(ZST + j * 2048 + t * 1024, r * 1024 + (((lane & 3) ^ ((r >> 1) & 3)) << 4), rq, (64 * rt + 32 * mh) * 1024 + ocol * 2);
      });
      asm volatile("s_waitcnt vmcnt(0)" : "+v"(kvr[0]), "+v"(kvr[1]), "+v"(kvr[2]), "+v"(kvr[3])::"memory");
      if (cp && lane == 63) kvr[3].w &= 0xffffu;
      sfor<4>([&](auto t) AI { ldsw<u32x4>(kown + t * 1024 + lane * 16, kvr[t]); });
      sfor<4>([&](auto g) AI { xr[g] = ldsr<u32x2>(ZST + j * 2048 + s31 * 64 + ((g ^ ((s31 >> 1) & 3)) << 4) + 8 * hh); });
      waitl();
      pin(xr[0]); pin(xr[1]); pin(xr[2]); pin(xr[3]);
      lds_set(CVP + j, 2);
      STAMP(5);
    }
    __builtin_amdgcn_s_setprio(0);
    wait_min4<1>(FREE, CONVAFTER);
    wait_byte<0>(CVP + (j ^ 2), 2);
    __builtin_amdgcn_s_setprio(PRC);
    STAMP(6);
    f32x4 dacc[8];
    sfor<8>([&](auto i) AI { dacc[i] = f32x4{0.f, 0.f, 0.f, 0.f}; });
    u32 kv2[8];
    {
      u32 hb = cb + ((c16 & 1) ? HA : HB) + 4 * (c16 - 15) + 16 * q + 256 * cp;
      u32 kb_ = cb + ((c16 & 1) ? KA : KB) + 448 - 64 * (c16 >> 1) + 16 * q + 256 * cp;
      u32 hb2 = hb + 8;
      pin(hb); pin(hb2); pin(kb_);
      bf16x8 ta[12], kf[12];
      auto ld1 = [&](auto odd, auto n, auto w, auto off) AI {
        constexpr int r = (n < 4 && odd) ? 8 + n : n;
        if constexpr (w == 0) kf[r] = ldsr<bf16x8>(kb_ + 512 * (7 - n) + off);
        if constexpr (w == 1) { const u32x2 v = ldsr<u32x2>(hb + 512 * n + off); u32x4 t = __builtin_bit_cast(u32x4, ta[r]); t.x = v.x; t.y = v.y; ta[r] = __builtin_bit_cast(bf16x8, t); }
        if constexpr (w == 2) { const u32x2 v = ldsr<u32x2>(hb2 + 512 * n + off); u32x4 t = __builtin_bit_cast(u32x4, ta[r]); t.z = v.x; t.w = v.y; ta[r] = __builtin_bit_cast(bf16x8, t); }
      };
      auto cstep = [&](auto odd, auto off) AI {
        sfor<36>([&](auto g) AI {
          constexpr int c = cpair(35 - g) >> 3, dl = cpair(35 - g) & 7;
          constexpr int rc = (c < 4 && odd) ? 8 + c : c, rd_ = (dl < 4 && odd) ? 8 + dl : dl;
          dacc[c + dl] = __builtin_amdgcn_mfma_f32_16x16x32_bf16(kf[rc], ta[rd_], dacc[c + dl], 0, 0, 0);
          if constexpr (g < 12) ld1(IC<1 - odd>{}, IC<g / 3>{}, IC<g % 3>{}, off);
          else if constexpr (g < 24) ld1(IC<1 - odd>{}, IC<7 - (g - 12) / 3>{}, IC<g % 3>{}, off);
          GAP();
        });
      };
      sfor<24>([&](auto i) AI { ld1(IC<0>{}, IC<(i < 3 ? 0 : i < 6 ? 7 : i < 9 ? 1 : i < 12 ? 6 : i < 15 ? 2 : i < 18 ? 5 : i < 21 ? 3 : 4)>{}, IC<i % 3>{}, IC<0>{}); GAP(); });
#pragma unroll 1
      for (int i = 0; i < 2; ++i) {
        cstep(IC<0>{}, IC<64>{});
        cstep(IC<1>{}, IC<128>{});
        hb += 128; hb2 += 128; kb_ += 128;
      }
    }
    __builtin_amdgcn_s_setprio(3);
    STAMP(25);
    sfor<4>([&](auto i) AI { ldsw<f32x4>(XCH + j * 4096 + i * 1024 + lane * 16, cp ? dacc[i] : dacc[4 + i]); });
    lds_set(CVP + j, 4);
    {
      const int l = lane_id();
      u32 a = cb + KA + 2 * (2046 - 2 * (l & 15) - 64 * (l >> 4) - 1024 * cp) - 1600;
      pin(a);
      sfor<8>([&](auto i) AI { kv2[i] = ldsr<u32>(a + 64 * (1 - (i & 1)) + 512 * (3 - (i >> 1))); });
    }
    STAMP(7);
    {
      const float dv = bflo(dvv);
      sfor<8>([&](auto i) AI { kv2[i] = pk2s(bfhi(kv2[i]) * dv, bflo(kv2[i]) * dv); pin(kv2[i]); });
      wait_byte<0>(CVP + (j ^ 2), 4);
      f32x4 o[4];
      sfor<4>([&](auto i) AI { o[i] = ldsr<f32x4>(XCH + (j ^ 2) * 4096 + i * 1024 + lane * 16); });
      asm volatile("" ::: "memory");
      lds_set(CVP + j, 5);
      sfor<4>([&](auto i) AI {
        const f32x4 m = cp ? dacc[4 + i] : dacc[i];
        sfor<2>([&](auto rr) AI {
          const u32 yb = pk2s(m[2 * rr] + o[i][2 * rr], m[2 * rr + 1] + o[i][2 * rr + 1]), kd = kv2[2 * i + rr];
          ldsw<u32>(ZST + j * 2048 + 4 * c16 + 128 * q + 64 * rr + 512 * i, pk2s(bflo(yb) + bflo(kd), bfhi(yb) + bfhi(kd)));
        });
      });
      waitl();
      u32x2 zo[4];
      sfor<4>([&](auto t) AI { zo[t] = ldsr<u32x2>(ZST + j * 2048 + t * 512 + lane * 8); });
      sfor<4>([&](auto t) AI {
        __builtin_amdgcn_raw_buffer_store_b128(u32x4{zo[t].x, tag, zo[t].y, tag}, rws, lane * 16 + W_ZT + (2 * b + ch) * 8192 + cp * 4096 + t * 1024, 0, 16);
      });
    }
    STAMP(8);
    const float mean = bflo(sg), rs = __builtin_amdgcn_rcpf(bfhi(sg));
    u32 xn[8];
    sfor<8>([&](auto i) AI {
      const u32 x2 = xr[i >> 1][i & 1], n2 = pk2s(bflo(x2) - mean, bfhi(x2) - mean);
      xn[i] = pk2s(bflo(n2) * rs, bfhi(n2) * rs);
      pin(xn[i]);
    });
    if (b == 0 && j == 3) {
      sfor<4>([&](auto t) AI { poll_flags<1>(ws, W_PF + t * 4096, 64, epoch, lane); });
      if (lane == 0) __hip_atomic_store((GAS u64*)(ws + W_E), epoch, RLX_AGENT);
    }
    wait_min4<2>(FREE, ZAFTER);
    u32x4 zr[4];
#pragma unroll 1
    for (u32 it = 0; it < SPIN_CAP; ++it) {
      sfor<4>([&](auto t) AI { bld4_sc1<0>(zr[t], l8 * 8192 + c8 * 16, rws, W_ZT + (ocol + 8 * t) * 8192 + (64 * rt + 32 * mh) * 4); });
      asm volatile("s_waitcnt vmcnt(0)" : "+v"(zr[0]), "+v"(zr[1]), "+v"(zr[2]), "+v"(zr[3])::"memory");
      u32 bad = 0;
      sfor<4>([&](auto t) AI { bad |= (zr[t].y ^ tag) | (zr[t].w ^ tag); });
      if (__all(bad == 0u)) break;
      __builtin_amdgcn_s_sleep(ZSLEEP);
    }
    STAMP(10);
    sfor<4>([&](auto t) AI { ldsw<u32x2>(ZST + j * 2048 + t * 512 + lane * 8, u32x2{zr[t].x, zr[t].z}); });
    waitl();
    float tt[16];
    sfor<4>([&](auto g) AI {
      const u32x2 z4 = ldtr(ZST + j * 2048 + (8 * g + 4 * hh + ((lane & 15) >> 2)) * 64 + 32 * ((lane >> 4) & 1) + 8 * (lane & 3));
      sfor<2>([&](auto e) AI {
        const u32 x2 = xr[g][(int)e], z2 = z4[(int)e];
        u32 n2 = pk2s(bflo(xn[2 * g + e]) * bflo(z2), bfhi(xn[2 * g + e]) * bfhi(z2));
        n2 = pk2s(bflo(x2) + bflo(n2), bfhi(x2) + bfhi(n2));
        tt[4 * g + 2 * e] = bflo(n2); tt[4 * g + 2 * e + 1] = bfhi(n2);
      });
    });
    sfor<16>([&](auto i) AI { pin(tt[i]); });
    STAMP(11);
    wait_min4<0>(FREE, 12);
    STAMP(12);
    u32x2 ob[4];
    sfor<4>([&](auto g) AI {
      const f32x4 v = ldsr<f32x4>(PART + mh * 8192 + db * 4096 + g * 1024 + lane * 16) + ldsr<f32x4>(PART + (2 + mh) * 8192 + db * 4096 + g * 1024 + lane * 16);
      ob[g].x = pk2(tt[4 * g] + rs * v.x, tt[4 * g + 1] + rs * v.y);
      ob[g].y = pk2(tt[4 * g + 2] + rs * v.z, tt[4 * g + 3] + rs * v.w);
    });
    sfor<2>([&](auto gp) AI {
      auto s0 = __builtin_amdgcn_permlane32_swap(ob[2 * gp].x, ob[2 * gp + 1].x, false, false);
      auto s1 = __builtin_amdgcn_permlane32_swap(ob[2 * gp].y, ob[2 * gp + 1].y, false, false);
      const u32x4 v = {(u32)s0[0], (u32)s1[0], (u32)s0[1], (u32)s1[1]};
      *(GAS u32x4*)((GAS char*)mkp(kb[2], kb[3]) + orow * 1024 + ocol * 2 + 32 * gp + 16 * hh) = v;
    });
    STAMP(13);
    DONE();
  } else if (wave < 4) {
    u32 junk, junk2;
    asm volatile("s_load_dword %0, %2, %3\n\ts_load_dword %1, %2, %4" : "=&s"(junk), "=&s"(junk2)
                 : "s"(pc0 & ~127ull), "s"((u32)(li + 32 * j) * 128u), "s"(min((u32)(li + 32 * (4 + j)) * 128u, CODE_BYTES)));
    const rsrc_t rws = mk_rsrc(ws, W_END);
    const int wm = wave >> 1, wn = wave & 1;
    const int l0 = lane_id();
    u32 wa = ((l0 >> 4) & 1) * (12288 + UPAD) + (8 * (l0 >> 5) + ((l0 & 15) >> 2)) * 192 + 96 * wn + 8 * (l0 & 3);
    u64 epoch;
    f32x16 acc[2][3];
    sfor<2>([&](auto m) AI { sfor<3>([&](auto n) AI { sfor<16>([&](auto r) AI { acc[m][n][(int)r] = 0.f; }); pin(acc[m][n]); }); });
    u32 xa[4];
    sfor<4>([&](auto ks) AI { xa[ks] = (64 * wm + (l0 & 31)) * 128 + (((2 * ks + (l0 >> 5)) ^ ((l0 >> 1) & 7)) << 4); pin(xa[ks]); });
    asm volatile("s_waitcnt lgkmcnt(0)" : "+s"(junk), "+s"(junk2));
    STAMP(10);
    if (has) {
      bf16x8 xf[2][2], wf[2][3];
      auto ldw = [&](auto st, auto n) AI { const u32 a = wa + G_OFF + (st & 3) * 3072 + n * 32; wf[st & 1][n] = cat(ldtr(a), ldtr(a + 768)); };
      auto ldx = [&](auto st, auto m) AI { xf[st & 1][m] = ldsr<bf16x8>(xa[st & 3] + m * 4096); };
      wait_min4<1>(FULL, 1);
      u32 accw = 0, cntw = 0;
      STAMP(1);
      ldw(IC<0>{}, IC<0>{}); ldx(IC<0>{}, IC<0>{}); ldx(IC<0>{}, IC<1>{}); ldw(IC<0>{}, IC<1>{}); ldw(IC<0>{}, IC<2>{});
      ldw(IC<1>{}, IC<0>{}); ldw(IC<1>{}, IC<1>{}); ldx(IC<1>{}, IC<0>{});
      GAP();
      u32 needw = 0x01010101u, off = 0;
#pragma unroll 1
      for (int c = 0; c < 8; ++c) {
        u32 fl, adv, have;
        sfor<24>([&](auto gg) AI {
          constexpr int st = gg / 6, g = gg % 6, n = g >> 1, m = g & 1;
          acc[m][n] = __builtin_amdgcn_mfma_f32_32x32x16_bf16(wf[st & 1][n], xf[st & 1][m], acc[m][n], 0, 0, 0);
          if constexpr (g == 0) { ldw(IC<st + 1>{}, IC<2>{}); ldx(IC<st + 1>{}, IC<1>{}); }
          if constexpr (g == 2) ldw(IC<st + 2>{}, IC<0>{});
          if constexpr (g == 4) ldw(IC<st + 2>{}, IC<1>{});
          if constexpr (g == 5) ldx(IC<st + 2>{}, IC<0>{});
          if constexpr (st == 0 && g == 3) { const u32 o2 = off == 2 * SLOT ? 0u : off + SLOT; adv = o2 - off; off = o2; needw += 0x01010101u; spin(adv); spin(off); spin(needw); }
          if constexpr (st == 1 && g == 1) fl = lds_ld1(FULL);
          if constexpr (st == 1 && g == 3) { xa[0] += adv; xa[1] += adv; xa[2] += adv; pin(xa[0]); pin(xa[1]); pin(xa[2]); }
          if constexpr (st == 2 && g == 0) { have = (rfl(fl) - needw) & 0x80808080u; spin(have); }
          if constexpr (st == 2 && g == 1) {
            xa[3] += adv; wa += adv; pin(xa[3]); pin(wa);
            if (__builtin_expect(have != 0u, 0)) wait_min4<0>(FULL, c + 2);
          }
          if constexpr (st == 3 && g == 1) lds_set(FREE + wave, c + 1);
          GAP();
        });
      }
      STAMP(2);
      __builtin_amdgcn_s_setprio(PRE);
      const int lane = lane_id(), s31 = lane & 31, hh = lane >> 5;
      f32x2 st[2];
      f32x4 ca[4];
      auto ldc = [&](auto n) AI { sfor<4>([&](auto g) AI { const u32 a = CS + (wn * 96 + n * 32 + 8 * g + 4 * hh) * 4; ca[g] = ldsr<f32x4>(a); }); };
#pragma unroll 1
      for (u32 it = 0; it < SPIN_CAP * 16; ++it) {
        const u32 r1 = lds_ld1(STRDY);
        asm volatile("" ::: "memory");
        const u32x2 e = lds_ld2(EPO);
        sfor<2>([&](auto m) AI { st[m] = ldsr<f32x2>(ST + (64 * wm + 32 * m + s31) * 8); });
        ldc(IC<0>{});
        asm volatile("" ::: "memory");
        epoch = ((u64)rfl(e.y) << 32) | rfl(e.x);
        if (__builtin_expect(all_ge(rfl(r1), 1), 1)) break;
      }
      STAMP(11);
      sfor<3>([&](auto n) AI {
        const int ks = 6 * cg + 3 * wn + n;
        if (ks < 86) {
          f32x4 c4[4];
          sfor<4>([&](auto g) AI { c4[g] = ca[g]; });
          f32x2 g[8], u[8], e[8];
          sfor<8>([&](auto i) AI {
            constexpr int m = i >> 2, r = 2 * (i & 3);
            const f32x2 cg2 = {c4[r >> 2][r & 3], c4[r >> 2][(r & 3) + 1]};
            g[i] = f32x2{acc[m][n][r], acc[m][n][r + 1]} - st[m].x * cg2;
          });
          GAP();
          sfor<8>([&](auto i) AI { g[i] = g[i] * st[i >> 2].y; });
          GAP();
          sfor<8>([&](auto i) AI { e[i] = g[i] * g[i]; });
          GAP();
          sfor<8>([&](auto i) AI { e[i] = e[i] * -0.1029432397f - 2.3022081985f; });
          GAP();
          sfor<8>([&](auto i) AI { e[i] = e[i] * g[i]; });
          GAP();
          sfor<8>([&](auto i) AI {
            constexpr int m = i >> 2, r = 2 * (i & 3);
            const f32x2 cu2 = {c4[2 + (r >> 2)][r & 3], c4[2 + (r >> 2)][(r & 3) + 1]};
            e[i] = f32x2{__builtin_amdgcn_exp2f(e[i].x), __builtin_amdgcn_exp2f(e[i].y)};
            u[i] = f32x2{acc[m][n][r + 8], acc[m][n][r + 9]} - st[m].x * cu2;
            GAP();
          });
          if constexpr (n < 2) ldc(IC<n + 1>{});
          sfor<8>([&](auto i) AI { e[i] = e[i] + 1.0f; });
          GAP();
          sfor<8>([&](auto i) AI {
            e[i] = f32x2{__builtin_amdgcn_rcpf(e[i].x), __builtin_amdgcn_rcpf(e[i].y)};
            g[i] = g[i] * u[i];
            GAP();
          });
          sfor<8>([&](auto i) AI { g[i] = g[i] * e[i]; });
          GAP();
          sfor<2>([&](auto m) AI {
            const u32x4 o = {pk2(g[4 * m].x, g[4 * m].y), pk2(g[4 * m + 1].x, g[4 * m + 1].y), pk2(g[4 * m + 2].x, g[4 * m + 2].y), pk2(g[4 * m + 3].x, g[4 * m + 3].y)};
            __builtin_amdgcn_raw_buffer_store_b128(o, rws, lane * 16 + W_H + ((4 * rb + 2 * wm + m) * 86 + ks) * 1024, 0, 16);
          });
          GAP();
          if constexpr (n < 2) STAMP(20 + n);
        }
      });
      lds_set(FREE + wave, 9);
      STAMP(3);
      waitv<0>();
      __builtin_amdgcn_s_setprio(PRW);
      if (lds_arrive(CNT + 0, lane) == 4 && lane == 0) __hip_atomic_store((GAS u64*)(ws + W_HF + (rb * 16 + cg) * 64), epoch, RLX_AGENT);
      STAMP(4);
    } else {
      if (csw) {
        f32x4 cs[3];
        sfor<3>([&](auto n) AI { cs[n] = f32x4{0.f, 0.f, 0.f, 0.f}; });
        const u32 o2_ = (l0 & 15) == ((l0 >> 4) & 1) ? 0x3f803f80u : 0u;
        const bf16x8 sel = __builtin_bit_cast(bf16x8, u32x4{o2_, o2_, o2_, o2_});
        wa += wm * 6144;
#pragma unroll 1
        for (int c = 0; c < 8; ++c) {
          wait_min4<1>(FULL, c + 1);
          bf16x8 w[6];
          sfor<6>([&](auto i) AI { const u32 a = wa + (i / 3) * 3072 + (i % 3) * 32; w[i] = cat(ldtr(a), ldtr(a + 768)); });
          asm volatile("" ::: "memory");
          lds_set(FREE + wave, c < 3 ? c + 1 : 3);
          sfor<6>([&](auto i) AI { cs[i % 3] = __builtin_amdgcn_mfma_f32_16x16x32_bf16(w[i], sel, cs[i % 3], 0, 0, 0); });
          wa += c == 4 ? (u32)(-4 * (int)CSLOT) : CSLOT;
        }
        if ((l0 & 15) < 2) sfor<3>([&](auto n) AI { ldsw<f32x4>(CS + (wm * 192 + wn * 96 + n * 32 + (l0 & 15) * 16 + 4 * (l0 >> 4)) * 4, cs[n]); });
      }
      lds_set(FREE + wave, 9);
      epoch = wait_epoch();
      if (csw && wave < 3) {
        wait_min4<1>(FREE, 9);
        const u32 i = 64 * wave + l0;
        const float v = ldsr<float>(CS + i * 4) + ldsr<float>(CS + 768 + i * 4);
        __hip_atomic_store((GAS u64*)(ws + W_CG + (cg * 192 + i) * 8), ((u64)(u32)(epoch >> 17) << 32) | __float_as_uint(v), RLX_AGENT);
      }
      STAMP(4);
    }
    const int lane = lane_id(), hh = lane >> 5, g16 = lane >> 4, q4 = (lane & 15) >> 2, p4 = lane & 3;
    const int kh = wave >> 1, mh = wave & 1;
    u32x4 hf[43];
    const u32 hso = W_H + ((2 * rt + mh) * 86 + 43 * kh) * 1024, hvo = lane * 16;
    f32x16 o2[2];
    sfor<2>([&](auto n) AI { sfor<16>([&](auto r) AI { o2[n][(int)r] = 0.f; }); pin(o2[n]); });
    u32 db_[2][3];
    sfor<2>([&](auto n) AI {
      const u32 da = kh * 8192 + (4 * hh + q4) * 128 + ((4 * ((int)n ^ (q4 >> 1)) + 2 * (g16 & 1) + (p4 >> 1)) << 4) + 8 * (p4 & 1);
      sfor<3>([&](auto w) AI { db_[n][w] = da + w * 61440; pin(db_[n][w]); });
    });
    bf16x8 df[3][2];
    u32 fl;
    auto ldd = [&](auto k) AI {
      constexpr int c = k >> 2, ks = k & 3;
      constexpr u32 wb = (c < 7 ? 16384u * c : c == 7 ? CONV : c == 8 ? CONV + CH_SZ : 16384u * (c - 9)) + ks * 2048;
      constexpr int w = wb / 61440 > 2 ? 2 : wb / 61440;
      sfor<2>([&](auto n) AI { const u32 a = db_[n][w] + (wb - w * 61440); df[k % 3][n] = cat(ldtr(a), ldtr(a + 1024)); });
    };
    if (wave == 0) { poll_flags<1>(ws, W_HF + (rt >> 1) * 16 * 64, 15, epoch, lane); lds_set(CVP + 6, 1); }
    else wait_byte<WSL>(CVP + 6, 1);
    __builtin_amdgcn_s_setprio(2);
    STAMP(5);
    auto hload = [&](auto k) AI { bld4_sc1<(k & 3) * 1024>(hf[k], hvo, rws, hso + (k >> 2) * 4096); };
    sfor<PD>([&](auto k) AI { hload(k); });
    wait_min4<0>(WDRDY, 1);
    STAMP(6);
    ldd(IC<0>{}); ldd(IC<1>{});
    GAP();
    sfor<43>([&](auto k) AI {
      if constexpr (k == 10) lds_set(FREE + wave, 10);
      if constexpr (k == 18) lds_set(FREE + wave, 11);
      if constexpr (k == 20 || k == 32) fl = lds_ld1(WDRDY);
      if constexpr (k == 40) fl = lds_ld1(FREE);
      if constexpr (k == 22 || k == 34) { STAMP(k == 22 ? 12 : 14); if (__builtin_expect(!all_ge(rfl(fl), k == 22 ? 2u : 3u), 0)) wait_min4<0>(WDRDY, k == 22 ? 2 : 3); STAMP(k == 22 ? 13 : 15); GAP(); }
      constexpr int younger = (k + PD <= 43 ? PD : 43 - k) - 1;
      waitv_pin<younger>(hf[k]);
      if constexpr (k == 0) STAMP(24);
      if constexpr (k == 8) STAMP(25);
      if constexpr (k == 16) STAMP(26);
      o2[0] = __builtin_amdgcn_mfma_f32_32x32x16_bf16(df[k % 3][0], __builtin_bit_cast(bf16x8, hf[k]), o2[0], 0, 0, 0);
      if constexpr (k + 2 < 43) ldd(IC<k + 2>{});
      GAP();
      o2[1] = __builtin_amdgcn_mfma_f32_32x32x16_bf16(df[k % 3][1], __builtin_bit_cast(bf16x8, hf[k]), o2[1], 0, 0, 0);
      if constexpr (k + PD < 43) hload(IC<k + PD>{});
      GAP();
    });
    STAMP(7);
    if (!all_ge(rfl(fl), 11u)) wait_min4<0>(FREE, 11);
    sfor<2>([&](auto n) AI { sfor<4>([&](auto g) AI {
      f32x4 v = {o2[n][4 * g], o2[n][4 * g + 1], o2[n][4 * g + 2], o2[n][4 * g + 3]};
      ldsw<f32x4>(PART + wave * 8192 + n * 4096 + g * 1024 + lane * 16, v);
    }); });
    waitl();
    lds_set(FREE + wave, 12);
    STAMP(8);
    DONE();
  } else {
    __builtin_amdgcn_s_setprio(PRL);
    const rsrc_t rg = mk_rsrc(mkp(ka[2], ka[3]), D * F * 2), ru = mk_rsrc(mkp(ka[4], ka[5]), D * F * 2);
    const int crot = ROT * ((rb + cg) & 1);
    auto chx = [&](int c, u32 sb) AI { dma4(sb + 4 * j * 1024, xv[0], xv[1], xv[2], xv[3], rq, (rb * 128 + 32 * j) * 1024 + (c ^ crot) * 128); };
    auto chw = [&](int c, u32 sb) AI {
      dma3(sb + G_OFF + 3 * j * 1024, wv[0], wv[1], wv[2], rg, (c ^ crot) * 64 * F * 2);
      dma3(sb + U_OFF + 3 * j * 1024, wv[0], wv[1], wv[2], ru, (c ^ crot) * 64 * F * 2);
    };
    if (has) {
      chx(0, 0); chw(0, 0); chx(1, SLOT); chw(1, SLOT);
      int ic = 2, lc = 0;
      u32 sb = 2 * SLOT;
#pragma unroll 1
      for (u32 it = 0; it < SPIN_CAP * 16; ++it) {
        if (lc < ic && vm_pending() <= 10u * (u32)(ic - lc - 1)) {
          asm volatile("" ::: "memory");
          ++lc;
          lds_set(FULL + j, lc == 8 ? 9 : lc);
          if (lc == 1) STAMP(1);
          if (lc == 8) break;
        } else if (ic < 8 && all_ge(rfl(lds_ld1(FREE)), ic - 2)) {
          asm volatile("" ::: "memory");
          chx(ic, sb);
          if (lc < ic && vm_pending() <= 10u * (u32)(ic - lc - 1) + 4u) {
            lds_set(FULL + j, ++lc);
          }
          chw(ic, sb);
          ++ic;
          sb = sb == 2 * SLOT ? 0 : sb + SLOT;
        } else {
          __builtin_amdgcn_s_sleep(LSLEEP);
        }
      }
      waitv<0>();
    } else if (csw) {
      sfor<8>([&](auto c) AI {
        if constexpr (c >= 5) { waitv<24>(); lds_set(FULL + j, (6u * c - vm_pending()) * 43u >> 8); wait_min4<1>(FREE, c - 4); }
        chw(c, (c % 5) * CSLOT - G_OFF);
        if constexpr (c >= 2) lds_set(FULL + j, (6u * (c + 1) - vm_pending()) * 43u >> 8);
      });
      waitv<12>(); lds_set(FULL + j, 6);
      waitv<6>(); lds_set(FULL + j, 7);
      waitv<0>(); lds_set(FULL + j, 8);
    }
    STAMP(2);
    const rsrc_t rd = mk_rsrc(mkp(kb[0], kb[1]), F * D * 2);
    u32 dv[4];
    sfor<4>([&](auto t) AI { dv[t] = (7 * t + l8) * 1024 + ((c8 ^ (((l8 >> 1) & 1) << 2)) << 4) + ct * 128; });
    auto wso = [&](int g) AI { return (u32)(((g >> 1) & 1) * 688 + (g >> 2) * 64 + (g & 1) * 32) * 1024u; };
    auto wd_groups = [&](int lo, int hi) AI {
#pragma unroll 1
      for (int g = lo + j; g < hi; g += 4) { dma4(g * 4096, dv[0], dv[1], dv[2], dv[3], rd, wso(g)); waitv<WDFL>(); }
    };
    u32x4 wr[5][4];
    auto wst = [&](auto t, u32 a) AI { sfor<4>([&](auto i) AI { pin(wr[t][i]); ldsw<u32x4>(a + j * 4096 + i * 1024 + lane * 16, wr[t][i]); }); };
    auto wrl = [&]() AI { sfor<5>([&](auto t) AI { const u32 so = wso(24 + 4 * t + j); sfor<4>([&](auto i) AI { bld4o<i * 1024>(wr[t][i], dv[i], rd, so); }); waitv<WRFL>(); }); };
    wrl();
    wait_min4<1>(FREE, WD1);
    wd_groups(21, 24);
    wait_min4<1>(FREE, WD2);
    wd_groups(0, 10);
    wait_min4<1>(FREE, WD3);
    wd_groups(10, 21);
    waitv<0>();
    lds_set(WDRDY + j, 1);
    STAMP(3);
    wait_min4<4>(CVP, 5);
    wst(IC<0>{}, XCH); wst(IC<1>{}, CONV); wst(IC<2>{}, CONV + CH_SZ);
    waitl();
    lds_set(WDRDY + j, 2);
    STAMP(4);
    wait_min4<2>(FREE, 10);
    wst(IC<3>{}, 0); wst(IC<4>{}, 16384);
    waitl();
    lds_set(WDRDY + j, 3);
    STAMP(5);
    DONE();
  }
}

extern "C" void kernel_launch(void* const* d_in, const int* in_sizes, int n_in, void* d_out, int out_size, void* d_ws, size_t ws_size,
                              hipStream_t stream) {
  Args a;
  a.q = (const u16*)d_in[0]; a.k = (const u16*)d_in[1]; a.v = (const u16*)d_in[2]; a.sk = (const u16*)d_in[3];
  a.h = (const u16*)d_in[4]; a.dv = (const u16*)d_in[5];
  a.wg = (const u16*)d_in[7]; a.wu = (const u16*)d_in[8]; a.wd = (const u16*)d_in[9];
  a.out = (u16*)d_out; a.ws = (char*)d_ws; a.pad = 0;
  (void)hipFuncSetAttribute((const void*)fused, hipFuncAttributeMaxDynamicSharedMemorySize, LDS_BYTES);
  fused<<<256, 768, LDS_BYTES, stream>>>(a);
}
```

```cpp
#include <hip/hip_runtime.h>
#include <stdint.h>
#include <utility>

typedef unsigned short u16;
typedef unsigned int u32;
typedef unsigned long long u64;
typedef __bf16 bf16x8 __attribute__((ext_vector_type(8)));
typedef __bf16 bf16x2 __attribute__((ext_vector_type(2)));
typedef short s16x4 __attribute__((ext_vector_type(4)));
typedef short s16x8 __attribute__((ext_vector_type(8)));
typedef float f32x16 __attribute__((ext_vector_type(16)));
typedef float f32x4 __attribute__((ext_vector_type(4)));
typedef float f32x2 __attribute__((ext_vector_type(2)));
typedef u32 u32x4 __attribute__((ext_vector_type(4)));
typedef u32 u32x2 __attribute__((ext_vector_type(2)));
typedef __amdgpu_buffer_rsrc_t rsrc_t;
#define DI __device__ __forceinline__
#define AI __attribute__((always_inline))
#define LAS __attribute__((address_space(3)))
#define GAS __attribute__((address_space(1)))
#define RLX_AGENT __ATOMIC_RELAXED, __HIP_MEMORY_SCOPE_AGENT
#define RLX_WG __ATOMIC_RELAXED, __HIP_MEMORY_SCOPE_WORKGROUP

constexpr int S = 2048, D = 512, F = 1376;
#define UPAD 32
#define SLOTB (40960 + UPAD)
constexpr u32 SLOT = SLOTB, G_OFF = 16384, U_OFF = 16384 + 12288 + UPAD;
constexpr u32 CSLOT = 2 * (12288 + UPAD) + 64 - 2 * UPAD % 64;
constexpr u32 CONV = 3 * 41216;
constexpr u32 CH_SZ = 17520, HA = 64, HB = 4292, KA = 8400, KB = 12976, XB = 4304;
constexpr u32 MISC = CONV + 2 * CH_SZ;
constexpr u32 CS = MISC, ST = MISC + 1536, CNT = MISC + 2560;
constexpr u32 CKV = CNT + 8, CSP = CNT + 12, EPO = CNT + 16, CVP = CNT + 24, FULL = CNT + 32, FREE = CNT + 36, STRDY = CNT + 40, WDRDY = CNT + 44;
constexpr u32 LDS_BYTES = MISC + 2816;
static_assert(LDS_BYTES <= 163840, "LDS");
constexpr u32 XCH = 98304, ZST = 114688, PART = 32768;
constexpr u32 W_E = 0, W_PF = 4096, W_HF = W_PF + 16384, W_ZF = W_HF + 16384, W_SG = W_ZF + 16384, W_CG = W_SG + 16384;
constexpr u32 W_KV = 131072, W_ZT = W_KV + 2097152 + 4096, W_H = W_ZT + 4194304, W_END = W_H + 64 * 86 * 1024;
constexpr u32 W_DBG = 16u << 20;
constexpr u32 CODE_BYTES = 21760;
#define PRL 1
#define PRS 0
#define CONVAFTER 17
#define LSLEEP 1
#define PRA 0
#define ROT 5
#define S2AFTER 16
#define WDFL 4
#define KVSTAG 0
#define SSTOP 9
#define POLLS 4
#define PD 10
#define WD1 12
#define WD2 14
#define PFLO 8
#define PFPRIO 3
#define PFWAIT 0
#define KVMODE 0
#define WD3 16
#define PRE 2
#define PRW 0
#define WSL 1
#define PRC 3
#define ZSLEEP 4
#define ZAFTER 19
#define WRFL 40
#define PFM 0
#define PFS 2
#define PFSAFTER 0
#define PFK 2
#define TA0 8
#define TS0 8
#define KVB 0
#define KVS 1
#define KVPRIO 0
#define KVE 1
#define WDA 24
#define SPLIT 0
#define WFLAG 0
#define KVL 1
#define PFLATE 0
#define KVAFTER 2
constexpr u32 SPIN_CAP = 1u << 18;

constexpr int cpair(int g) {
  int i = 0;
  for (int n = 0; n < 8; ++n) for (int m = 0; m <= n; ++m) if (m + n <= 7) {
    if (i++ == g) return n * 8 + m;
    if (m < n && i++ == g) return m * 8 + n;
  }
  return 0;
}
template <class Fn, int... I> DI void sfor_impl(Fn&& f, std::integer_sequence<int, I...>) { (f(std::integral_constant<int, I>{}), ...); }
template <int N, class Fn> DI void sfor(Fn&& f) { sfor_impl(f, std::make_integer_sequence<int, N>{}); }

DI rsrc_t mk_rsrc(const void* p, u32 bytes) { return __builtin_amdgcn_make_buffer_rsrc((void*)p, (short)0, (int)bytes, 0x00020000); }
DI void dma(u32 lds, u32 voff, rsrc_t r, u32 soff) {
  asm volatile("s_mov_b32 m0, %0\n\ts_nop 0\n\tbuffer_load_dwordx4 %1, %2, %3 offen lds" ::"s"(lds), "v"(voff), "s"(r), "s"(soff) : "memory");
}

DI void dma4(u32 lds, u32 v0, u32 v1, u32 v2, u32 v3, rsrc_t r, u32 soff) {
  asm volatile("s_mov_b32 m0, %0\n\ts_nop 0\n\tbuffer_load_dwordx4 %1, %5, %6 offen lds\n\tbuffer_load_dwordx4 %2, %5, %6 offen offset:1024 lds\n\t"
               "buffer_load_dwordx4 %3, %5, %6 offen offset:2048 lds\n\tbuffer_load_dwordx4 %4, %5, %6 offen offset:3072 lds"
               ::"s"(lds), "v"(v0), "v"(v1), "v"(v2), "v"(v3), "s"(r), "s"(soff) : "memory");
}
DI void dma3(u32 lds, u32 v0, u32 v1, u32 v2, rsrc_t r, u32 soff) {
  asm volatile("s_mov_b32 m0, %0\n\ts_nop 0\n\tbuffer_load_dwordx4 %1, %4, %5 offen lds\n\tbuffer_load_dwordx4 %2, %4, %5 offen offset:1024 lds\n\t"
               "buffer_load_dwordx4 %3, %4, %5 offen offset:2048 lds"
               ::"s"(lds), "v"(v0), "v"(v1), "v"(v2), "s"(r), "s"(soff) : "memory");
}
template <int OFF> DI void bld4_sc1(u32x4& d, u32 voff, rsrc_t r, u32 soff) {
  asm volatile("buffer_load_dwordx4 %0, %1, %2, %3 offen offset:%4 sc1" : "=v"(d) : "v"(voff), "s"(r), "s"(soff), "i"(OFF));
}
template <int OFF> DI void bld4o(u32x4& d, u32 voff, rsrc_t r, u32 soff) { asm volatile("buffer_load_dwordx4 %0, %1, %2, %3 offen offset:%4" : "=v"(d) : "v"(voff), "s"(r), "s"(soff), "i"(OFF)); }
DI void bld4(u32x4& d, u32 voff, rsrc_t r, u32 soff) { asm volatile("buffer_load_dwordx4 %0, %1, %2, %3 offen" : "=v"(d) : "v"(voff), "s"(r), "s"(soff)); }
template <int OFF> DI void bld2(u32x2& d, u32 voff, rsrc_t r) { asm volatile("buffer_load_dwordx2 %0, %1, %2, 0 offen offset:%3" : "=v"(d) : "v"(voff), "s"(r), "i"(OFF)); }
template <int OFF> DI void gld4_sc1(u32x4& d, u32 voff, const char* base) {
  asm volatile("global_load_dwordx4 %0, %1, %2 offset:%3 sc1" : "=v"(d) : "v"(voff), "s"(base), "i"(OFF));
}
template <int N> DI void waitv_pin(u32x4& d) { asm volatile("s_waitcnt vmcnt(%1)" : "+v"(d) : "i"(N)); }
template <int N> DI void waitv() { asm volatile("s_waitcnt vmcnt(%0)" ::"i"(N) : "memory"); }
DI u64 next_epoch(u64 e) {
  e += 0x9E3779B97F4A7C15ull;
  e = (e ^ (e >> 30)) * 0xBF58476D1CE4E5B9ull; e = (e ^ (e >> 27)) * 0x94D049BB133111EBull;
  return (e ^ (e >> 31)) | 1ull;
}
DI u32 vm_pending() { u32 r; asm volatile("s_getreg_b32 %0, hwreg(HW_REG_IB_STS)" : "=s"(r)::"memory"); return (r & 15u) | ((r >> 18) & 48u); }
DI void bar() { asm volatile("s_waitcnt lgkmcnt(0)\n\ts_barrier" ::: "memory"); }
DI void waitl() { asm volatile("s_waitcnt lgkmcnt(0)" ::: "memory"); }
DI float bflo(u32 x) { return __uint_as_float(x << 16); }
DI float bfhi(u32 x) { return __uint_as_float(x & 0xffff0000u); }
DI u32 pk2(float lo, float hi) { f32x2 v = {lo, hi}; return __builtin_bit_cast(u32, __builtin_convertvector(v, bf16x2)); }
DI u32 pk2s(float lo, float hi) { asm("" : "+v"(lo)); asm("" : "+v"(hi)); return pk2(lo, hi); }
DI float rbf(float x) { return bflo(pk2(x, x)); }
template <class T> DI T ldsr(u32 a) { return *(const LAS T*)a; }
template <class T> DI void ldsw(u32 a, T v) { *(LAS T*)a = v; }
DI u32x2 ldtr(u32 a) { return __builtin_bit_cast(u32x2, __builtin_amdgcn_ds_read_tr16_b64_v4i16((LAS s16x4*)a)); }
DI bf16x8 cat(u32x2 a, u32x2 b) { u32x4 v = {a.x, a.y, b.x, b.y}; return __builtin_bit_cast(bf16x8, v); }
template <int CTRL, int RM> DI float dpp_add(float v) {
  return v + __builtin_bit_cast(float, __builtin_amdgcn_update_dpp(0, __builtin_bit_cast(int, v), CTRL, RM, 0xf, false));
}
DI float wave_sum(float v) {
  v = dpp_add<0x111, 0xf>(v); v = dpp_add<0x112, 0xf>(v); v = dpp_add<0x114, 0xf>(v); v = dpp_add<0x118, 0xf>(v);
  v = dpp_add<0x142, 0xa>(v); v = dpp_add<0x143, 0xc>(v);
  return __builtin_bit_cast(float, __builtin_amdgcn_readlane(__builtin_bit_cast(int, v), 63));
}
DI void wave_sum2(float& a, float& c) {
  a = dpp_add<0x111, 0xf>(a); c = dpp_add<0x111, 0xf>(c); a = dpp_add<0x112, 0xf>(a); c = dpp_add<0x112, 0xf>(c);
  a = dpp_add<0x114, 0xf>(a); c = dpp_add<0x114, 0xf>(c); a = dpp_add<0x118, 0xf>(a); c = dpp_add<0x118, 0xf>(c);
  a = dpp_add<0x142, 0xa>(a); c = dpp_add<0x142, 0xa>(c); a = dpp_add<0x143, 0xc>(a); c = dpp_add<0x143, 0xc>(c);
  a = __builtin_bit_cast(float, __builtin_amdgcn_readlane(__builtin_bit_cast(int, a), 63));
  c = __builtin_bit_cast(float, __builtin_amdgcn_readlane(__builtin_bit_cast(int, c), 63));
}
DI void gld8_sc1(u64& d, const char* p) { asm volatile("global_load_dwordx2 %0, %1, off sc1" : "=v"(d) : "v"(p)); }
DI void gld_u16(u32& d, u32 voff, const char* base) { asm volatile("global_load_ushort %0, %1, %2" : "=v"(d) : "v"(voff), "s"(base)); }
template <int SLEEP> DI void poll_flags(const char* ws, u32 off, int n, u64 epoch, int lane) {
  const GAS u64* p = (const GAS u64*)(ws + off + (u32)(lane < n ? lane : 0) * 64u);
#pragma unroll 1
  for (u32 it = 0; it < SPIN_CAP; ++it) {
    u64 v = __hip_atomic_load(p, RLX_AGENT);
    if (__all(v == epoch)) break;
    __builtin_amdgcn_s_sleep(SLEEP);
  }
  asm volatile("" ::: "memory");
}
DI u32 poll_granule(const char* ws, u32 row, u32 tag) {
  const GAS u64* p = (const GAS u64*)(ws + W_SG + row * 8u);
  u64 v = 0;
#pragma unroll 1
  for (u32 it = 0; it < SPIN_CAP; ++it) {
    v = __hip_atomic_load(p, RLX_AGENT);
    if (__all((u32)(v >> 32) == tag)) break;
    __builtin_amdgcn_s_sleep(1);
  }
  return (u32)v;
}
DI u32 lds_arrive(u32 a, int lane) {
  u32 r = 0;
  waitl();
  if (lane == 0) r = __hip_atomic_fetch_add((LAS u32*)a, 1u, RLX_WG);
  return (u32)__builtin_amdgcn_readfirstlane(r) + 1u;
}
DI void lds_wait(u32 a, u32 want) {
#pragma unroll 1
  for (u32 it = 0; it < SPIN_CAP * 16; ++it) {
    u32 v = __hip_atomic_load((LAS u32*)a, RLX_WG);
    if ((u32)__builtin_amdgcn_readfirstlane(v) >= want) break;
  }
  asm volatile("" ::: "memory");
}

DI int lane_id() { int l; asm volatile("v_mbcnt_lo_u32_b32 %0, -1, 0\n\tv_mbcnt_hi_u32_b32 %0, -1, %0" : "=v"(l)); return l; }
DI u32 rfl(u32 x) { return (u32)__builtin_amdgcn_readfirstlane(x); }
DI u32x2 lds_ld2(u32 a) { return __builtin_bit_cast(u32x2, __hip_atomic_load((LAS u64*)a, RLX_WG)); }
template <int SLEEP> DI void wait_min2(u32 a, u32 want) {
#pragma unroll 1
  for (u32 it = 0; it < SPIN_CAP * 16; ++it) {
    const u32x2 v = lds_ld2(a);
    if (rfl(min(v.x, v.y)) >= want) break;
    if (SLEEP) __builtin_amdgcn_s_sleep(SLEEP);
  }
  asm volatile("" ::: "memory");
}
DI u32 lds_ld1(u32 a) { return __hip_atomic_load((LAS u32*)a, RLX_WG); }
DI bool all_ge(u32 w, u32 want) { return ((w - want * 0x01010101u) & 0x80808080u) == 0u; }
template <int SLEEP> DI void wait_min4(u32 a, u32 want) {
#pragma unroll 1
  for (u32 it = 0; it < SPIN_CAP * 16; ++it) {
    if (all_ge(rfl(lds_ld1(a)), want)) break;
    if (SLEEP) __builtin_amdgcn_s_sleep(SLEEP);
  }
  asm volatile("" ::: "memory");
}
template <int SLEEP> DI void wait_byte(u32 a, u32 want) {
#pragma unroll 1
  for (u32 it = 0; it < SPIN_CAP * 16; ++it) {
    if (rfl(__hip_atomic_load((LAS unsigned char*)a, RLX_WG)) >= want) break;
    if (SLEEP) __builtin_amdgcn_s_sleep(SLEEP);
  }
  asm volatile("" ::: "memory");
}
DI u64 wait_epoch() {
  u32x2 e;
#pragma unroll 1
  for (u32 it = 0; it < SPIN_CAP * 16; ++it) {
    e = lds_ld2(EPO);
    if (rfl(e.x) != 0u) break;
    __builtin_amdgcn_s_sleep(2);
  }
  return ((u64)rfl(e.y) << 32) | rfl(e.x);
}
DI u64 gld_agent(const char* p) { return __hip_atomic_load((const GAS u64*)p, RLX_AGENT); }
DI void lds_set(u32 a, u32 v) { __hip_atomic_store((LAS unsigned char*)a, (unsigned char)v, RLX_WG); }
DI void gather(u32& junk, u32 voff, rsrc_t r, u32 soff) { asm volatile("buffer_load_dword %0, %1, %2, %3 offen" : "=v"(junk) : "v"(voff), "s"(r), "s"(soff)); }
template <int N> using IC = std::integral_constant<int, N>;

#define DONE() do {} while (0)
#define STAMP(id) do {} while (0)

struct Args { const u16 *q, *wg, *wu, *k, *v, *sk, *h, *dv, *wd; u16* out; char* ws; u64 pad; };
typedef u32 u32x16 __attribute__((ext_vector_type(16)));
typedef u32 u32x8 __attribute__((ext_vector_type(8)));
DI char* mkp(u32 lo, u32 hi) { return (char*)(((u64)hi << 32) | lo); }
#define GAP() __builtin_amdgcn_sched_barrier(0)
DI void barx() { asm volatile("s_barrier" ::: "memory"); }
template <class T> DI void spin(T& x) { asm volatile("" : "+s"(x)); }
template <class T> DI void pin(T& x) { asm volatile("" : "+v"(x)); }

__global__ void __launch_bounds__(768, 3) fused(Args) {
  u64 pc0; asm volatile("s_getpc_b64 %0" : "=s"(pc0));
  u32x16 ka; u32x8 kb;
  asm volatile("s_load_dwordx16 %0, %2, 0x0\n\ts_load_dwordx8 %1, %2, 0x40" : "=&s"(ka), "=&s"(kb) : "s"(__builtin_amdgcn_kernarg_segment_ptr()));
  GAP();
  const int tid = threadIdx.x, lane = tid & 63;
  const int wave = __builtin_amdgcn_readfirstlane(tid >> 6);
  const int b = blockIdx.x, xcd = b & 7, li = b >> 3;
  if (tid < 16) ldsw<u32>(CNT + tid * 4, 0u);
  const bool has = li < 30;
  const int id = 30 * xcd + (has ? li : 0), rr_ = id % 120, rb = 8 * (id / 120) + (rr_ & 7), cg = rr_ >> 3;
  const int rt = 4 * xcd + (li & 3), ct = li >> 2;
  const int j = wave & 3, l8 = lane >> 3, c8 = lane & 7;
  u32 xv[4], wv[3];
  sfor<4>([&](auto t) AI { xv[t] = (7 * t + l8) * 1024 + ((c8 ^ ((4 * t + (l8 >> 1)) & 7)) << 4); pin(xv[t]); });
  sfor<3>([&](auto t) AI {
    const u32 byte = t * 1024 + lane * 16, row = byte / 192u;
    const u32 seg = byte - row * 192u;
    wv[t] = cg * 192 + seg >= F * 2 ? 0x40000000u : (16 * (j & 1) + row) * (F * 2) + cg * 192 + seg - t * 1024;
    pin(wv[t]);
  });
  asm volatile("s_waitcnt lgkmcnt(0)\n\ts_barrier" : "+s"(ka), "+s"(kb)::"memory");
  STAMP(9);
  char* const ws = mkp(kb[4], kb[5]);
  const rsrc_t rq = mk_rsrc(mkp(ka[0], ka[1]), S * D * 2);
  if (wave >= 8) {
    __builtin_amdgcn_s_setprio(3);
    u64 epoch;
    asm volatile("s_load_dwordx2 %0, %1, 0x0" : "=s"(epoch) : "s"(ws));
    const int lane = lane_id(), l8 = lane >> 3, c8 = lane & 7;
    const int ch = j & 1, cp = j >> 1, c16 = lane & 15, q = lane >> 4;
    const rsrc_t rws = mk_rsrc(ws, W_END);
    const u32 cb = CONV + ch * CH_SZ, hown = cb + (cp ? XB : HA), kown = cb + (cp ? KB : KA);
    const int pa = b & 31, pg = b >> 5;
    const int srow = 256 * xcd + 8 * li + 2 * j, dch = 64 * pg + 16 * j + c16;
    u32x4 sx[2];
    u32x2 skw;
    u32 dvv;
    sfor<2>([&](auto r) AI { bld4(sx[r], lane * 16, rq, (srow + r) * 1024); });
    wait_min4<1>(FULL, 1);
    {
      const rsrc_t rk = mk_rsrc(mkp(ka[6], ka[7]), S * D * 2), rv = mk_rsrc(mkp(ka[8], ka[9]), S * D * 2);
      const u32 kvv = l8 * 1024 + c8 * 16 + pg * 128;
      const int r0 = 64 * pa - 8;
      dma(CONV + j * 1024, kvv, rk, max(r0 + 8 * j, 0) * 1024);
      dma(CONV + (j + 4) * 1024, kvv, rk, (r0 + 8 * j + 32) * 1024);
      if (j == 0) dma(CONV + 8 * 1024, kvv, rk, (r0 + 64) * 1024);
      dma(CONV + (9 + j) * 1024, kvv, rv, (64 * pa + 8 * j) * 1024);
      dma(CONV + (13 + j) * 1024, kvv, rv, (64 * pa + 8 * j + 32) * 1024);
      bld2<0>(skw, dch * 8, mk_rsrc(mkp(ka[10], ka[11]), D * 8));
      gld_u16(dvv, (2 * b + ch) * 2, mkp(ka[14], ka[15]));
    }
    asm volatile("s_waitcnt lgkmcnt(0)" : "+s"(epoch));
    epoch = next_epoch(epoch);
    const u32 tag = (u32)(epoch >> 17);
    STAMP(22);
    barx();
    asm volatile("s_waitcnt vmcnt(6)" : "+v"(sx[0]), "+v"(sx[1]));
    __builtin_amdgcn_s_setprio(PRS);
    {
      float x[2][8], mean[2], var[2];
      sfor<2>([&](auto r) AI {
        sfor<4>([&](auto i) AI { x[r][2 * i] = bflo(sx[r][(int)i]); x[r][2 * i + 1] = bfhi(sx[r][(int)i]); });
        mean[r] = x[r][0];
        sfor<7>([&](auto i) AI { mean[r] += x[r][i + 1]; });
      });
      wave_sum2(mean[0], mean[1]);
      sfor<2>([&](auto r) AI {
        mean[r] *= 1.0f / D; var[r] = 0.f;
        sfor<8>([&](auto i) AI { float d = x[r][i] - mean[r]; asm("" : "+v"(d)); var[r] += d * d; });
      });
      wave_sum2(var[0], var[1]);
      const float mn = lane ? mean[1] : mean[0], vr = rbf((lane ? var[1] : var[0]) * (1.0f / D));
      const u32 g = pk2s(mn, __builtin_amdgcn_sqrtf(rbf(vr + 1e-5f)));
      if (lane < 2) __hip_atomic_store((GAS u64*)(ws + W_SG + (srow + lane) * 8), ((u64)tag << 32) | g, RLX_AGENT);
    }
    if (KVL) barx();
    asm volatile("s_waitcnt vmcnt(1)" : "+v"(skw), "+v"(dvv)::"memory");
    STAMP(2);
    barx();
    {
      const float w0 = bflo(skw.x), w1 = bfhi(skw.x), w2 = bflo(skw.y), w3 = bfhi(skw.y);
#pragma unroll 1
      for (int oo = 0; oo < 2; ++oo) {
        const int o = q + 4 * oo;
        const bool first = pa == 0 && o == 0;
        const u32 a = CONV + (8 * o + (c16 >> 2)) * 128 + 32 * j + 8 * (lane & 3);
        const u32x2 k0 = ldtr(a + 512), k1 = ldtr(a + 1024), k2 = ldtr(a + 1536), v0 = ldtr(a + 9216), v1 = ldtr(a + 9728);
        u32x4 ov;
        const float kk[11] = {first ? 0.f : bfhi(k0.x), first ? 0.f : bflo(k0.y), first ? 0.f : bfhi(k0.y), bflo(k1.x), bfhi(k1.x), bflo(k1.y), bfhi(k1.y), bflo(k2.x), bfhi(k2.x), bflo(k2.y), bfhi(k2.y)};
        const float vv[8] = {bflo(v0.x), bfhi(v0.x), bflo(v0.y), bfhi(v0.y), bflo(v1.x), bfhi(v1.x), bflo(v1.y), bfhi(v1.y)};
        u32 a4[4], m4[4];
        sfor<4>([&](auto t) AI { a4[t] = pk2s(w3 * kk[2 * t], w3 * kk[2 * t + 1]); m4[t] = pk2s(w2 * kk[2 * t + 1], w2 * kk[2 * t + 2]); });
        GAP();
        sfor<4>([&](auto t) AI { a4[t] = pk2s(bflo(a4[t]) + bflo(m4[t]), bfhi(a4[t]) + bfhi(m4[t])); m4[t] = pk2s(w1 * kk[2 * t + 2], w1 * kk[2 * t + 3]); });
        GAP();
        if (SPLIT) { sfor<4>([&](auto t) AI { pin(a4[t]); pin(m4[t]); }); barx(); }
        sfor<4>([&](auto t) AI { a4[t] = pk2s(bflo(a4[t]) + bflo(m4[t]), bfhi(a4[t]) + bfhi(m4[t])); m4[t] = pk2s(w0 * kk[2 * t + 3], w0 * kk[2 * t + 4]); });
        GAP();
        sfor<4>([&](auto t) AI { a4[t] = pk2s(bflo(a4[t]) + bflo(m4[t]), bfhi(a4[t]) + bfhi(m4[t])); });
        GAP();
        sfor<4>([&](auto t) AI { ov[3 - t] = pk2s(bfhi(a4[t]) * vv[2 * t + 1], bflo(a4[t]) * vv[2 * t]); });
        __builtin_amdgcn_s_setprio(3);
        __builtin_amdgcn_raw_buffer_store_b128(ov, rws, W_KV + dch * 4096 + (2040 - 64 * pa - 8 * o) * 2, 0, 16);
        __builtin_amdgcn_s_setprio(PRS);
        STAMP(16 + oo);
        barx();
      }
    }
    __builtin_amdgcn_s_setprio(3);
    const int mh = j & 1, db = j >> 1, s31 = lane & 31, hh = lane >> 5;
    const int orow = 64 * rt + 32 * mh + s31, ocol = 64 * ct + 32 * db;
    const char* p1 = ws + W_SG + (hh ? orow : rb * 128 + 32 * j + s31) * 8;
    u64 g1;
    u32 jv;
    auto pfs = [&]() AI {
      gather(jv, j & 1 ? b * 8192 + lane * 128 : (b * 43 + min(lane, 42)) * 128,
             mk_rsrc(j & 1 ? mkp(ka[12], ka[13]) : mkp(kb[0], kb[1]), j & 2 ? 0u : j & 1 ? D * S * 2 : F * D * 2), 0);
    };
    if (!SPLIT) {
      if (!KVL) barx();
      gld8_sc1(g1, p1);
      waitv<1>();
      STAMP(1);
      barx();
      if (j == 0 && lane == 0) __hip_atomic_store((GAS u64*)(ws + W_PF + b * 64), epoch, RLX_AGENT);
      pfs();
      asm volatile("s_waitcnt vmcnt(1)" : "+v"(g1)::"memory");
      STAMP(3);
      barx();
    } else {
      gld8_sc1(g1, p1);
      pfs();
      asm volatile("s_waitcnt vmcnt(1)" : "+v"(g1)::"memory");
      STAMP(3);
      barx();
      if (j == 0 && lane == 0) __hip_atomic_store((GAS u64*)(ws + W_PF + b * 64), epoch, RLX_AGENT);
    }
#pragma unroll 1
    for (u32 it = 0; it < SPIN_CAP; ++it) {
      if (__all((u32)(g1 >> 32) == tag)) break;
      g1 = gld_agent(p1);
    }
    u32 sg = (u32)g1;
    if (lane < 32) ldsw<f32x2>(ST + (32 * j + lane) * 8, f32x2{bflo(sg), __builtin_amdgcn_rcpf(bfhi(sg))});
    if (lane < (cp ? 28 : 30)) ldsw<u32x4>(kown + 4096 + lane * 16, u32x4{0u, 0u, 0u, 0u});
    if (lane < (cp ? 33 : 16)) ldsw<u32>(cb + (cp ? 4160 : 0) + lane * 4, 0u);
    sg = (u32)__builtin_amdgcn_permlane32_swap(sg, sg, false, false)[1];
    STAMP(4);
    waitl();
    barx();
    STAMP(24);
    u32x2 xr[4];
    {
      u64 f;
      gld8_sc1(f, ws + W_PF + ((b >> 5) * 32 + s31) * 64);
      dma4(cb + (cp ? HB : HA), lane * 16, lane * 16, lane * 16, lane * 16, mk_rsrc(mkp(ka[12], ka[13]), D * S * 2), (2 * b + ch) * 4096);
      sfor<2>([&](auto t) AI {
        const int r = 16 * t + (lane >> 2);
        dma(ZST + j * 2048 + t * 1024, r * 1024 + (((lane & 3) ^ ((r >> 1) & 3)) << 4), rq, (64 * rt + 32 * mh) * 1024 + ocol * 2);
      });
      asm volatile("s_waitcnt vmcnt(6)" : "+v"(f), "+v"(jv));
      if (!__all(f == epoch)) poll_flags<2>(ws, W_PF + (b >> 5) * 32 * 64, 32, epoch, lane);
      u32x4 kvr[4];
      const char* src = ws + W_KV + (2 * b + ch) * 4096 + 2 * cp;
      sfor<4>([&](auto t) AI { gld4_sc1<t * 1024>(kvr[t], lane * 16, src); });
      asm volatile("s_waitcnt vmcnt(0)" : "+v"(kvr[0]), "+v"(kvr[1]), "+v"(kvr[2]), "+v"(kvr[3])::"memory");
      if (cp && lane == 63) kvr[3].w &= 0xffffu;
      sfor<4>([&](auto t) AI { ldsw<u32x4>(kown + t * 1024 + lane * 16, kvr[t]); });
      sfor<4>([&](auto g) AI { xr[g] = ldsr<u32x2>(ZST + j * 2048 + s31 * 64 + ((g ^ ((s31 >> 1) & 3)) << 4) + 8 * hh); });
      waitl();
      pin(xr[0]); pin(xr[1]); pin(xr[2]); pin(xr[3]);
      lds_set(CVP + j, 2);
      STAMP(5);
    }
    __builtin_amdgcn_s_setprio(0);
    wait_min4<1>(FREE, CONVAFTER);
    wait_byte<0>(CVP + (j ^ 2), 2);
    __builtin_amdgcn_s_setprio(PRC);
    STAMP(6);
    f32x4 dacc[8];
    sfor<8>([&](auto i) AI { dacc[i] = f32x4{0.f, 0.f, 0.f, 0.f}; });
    u32 kv2[8];
    {
      u32 hb = cb + ((c16 & 1) ? HA : HB) + 4 * (c16 - 15) + 16 * q + 256 * cp;
      u32 kb_ = cb + ((c16 & 1) ? KA : KB) + 448 - 64 * (c16 >> 1) + 16 * q + 256 * cp;
      u32 hb2 = hb + 8;
      pin(hb); pin(hb2); pin(kb_);
      bf16x8 ta[12], kf[12];
      auto ld1 = [&](auto odd, auto n, auto w, auto off) AI {
        constexpr int r = (n < 4 && odd) ? 8 + n : n;
        if constexpr (w == 0) kf[r] = ldsr<bf16x8>(kb_ + 512 * (7 - n) + off);
        if constexpr (w == 1) { const u32x2 v = ldsr<u32x2>(hb + 512 * n + off); u32x4 t = __builtin_bit_cast(u32x4, ta[r]); t.x = v.x; t.y = v.y; ta[r] = __builtin_bit_cast(bf16x8, t); }
        if constexpr (w == 2) { const u32x2 v = ldsr<u32x2>(hb2 + 512 * n + off); u32x4 t = __builtin_bit_cast(u32x4, ta[r]); t.z = v.x; t.w = v.y; ta[r] = __builtin_bit_cast(bf16x8, t); }
      };
      auto cstep = [&](auto odd, auto off) AI {
        sfor<36>([&](auto g) AI {
          constexpr int c = cpair(35 - g) >> 3, dl = cpair(35 - g) & 7;
          constexpr int rc = (c < 4 && odd) ? 8 + c : c, rd_ = (dl < 4 && odd) ? 8 + dl : dl;
          dacc[c + dl] = __builtin_amdgcn_mfma_f32_16x16x32_bf16(kf[rc], ta[rd_], dacc[c + dl], 0, 0, 0);
          if constexpr (g < 12) ld1(IC<1 - odd>{}, IC<g / 3>{}, IC<g % 3>{}, off);
          else if constexpr (g < 24) ld1(IC<1 - odd>{}, IC<7 - (g - 12) / 3>{}, IC<g % 3>{}, off);
          GAP();
        });
      };
      sfor<24>([&](auto i) AI { ld1(IC<0>{}, IC<(i < 3 ? 0 : i < 6 ? 7 : i < 9 ? 1 : i < 12 ? 6 : i < 15 ? 2 : i < 18 ? 5 : i < 21 ? 3 : 4)>{}, IC<i % 3>{}, IC<0>{}); GAP(); });
#pragma unroll 1
      for (int i = 0; i < 2; ++i) {
        cstep(IC<0>{}, IC<64>{});
        cstep(IC<1>{}, IC<128>{});
        hb += 128; hb2 += 128; kb_ += 128;
      }
    }
    __builtin_amdgcn_s_setprio(3);
    STAMP(25);
    sfor<4>([&](auto i) AI { ldsw<f32x4>(XCH + j * 4096 + i * 1024 + lane * 16, cp ? dacc[i] : dacc[4 + i]); });
    lds_set(CVP + j, 4);
    {
      const int l = lane_id();
      u32 a = cb + KA + 2 * (2046 - 2 * (l & 15) - 64 * (l >> 4) - 1024 * cp) - 1600;
      pin(a);
      sfor<8>([&](auto i) AI { kv2[i] = ldsr<u32>(a + 64 * (1 - (i & 1)) + 512 * (3 - (i >> 1))); });
    }
    STAMP(7);
    {
      const float dv = bflo(dvv);
      sfor<8>([&](auto i) AI { kv2[i] = pk2s(bfhi(kv2[i]) * dv, bflo(kv2[i]) * dv); pin(kv2[i]); });
      wait_byte<0>(CVP + (j ^ 2), 4);
      f32x4 o[4];
      sfor<4>([&](auto i) AI { o[i] = ldsr<f32x4>(XCH + (j ^ 2) * 4096 + i * 1024 + lane * 16); });
      asm volatile("" ::: "memory");
      lds_set(CVP + j, 5);
      sfor<4>([&](auto i) AI {
        const f32x4 m = cp ? dacc[4 + i] : dacc[i];
        sfor<2>([&](auto rr) AI {
          const u32 yb = pk2s(m[2 * rr] + o[i][2 * rr], m[2 * rr + 1] + o[i][2 * rr + 1]), kd = kv2[2 * i + rr];
          ldsw<u32>(ZST + j * 2048 + 4 * c16 + 128 * q + 64 * rr + 512 * i, pk2s(bflo(yb) + bflo(kd), bfhi(yb) + bfhi(kd)));
        });
      });
      waitl();
      u32x2 zo[4];
      sfor<4>([&](auto t) AI { zo[t] = ldsr<u32x2>(ZST + j * 2048 + t * 512 + lane * 8); });
      sfor<4>([&](auto t) AI {
        __builtin_amdgcn_raw_buffer_store_b128(u32x4{zo[t].x, tag, zo[t].y, tag}, rws, lane * 16 + W_ZT + (2 * b + ch) * 8192 + cp * 4096 + t * 1024, 0, 16);
      });
    }
    STAMP(8);
    const float mean = bflo(sg), rs = __builtin_amdgcn_rcpf(bfhi(sg));
    u32 xn[8];
    sfor<8>([&](auto i) AI {
      const u32 x2 = xr[i >> 1][i & 1], n2 = pk2s(bflo(x2) - mean, bfhi(x2) - mean);
      xn[i] = pk2s(bflo(n2) * rs, bfhi(n2) * rs);
      pin(xn[i]);
    });
    if (b == 0 && j == 3) {
      sfor<4>([&](auto t) AI { poll_flags<1>(ws, W_PF + t * 4096, 64, epoch, lane); });
      if (lane == 0) __hip_atomic_store((GAS u64*)(ws + W_E), epoch, RLX_AGENT);
    }
    wait_min4<2>(FREE, ZAFTER);
    u32x4 zr[4];
#pragma unroll 1
    for (u32 it = 0; it < SPIN_CAP; ++it) {
      sfor<4>([&](auto t) AI { bld4_sc1<0>(zr[t], l8 * 8192 + c8 * 16, rws, W_ZT + (ocol + 8 * t) * 8192 + (64 * rt + 32 * mh) * 4); });
      asm volatile("s_waitcnt vmcnt(0)" : "+v"(zr[0]), "+v"(zr[1]), "+v"(zr[2]), "+v"(zr[3])::"memory");
      u32 bad = 0;
      sfor<4>([&](auto t) AI { bad |= (zr[t].y ^ tag) | (zr[t].w ^ tag); });
      if (__all(bad == 0u)) break;
      __builtin_amdgcn_s_sleep(ZSLEEP);
    }
    STAMP(10);
    sfor<4>([&](auto t) AI { ldsw<u32x2>(ZST + j * 2048 + t * 512 + lane * 8, u32x2{zr[t].x, zr[t].z}); });
    waitl();
    float tt[16];
    sfor<4>([&](auto g) AI {
      const u32x2 z4 = ldtr(ZST + j * 2048 + (8 * g + 4 * hh + ((lane & 15) >> 2)) * 64 + 32 * ((lane >> 4) & 1) + 8 * (lane & 3));
      sfor<2>([&](auto e) AI {
        const u32 x2 = xr[g][(int)e], z2 = z4[(int)e];
        u32 n2 = pk2s(bflo(xn[2 * g + e]) * bflo(z2), bfhi(xn[2 * g + e]) * bfhi(z2));
        n2 = pk2s(bflo(x2) + bflo(n2), bfhi(x2) + bfhi(n2));
        tt[4 * g + 2 * e] = bflo(n2); tt[4 * g + 2 * e + 1] = bfhi(n2);
      });
    });
    sfor<16>([&](auto i) AI { pin(tt[i]); });
    STAMP(11);
    wait_min4<0>(FREE, 20);
    STAMP(12);
    u32x2 ob[4];
    sfor<4>([&](auto g) AI {
      const f32x4 v = ldsr<f32x4>(PART + mh * 8192 + db * 4096 + g * 1024 + lane * 16) + ldsr<f32x4>(PART + (2 + mh) * 8192 + db * 4096 + g * 1024 + lane * 16);
      ob[g].x = pk2(tt[4 * g] + rs * v.x, tt[4 * g + 1] + rs * v.y);
      ob[g].y = pk2(tt[4 * g + 2] + rs * v.z, tt[4 * g + 3] + rs * v.w);
    });
    sfor<2>([&](auto gp) AI {
      auto s0 = __builtin_amdgcn_permlane32_swap(ob[2 * gp].x, ob[2 * gp + 1].x, false, false);
      auto s1 = __builtin_amdgcn_permlane32_swap(ob[2 * gp].y, ob[2 * gp + 1].y, false, false);
      const u32x4 v = {(u32)s0[0], (u32)s1[0], (u32)s0[1], (u32)s1[1]};
      *(GAS u32x4*)((GAS char*)mkp(kb[2], kb[3]) + orow * 1024 + ocol * 2 + 32 * gp + 16 * hh) = v;
    });
    STAMP(13);
    DONE();
  } else if (wave < 4) {
    u32 junk, junk2;
    asm volatile("s_load_dword %0, %2, %3\n\ts_load_dword %1, %2, %4" : "=&s"(junk), "=&s"(junk2)
                 : "s"(pc0 & ~127ull), "s"((u32)(li + 32 * j) * 128u), "s"(min((u32)(li + 32 * (4 + j)) * 128u, CODE_BYTES)));
    const rsrc_t rws = mk_rsrc(ws, W_END);
    const int wm = wave >> 1, wn = wave & 1;
    const int l0 = lane_id();
    u64 epoch;
    asm volatile("s_load_dwordx2 %0, %3, 0x0\n\ts_waitcnt lgkmcnt(0)" : "=s"(epoch), "+s"(junk), "+s"(junk2) : "s"(ws));
    epoch = next_epoch(epoch);
    u32 wa[2];
    sfor<2>([&](auto i) AI { wa[i] = G_OFF + ((l0 >> 4) & 1) * (12288 + UPAD) + (8 * (l0 >> 5) + ((l0 & 15) >> 2)) * 192 + 96 * wn + 8 * (l0 & 3) + ((int)i ^ wm) * 6144; });
    f32x16 acc[2][3];
    sfor<2>([&](auto m) AI { sfor<3>([&](auto n) AI { sfor<16>([&](auto r) AI { acc[m][n][(int)r] = 0.f; }); pin(acc[m][n]); }); });
    u32 xa[4];
    sfor<4>([&](auto ks) AI { xa[ks] = (64 * wm + (l0 & 31)) * 128 + (((2 * (ks ^ (2 * wm)) + (l0 >> 5)) ^ ((l0 >> 1) & 7)) << 4); pin(xa[ks]); });
    STAMP(10);
    if (has) {
      bf16x8 xf[2][2], wf[2][3];
      f32x4 cs[3];
      sfor<3>([&](auto n) AI { cs[n] = f32x4{0.f, 0.f, 0.f, 0.f}; });
      const u32 o2_ = (l0 & 15) == ((l0 >> 4) & 1) ? 0x3f803f80u : 0u;
      const bf16x8 sel = __builtin_bit_cast(bf16x8, u32x4{o2_, o2_, o2_, o2_});
      auto ldw = [&](auto st, auto n) AI { const u32 a = wa[(st >> 1) & 1] + (st & 1) * 3072 + n * 32; wf[st & 1][n] = cat(ldtr(a), ldtr(a + 768)); };
      auto ldx = [&](auto st, auto m) AI { xf[st & 1][m] = ldsr<bf16x8>(xa[st & 3] + m * 4096); };
      wait_min4<1>(FULL, 1);
      __builtin_amdgcn_s_setprio(PRA);
      u32 accw = 0, cntw = 0;
      STAMP(1);
      ldw(IC<0>{}, IC<0>{}); ldx(IC<0>{}, IC<0>{}); ldx(IC<0>{}, IC<1>{}); ldw(IC<0>{}, IC<1>{}); ldw(IC<0>{}, IC<2>{});
      ldw(IC<1>{}, IC<0>{}); ldw(IC<1>{}, IC<1>{}); ldx(IC<1>{}, IC<0>{});
      GAP();
      u32 off = 0;
#pragma unroll 1
      for (int c = 0; c < 8; ++c) {
        u32 adv;
        sfor<24>([&](auto gg) AI {
          constexpr int st = gg / 6, g = gg % 6, n = g >> 1, m = g & 1;
          acc[m][n] = __builtin_amdgcn_mfma_f32_32x32x16_bf16(wf[st & 1][n], xf[st & 1][m], acc[m][n], 0, 0, 0);
          if constexpr (st < 2 && m == 0) { GAP(); cs[n] = __builtin_amdgcn_mfma_f32_16x16x32_bf16(wf[st & 1][n], sel, cs[n], 0, 0, 0); }
          if constexpr (st == 2 && g == 4) {
            bar();
            ldw(IC<4>{}, IC<0>{});
          }
          if constexpr (g == 0) { ldw(IC<st + 1>{}, IC<2>{}); ldx(IC<st + 1>{}, IC<1>{}); }
          if constexpr (g == 2 && st != 2) ldw(IC<st + 2>{}, IC<0>{});
          if constexpr (g == 4 && st != 2) ldw(IC<st + 2>{}, IC<1>{});
          if constexpr (g == 5) { ldx(IC<st + 2>{}, IC<0>{}); if constexpr (st == 2) ldw(IC<4>{}, IC<1>{}); }
          if constexpr (st == 0 && g == 3) { const u32 o2 = off == 2 * SLOT ? 0u : off + SLOT; adv = o2 - off; off = o2; spin(adv); spin(off); }
          if constexpr (st == 1 && g == 3) { xa[0] += adv; xa[1] += adv; xa[2] += adv; wa[0] += adv; pin(xa[0]); pin(xa[1]); pin(xa[2]); pin(wa[0]); }
          if constexpr (st == 2 && g == 1) { xa[3] += adv; wa[1] += adv; pin(xa[3]); pin(wa[1]); }
          if constexpr (st == 2 && g == 2) {
            if (__builtin_expect(c == 7, 0)) {
              const int l = lane_id();
              if ((l & 15) < 2) sfor<3>([&](auto n) AI { ldsw<f32x4>(CS + (wm * 192 + wn * 96 + n * 32 + (l & 15) * 16 + 4 * (l >> 4)) * 4, cs[n]); });
            }
          }
          GAP();
        });
      }
      STAMP(2);
      __builtin_amdgcn_s_setprio(PRE);
      const int lane = lane_id(), s31 = lane & 31, hh = lane >> 5;
      f32x2 st[2];
      f32x4 ca[4], cb2[4];
      auto ldc = [&](auto n) AI { sfor<4>([&](auto g) AI { const u32 a = CS + (wn * 96 + n * 32 + 8 * g + 4 * hh) * 4; ca[g] = ldsr<f32x4>(a); cb2[g] = ldsr<f32x4>(a + 768); }); };
      {
        sfor<2>([&](auto m) AI { st[m] = ldsr<f32x2>(ST + (64 * wm + 32 * m + s31) * 8); });
        ldc(IC<0>{});
      }
      STAMP(11);
      sfor<3>([&](auto n) AI {
        const int ks = 6 * cg + 3 * wn + n;
        if (ks < 86) {
          f32x4 c4[4];
          sfor<4>([&](auto g) AI { c4[g] = ca[g] + cb2[g]; });
          GAP();
          f32x2 g[8], u[8], e[8];
          sfor<8>([&](auto i) AI {
            constexpr int m = i >> 2, r = 2 * (i & 3);
            const f32x2 cg2 = {c4[r >> 2][r & 3], c4[r >> 2][(r & 3) + 1]};
            g[i] = f32x2{acc[m][n][r], acc[m][n][r + 1]} - st[m].x * cg2;
          });
          GAP();
          sfor<8>([&](auto i) AI { g[i] = g[i] * st[i >> 2].y; });
          GAP();
          sfor<8>([&](auto i) AI { e[i] = g[i] * g[i]; });
          GAP();
          sfor<8>([&](auto i) AI { e[i] = e[i] * -0.1029432397f - 2.3022081985f; });
          GAP();
          sfor<8>([&](auto i) AI { e[i] = e[i] * g[i]; });
          GAP();
          sfor<8>([&](auto i) AI {
            constexpr int m = i >> 2, r = 2 * (i & 3);
            const f32x2 cu2 = {c4[2 + (r >> 2)][r & 3], c4[2 + (r >> 2)][(r & 3) + 1]};
            e[i] = f32x2{__builtin_amdgcn_exp2f(e[i].x), __builtin_amdgcn_exp2f(e[i].y)};
            u[i] = f32x2{acc[m][n][r + 8], acc[m][n][r + 9]} - st[m].x * cu2;
            GAP();
          });
          if constexpr (n < 2) ldc(IC<n + 1>{});
          sfor<8>([&](auto i) AI { e[i] = e[i] + 1.0f; });
          GAP();
          sfor<8>([&](auto i) AI {
            e[i] = f32x2{__builtin_amdgcn_rcpf(e[i].x), __builtin_amdgcn_rcpf(e[i].y)};
            g[i] = g[i] * u[i];
            GAP();
          });
          sfor<8>([&](auto i) AI { g[i] = g[i] * e[i]; });
          GAP();
          sfor<2>([&](auto m) AI {
            const u32x4 o = {pk2(g[4 * m].x, g[4 * m].y), pk2(g[4 * m + 1].x, g[4 * m + 1].y), pk2(g[4 * m + 2].x, g[4 * m + 2].y), pk2(g[4 * m + 3].x, g[4 * m + 3].y)};
            __builtin_amdgcn_raw_buffer_store_b128(o, rws, lane * 16 + W_H + ((4 * rb + 2 * wm + m) * 86 + ks) * 1024, 0, 16);
          });
          GAP();
          if constexpr (n < 2) STAMP(20 + n);
        }
      });
      lds_set(FREE + wave, 17);
      STAMP(3);
      waitv<0>();
      __builtin_amdgcn_s_setprio(PRW);
      if (lds_arrive(CNT + 0, lane) == 4 && lane == 0) __hip_atomic_store((GAS u64*)(ws + W_HF + (rb * 16 + cg) * 64), epoch, RLX_AGENT);
      STAMP(4);
    } else {
#pragma unroll 1
      for (int c = 0; c < 8; ++c) barx();
      lds_set(FREE + wave, 17);
      STAMP(4);
    }
    const int lane = lane_id(), hh = lane >> 5, g16 = lane >> 4, q4 = (lane & 15) >> 2, p4 = lane & 3;
    const int kh = wave >> 1, mh = wave & 1;
    u32x4 hf[43];
    const u32 hso = W_H + ((2 * rt + mh) * 86 + 43 * kh) * 1024, hvo = lane * 16;
    f32x16 o2[2];
    sfor<2>([&](auto n) AI { sfor<16>([&](auto r) AI { o2[n][(int)r] = 0.f; }); pin(o2[n]); });
    u32 db_[2][3];
    sfor<2>([&](auto n) AI {
      const u32 da = kh * 8192 + (4 * hh + q4) * 128 + ((4 * ((int)n ^ (q4 >> 1)) + 2 * (g16 & 1) + (p4 >> 1)) << 4) + 8 * (p4 & 1);
      sfor<3>([&](auto w) AI { db_[n][w] = da + w * 61440; pin(db_[n][w]); });
    });
    bf16x8 df[3][2];
    u32 fl;
    auto ldd = [&](auto k) AI {
      constexpr int c = k >> 2, ks = k & 3;
      constexpr u32 wb = (c < 7 ? 16384u * c : c == 7 ? CONV : c == 8 ? CONV + CH_SZ : 16384u * (c - 9)) + ks * 2048;
      constexpr int w = wb / 61440 > 2 ? 2 : wb / 61440;
      sfor<2>([&](auto n) AI { const u32 a = db_[n][w] + (wb - w * 61440); df[k % 3][n] = cat(ldtr(a), ldtr(a + 1024)); });
    };
    if (wave == 0) { poll_flags<1>(ws, W_HF + (rt >> 1) * 16 * 64, 15, epoch, lane); lds_set(CVP + 6, 1); }
    else wait_byte<WSL>(CVP + 6, 1);
    __builtin_amdgcn_s_setprio(2);
    STAMP(5);
    auto hload = [&](auto k) AI { bld4_sc1<(k & 3) * 1024>(hf[k], hvo, rws, hso + (k >> 2) * 4096); };
    sfor<PD>([&](auto k) AI { hload(k); });
    wait_min4<0>(WDRDY, 1);
    STAMP(6);
    ldd(IC<0>{}); ldd(IC<1>{});
    GAP();
    sfor<43>([&](auto k) AI {
      if constexpr (k == 10) lds_set(FREE + wave, 18);
      if constexpr (k == 18) lds_set(FREE + wave, 19);
      if constexpr (k == 20 || k == 32) fl = lds_ld1(WDRDY);
      if constexpr (k == 40) fl = lds_ld1(FREE);
      if constexpr (k == 22 || k == 34) { STAMP(k == 22 ? 12 : 14); if (__builtin_expect(!all_ge(rfl(fl), k == 22 ? 2u : 3u), 0)) wait_min4<0>(WDRDY, k == 22 ? 2 : 3); STAMP(k == 22 ? 13 : 15); GAP(); }
      constexpr int younger = (k + PD <= 43 ? PD : 43 - k) - 1;
      waitv_pin<younger>(hf[k]);
      if constexpr (k == 0) STAMP(24);
      if constexpr (k == 8) STAMP(25);
      if constexpr (k == 16) STAMP(26);
      o2[0] = __builtin_amdgcn_mfma_f32_32x32x16_bf16(df[k % 3][0], __builtin_bit_cast(bf16x8, hf[k]), o2[0], 0, 0, 0);
      if constexpr (k + 2 < 43) ldd(IC<k + 2>{});
      GAP();
      o2[1] = __builtin_amdgcn_mfma_f32_32x32x16_bf16(df[k % 3][1], __builtin_bit_cast(bf16x8, hf[k]), o2[1], 0, 0, 0);
      if constexpr (k + PD < 43) hload(IC<k + PD>{});
      GAP();
    });
    STAMP(7);
    if (!all_ge(rfl(fl), 19u)) wait_min4<0>(FREE, 19);
    sfor<2>([&](auto n) AI { sfor<4>([&](auto g) AI {
      f32x4 v = {o2[n][4 * g], o2[n][4 * g + 1], o2[n][4 * g + 2], o2[n][4 * g + 3]};
      ldsw<f32x4>(PART + wave * 8192 + n * 4096 + g * 1024 + lane * 16, v);
    }); });
    waitl();
    lds_set(FREE + wave, 20);
    STAMP(8);
    DONE();
  } else {
    __builtin_amdgcn_s_setprio(PRL);
    const rsrc_t rg = mk_rsrc(mkp(ka[2], ka[3]), D * F * 2), ru = mk_rsrc(mkp(ka[4], ka[5]), D * F * 2);
    const int crot = 2 * ((rb + cg) & 1);
    auto chx = [&](int c, u32 sb) AI { dma4(sb + 4 * j * 1024, xv[0], xv[1], xv[2], xv[3], rq, (rb * 128 + 32 * j) * 1024 + (c ^ crot) * 128); };
    const rsrc_t rw = j & 2 ? ru : rg;
    auto chw = [&](int c, u32 sb, int h) AI {
      dma3(sb + (j & 2 ? U_OFF : G_OFF) + (6 * h + 3 * (j & 1)) * 1024, wv[0], wv[1], wv[2], rw, ((c ^ crot) * 64 + 32 * h) * F * 2);
    };
    auto chunk = [&](int c, u32 sb) AI { if (has) { chx(c, sb); chw(c, sb, 0); chw(c, sb, 1); } };
    chunk(0, 0); chunk(1, SLOT);
    waitv<10>(); lds_set(FULL + j, 1);
    STAMP(1);
    chunk(2, 2 * SLOT);
    {
      u32 sb = 0;
#pragma unroll 1
      for (int c = 3; c < 8; ++c) {
        waitv<10>(); barx();
        chunk(c, sb);
        sb = sb == 2 * SLOT ? 0 : sb + SLOT;
      }
    }
    waitv<10>(); barx();
    STAMP(2);
    const rsrc_t rd = mk_rsrc(mkp(kb[0], kb[1]), F * D * 2);
    u32 dv[4];
    sfor<4>([&](auto t) AI { dv[t] = (7 * t + l8) * 1024 + ((c8 ^ (((l8 >> 1) & 1) << 2)) << 4) + ct * 128; });
    auto wso = [&](int g) AI { return (u32)(((g >> 1) & 1) * 688 + (g >> 2) * 64 + (g & 1) * 32) * 1024u; };
    auto wd_groups = [&](int lo, int hi) AI {
#pragma unroll 1
      for (int g = lo + j; g < hi; g += 4) { dma4(g * 4096, dv[0], dv[1], dv[2], dv[3], rd, wso(g)); waitv<WDFL>(); }
    };
    u32x4 wr[5][4];
    auto wst = [&](auto t, u32 a) AI { sfor<4>([&](auto i) AI { pin(wr[t][i]); ldsw<u32x4>(a + j * 4096 + i * 1024 + lane * 16, wr[t][i]); }); };
    auto wrl = [&]() AI { sfor<5>([&](auto t) AI { const u32 so = wso(24 + 4 * t + j); sfor<4>([&](auto i) AI { bld4o<i * 1024>(wr[t][i], dv[i], rd, so); }); waitv<WRFL>(); }); };
    auto wrl1 = [&](auto t) AI { const u32 so = wso(24 + 4 * t + j); sfor<4>([&](auto i) AI { bld4o<i * 1024>(wr[t][i], dv[i], rd, so); }); };
    wrl1(IC<0>{});
    waitv<4>(); barx();
    STAMP(6);
    wrl1(IC<1>{}); wrl1(IC<2>{});
    barx();
    wd_groups(0, WDA);
    wrl1(IC<3>{}); wrl1(IC<4>{});
    wd_groups(WDA, 24);
    waitv<0>();
    lds_set(WDRDY + j, 1);
    STAMP(3);
    wait_min4<4>(CVP, 5);
    wst(IC<0>{}, XCH); wst(IC<1>{}, CONV); wst(IC<2>{}, CONV + CH_SZ);
    waitl();
    lds_set(WDRDY + j, 2);
    STAMP(4);
    wait_min4<2>(FREE, 18);
    wst(IC<3>{}, 0); wst(IC<4>{}, 16384);
    waitl();
    lds_set(WDRDY + j, 3);
    STAMP(5);
    DONE();
  }
}

extern "C" void kernel_launch(void* const* d_in, const int* in_sizes, int n_in, void* d_out, int out_size, void* d_ws, size_t ws_size,
                              hipStream_t stream) {
  Args a;
  a.q = (const u16*)d_in[0]; a.k = (const u16*)d_in[1]; a.v = (const u16*)d_in[2]; a.sk = (const u16*)d_in[3];
  a.h = (const u16*)d_in[4]; a.dv = (const u16*)d_in[5];
  a.wg = (const u16*)d_in[7]; a.wu = (const u16*)d_in[8]; a.wd = (const u16*)d_in[9];
  a.out = (u16*)d_out; a.ws = (char*)d_ws; a.pad = 0;
  (void)hipFuncSetAttribute((const void*)fused, hipFuncAttributeMaxDynamicSharedMemorySize, LDS_BYTES);
  fused<<<256, 768, LDS_BYTES, stream>>>(a);
}
```
